```python
import torch

def _softmax(x, axis=-1):
    return torch.softmax(x, dim=axis)
def _gelu(x):
    return torch.nn.functional.gelu(x, approximate="tanh")
def _silu(x):
    return torch.nn.functional.silu(x)
def _sigmoid(x):
    return torch.sigmoid(x)
def _layernorm(x, axis=-1, eps=1e-5):
    m = x.mean(dim=axis, keepdim=True)
    v = x.var(dim=axis, keepdim=True, unbiased=False)
    return (x - m) / torch.sqrt(v + eps)
def _scatter_add(tgt, idx, src, axis=0):
    return tgt.clone().index_add_(axis, idx, src)
def _segment_sum(src, seg, n):
    out = torch.zeros((n,) + src.shape[1:], dtype=src.dtype)
    return out.index_add_(0, seg, src)
def _pad(x, pw):
    flat = tuple(v for pair in reversed(pw) for v in pair)
    return torch.nn.functional.pad(x, flat)

kernel_name = 'gated_mix__mha_block_cached__glu_gated_swiglu__decode'

def setup_inputs():

    _w = (lambda a: (lambda t: t.to(torch.bfloat16) if t.is_floating_point() else t)(torch.as_tensor(a)))
    def _A_setup():
        B, Sq, D = 4, 1, 640
        H, Sk, Dh = 32, 8192, 192
        return {
            "x": _w((lambda *s: torch.randn(*s, dtype=torch.bfloat16) * 0.05)(B, Sq, D)),
            "k_cache": _w((lambda *s: torch.randn(*s, dtype=torch.bfloat16) * 6.4)(B, H, Sk, Dh)),
            "v_cache": _w((lambda *s: torch.randn(*s, dtype=torch.bfloat16) * 0.05)(B, H, Sk, Dh)),
            "wq": _w((lambda *s: torch.randn(*s, dtype=torch.bfloat16) * 6.4)(D, H * Dh) * (D ** -0.5)),
            "wo": _w((lambda *s: torch.randn(*s, dtype=torch.bfloat16) * 0.05)(H * Dh, D) * ((H * Dh) ** -0.5)),
        }
    _A_in = _A_setup()

    def _B_setup():
        B, S, D, F = 4, 1, 640, 1536
        return {
            "x": _w((lambda *s: torch.randn(*s, dtype=torch.bfloat16) * 0.05)(B, S, D)),
            "w1": _w((lambda *s: torch.randn(*s, dtype=torch.bfloat16) * 0.4)(D, F) * (2.0 / D) ** 0.5),
            "w3": _w((lambda *s: torch.randn(*s, dtype=torch.bfloat16) * 0.4)(D, F) * (2.0 / D) ** 0.5),
            "w2": _w((lambda *s: torch.randn(*s, dtype=torch.bfloat16) * 0.4)(F, D) * (2.0 / F) ** 0.5),
        }
    _B_in = _B_setup()

    return {
        "A_x": _A_in["x"],
        "A_k_cache": _A_in["k_cache"],
        "A_v_cache": _A_in["v_cache"],
        "A_wq": _A_in["wq"],
        "A_wo": _A_in["wo"],
        "B_x": _B_in["x"],
        "B_w1": _B_in["w1"],
        "B_w3": _B_in["w3"],
        "B_w2": _B_in["w2"],
        "gate_w": _w((lambda *s: torch.randn(*s, dtype=torch.bfloat16) * 0.05)(640) * 16.0),
    }

def reference(A_x, A_k_cache, A_v_cache, A_wq, A_wo, B_x, B_w1, B_w3, B_w2, gate_w):
    def _A_ref(x, k_cache, v_cache, wq, wo):
        B, Sq, D = x.shape
        H, Dh = 32, 192
        q = (lambda x, *d: x.permute(*d))((x @ wq).reshape(B, Sq, H, Dh), 0, 2, 1, 3)
        scores = torch.einsum("bhqd,bhkd->bhqk", q, k_cache) * (Dh ** -0.5)
        p = _softmax(scores, axis=-1)
        o = torch.einsum("bhqk,bhkd->bhqd", p, v_cache)
        return (lambda x, *d: x.permute(*d))(o, 0, 2, 1, 3).reshape(B, Sq, H * Dh) @ wo

    def _B_ref(x, w1, w3, w2):
        gate = torch.einsum("bsd,df->bsf", x, w1)
        up = torch.einsum("bsd,df->bsf", x, w3)
        h = _silu(gate) * up
        out = torch.einsum("bsf,fd->bsd", h, w2)
        return out

    x = A_x
    g = _sigmoid((x * gate_w).sum(-1, keepdims=True))
    a = _A_ref(x, A_k_cache, A_v_cache, A_wq, A_wo)
    b = _B_ref(x, B_w1, B_w3, B_w2)
    return g * a + (1 - g) * b
```

```cpp
#include <hip/hip_runtime.h>
#include <hip/hip_bf16.h>
#include <stdint.h>

#define GAS __attribute__((address_space(1)))
#define LAS __attribute__((address_space(3)))
typedef unsigned v4u __attribute__((ext_vector_type(4)));
typedef unsigned v2u __attribute__((ext_vector_type(2)));
typedef float v4f __attribute__((ext_vector_type(4)));
typedef float v2f __attribute__((ext_vector_type(2)));
typedef __bf16 bf16x2 __attribute__((ext_vector_type(2)));
typedef unsigned long long u64;
typedef unsigned short u16;

namespace {
constexpr int NB = 4, NH = 32, SK = 8192, DH = 192, DM = 640, FF = 1536;
constexpr int NWG = 256, NWAVE = 8, NTHR = NWAVE * 64;
constexpr int KEYS_WG = SK / 2;
constexpr int ROWB = DH * 2;
constexpr int TILEB = 8 * ROWB;
constexpr int NT = 4;
constexpr int ITERB = NT * NWAVE * TILEB;
constexpr int NIT = KEYS_WG * ROWB / ITERB;
constexpr int RSRC3 = 0x00020000;
constexpr int AUX_NT = 2, AUX_SC1 = 16;
constexpr float CS = 0.07216878364870322f * 1.4426950408889634f;

constexpr int WS_EPOCH = 0;
constexpr int WS_ML = 4096;
constexpr int WS_INBOX = 65536;
constexpr int WS_BYTES = WS_INBOX + NB * 64 * 64 * 80;

constexpr int L_XS = 0;
constexpr int L_QP = 1280;
constexpr int L_QB = L_QP + 8 * 192 * 4;
constexpr int L_OB = L_QB + 384;
constexpr int L_MISC = L_OB + 384;
constexpr int L_SC = L_MISC + 256;
constexpr int L_FP = L_SC + 8 * 512 * 4;
constexpr int L_BYTES = L_FP + 8 * 60 * 16 * 4;

constexpr unsigned SPIN_CAP = 1u << 22;

__device__ __forceinline__ float dot2(unsigned a, unsigned b, float c) {
  return __builtin_amdgcn_fdot2_f32_bf16(__builtin_bit_cast(bf16x2, a), __builtin_bit_cast(bf16x2, b), c, false);
}
template <int CTRL> __device__ __forceinline__ float dpp(float x) {
  return __builtin_bit_cast(float, __builtin_amdgcn_mov_dpp(__builtin_bit_cast(int, x), CTRL, 0xf, 0xf, true));
}
constexpr int XOR1 = 0xB1, XOR2 = 0x4E, XOR7 = 0x141, XOR8 = 0x128;
__device__ __forceinline__ float sum16_32(float x) {
  auto s = __builtin_amdgcn_permlane16_swap(__float_as_uint(x), __float_as_uint(x), false, false);
  x = __uint_as_float(s[0]) + __uint_as_float(s[1]);
  auto t = __builtin_amdgcn_permlane32_swap(__float_as_uint(x), __float_as_uint(x), false, false);
  return __uint_as_float(t[0]) + __uint_as_float(t[1]);
}
__device__ __forceinline__ float max16_32(float x) {
  auto s = __builtin_amdgcn_permlane16_swap(__float_as_uint(x), __float_as_uint(x), false, false);
  x = fmaxf(__uint_as_float(s[0]), __uint_as_float(s[1]));
  auto t = __builtin_amdgcn_permlane32_swap(__float_as_uint(x), __float_as_uint(x), false, false);
  return fmaxf(__uint_as_float(t[0]), __uint_as_float(t[1]));
}
__device__ __forceinline__ float oct_sum(float x) { x += dpp<XOR1>(x); x += dpp<XOR2>(x); x += dpp<XOR7>(x); return x; }
__device__ __forceinline__ float xoct_sum(float x) { x += dpp<XOR8>(x); return sum16_32(x); }
__device__ __forceinline__ float wave_sum(float x) { return xoct_sum(oct_sum(x)); }
__device__ __forceinline__ float wave_max(float x) {
  x = fmaxf(x, dpp<XOR1>(x)); x = fmaxf(x, dpp<XOR2>(x)); x = fmaxf(x, dpp<XOR7>(x)); x = fmaxf(x, dpp<XOR8>(x)); return max16_32(x);
}
__device__ __forceinline__ unsigned cvtpk(float lo, float hi) { unsigned r; asm("v_cvt_pk_bf16_f32 %0, %1, %2" : "=v"(r) : "v"(lo), "v"(hi)); return r; }
__device__ __forceinline__ float bflo(unsigned u) { return __uint_as_float(u << 16); }
__device__ __forceinline__ float bfhi(unsigned u) { return __uint_as_float(u & 0xffff0000u); }
__device__ __forceinline__ float ex2(float x) { return __builtin_amdgcn_exp2f(x); }
__device__ __forceinline__ __amdgpu_buffer_rsrc_t rsrc(const void* p, int bytes) { return __builtin_amdgcn_make_buffer_rsrc((void*)p, (short)0, bytes, RSRC3); }
template <int AUX> __device__ __forceinline__ v4u ld16(__amdgpu_buffer_rsrc_t r, int voff, int soff) { return __builtin_amdgcn_raw_buffer_load_b128(r, voff, soff, AUX); }
#define SCHED_FENCE() __builtin_amdgcn_sched_barrier(0)
#define LDS_FENCE() asm volatile("s_waitcnt lgkmcnt(0)" ::: "memory")

__global__ void __launch_bounds__(NTHR, 2)
fused_decode(const u16* __restrict__ x, const u16* __restrict__ Kc, const u16* __restrict__ Vc, const u16* __restrict__ wq,
             const u16* __restrict__ wo, const u16* __restrict__ w1, const u16* __restrict__ w3, const u16* __restrict__ w2,
             const u16* __restrict__ gw, u16* __restrict__ out, unsigned char* __restrict__ ws) {
  __shared__ __attribute__((aligned(16))) unsigned char lds_raw[L_BYTES];
  LAS unsigned char* L = (LAS unsigned char*)lds_raw;
  const int tid = threadIdx.x, lane = tid & 63, w = __builtin_amdgcn_readfirstlane(tid >> 6);
  const int k = lane >> 3, c = lane & 7;
  const int blk = blockIdx.x, xg = blk & 7, idx = blk >> 3;
  const int h = xg * 4 + (idx & 3), b = idx >> 3, s = (idx >> 2) & 1;
  const int j = h * 2 + s;

  const __amdgpu_buffer_rsrc_t rws = rsrc(ws, WS_BYTES);
  const u64 epoch = __hip_atomic_load((GAS u64*)(ws + WS_EPOCH + b * 256), __ATOMIC_RELAXED, __HIP_MEMORY_SCOPE_AGENT);
  const __amdgpu_buffer_rsrc_t rx = rsrc(x + b * DM, DM * 2);
  v4u xv = ld16<0>(rx, (w * 80 + lane * 8) * 2 + (lane < 10 ? 0 : 1 << 20), 0);
  v4u gx0, gx1, gg0, gg1;
  if (w == 0) {
    const __amdgpu_buffer_rsrc_t rg = rsrc(gw, DM * 2);
    gx0 = ld16<0>(rx, lane * 16, 0); gx1 = ld16<0>(rx, 1024 + lane * 16, 0);
    gg0 = ld16<0>(rg, lane * 16, 0); gg1 = ld16<0>(rg, 1024 + lane * 16, 0);
  }
  v4u W[32];
  {
    const __amdgpu_buffer_rsrc_t rq = rsrc(wq + h * DH, (DM * NH * DH - h * DH) * 2);
    const int vo = (w * 80 + k) * (NH * DH * 2) + c * 16;
#pragma unroll
    for (int t = 0; t < 10; ++t)
#pragma unroll
      for (int i = 0; i < 3; ++i) W[t * 3 + i] = ld16<0>(rq, vo + i * 128, t * 8 * (NH * DH * 2));
  }
  SCHED_FENCE();
  const size_t kvoff = ((size_t)(b * NH + h) * SK + (size_t)s * KEYS_WG) * DH;
  const __amdgpu_buffer_rsrc_t rK = rsrc(Kc + kvoff, KEYS_WG * ROWB), rV = rsrc(Vc + kvoff, KEYS_WG * ROWB);
  const int kvo = w * TILEB + k * ROWB + c * 16;
  v4u R[NT][3];
#pragma unroll
  for (int t = 0; t < NT; ++t)
#pragma unroll
    for (int i = 0; i < 3; ++i) R[t][i] = ld16<AUX_NT>(rK, kvo + i * 128, t * NWAVE * TILEB);
  SCHED_FENCE();

  const unsigned tagA = (unsigned)epoch + 1u, tagB = (unsigned)(epoch >> 32) + 1u;

  if (lane < 10) *(LAS v4u*)(L + L_XS + (w * 80 + lane * 8) * 2) = xv;
  if (w == 0) {
    float d = 0.f;
#pragma unroll
    for (int e = 0; e < 4; ++e) { d = dot2(gx0[e], gg0[e], d); d = dot2(gx1[e], gg1[e], d); }
    d = wave_sum(d);
    const float g = 1.f / (1.f + __expf(-d));
    if (lane == 0) *(LAS float*)(L + L_MISC) = g;
  }
  LDS_FENCE();
  {
    float qa[24];
#pragma unroll
    for (int e = 0; e < 24; ++e) qa[e] = 0.f;
#pragma unroll
    for (int t = 0; t < 10; ++t) {
      const unsigned xl = *(LAS u16*)(L + L_XS + (w * 80 + t * 8 + k) * 2), xh = xl << 16;
#pragma unroll
      for (int i = 0; i < 3; ++i)
#pragma unroll
        for (int d = 0; d < 4; ++d) { qa[i * 8 + 2 * d] = dot2(W[t * 3 + i][d], xl, qa[i * 8 + 2 * d]); qa[i * 8 + 2 * d + 1] = dot2(W[t * 3 + i][d], xh, qa[i * 8 + 2 * d + 1]); }
    }
#pragma unroll
    for (int e = 0; e < 24; ++e) qa[e] = xoct_sum(qa[e]);
    if (k == 0) {
#pragma unroll
      for (int i = 0; i < 3; ++i) {
        LAS v4f* p = (LAS v4f*)(L + L_QP + (w * 192 + i * 64 + c * 8) * 4);
        p[0] = v4f{qa[i * 8], qa[i * 8 + 1], qa[i * 8 + 2], qa[i * 8 + 3]}; p[1] = v4f{qa[i * 8 + 4], qa[i * 8 + 5], qa[i * 8 + 6], qa[i * 8 + 7]};
      }
    }
  }
  const int fr = lane / 3, fc = lane - fr * 3;
  const int yr = lane / 10, yc = lane - yr * 10;
  {
    const __amdgpu_buffer_rsrc_t r1 = rsrc(w1, DM * FF * 2), r3 = rsrc(w3, DM * FF * 2), r2 = rsrc(w2, FF * DM * 2);
    const int vo = (w * 80 + fr) * (FF * 2) + j * 48 + fc * 16 + (lane < 60 ? 0 : 1 << 24);
#pragma unroll
    for (int t = 0; t < 4; ++t) { W[t] = ld16<0>(r1, vo, t * 20 * (FF * 2)); W[4 + t] = ld16<0>(r3, vo, t * 20 * (FF * 2)); }
    const int vo2 = (j * 24 + yr) * (DM * 2) + w * 160 + yc * 16 + (lane < 60 ? 0 : 1 << 24);
#pragma unroll
    for (int t = 0; t < 4; ++t) W[8 + t] = ld16<0>(r2, vo2, t * 6 * (DM * 2));
  }
  SCHED_FENCE();
  __syncthreads();
  if (tid < 96) {
    float a0 = 0.f, a1 = 0.f;
#pragma unroll
    for (int ww = 0; ww < 8; ++ww) { const v2f v = *(LAS v2f*)(L + L_QP + (ww * 192 + tid * 2) * 4); a0 += v[0]; a1 += v[1]; }
    *(LAS unsigned*)(L + L_QB + tid * 4) = cvtpk(a0, a1);
  }
  __syncthreads();
  v4u Q[3];
#pragma unroll
  for (int i = 0; i < 3; ++i) Q[i] = *(LAS v4u*)(L + L_QB + (c + 8 * i) * 16);
  const float gate = __uint_as_float(__builtin_amdgcn_readfirstlane(*(LAS unsigned*)(L + L_MISC)));

  float mx = -INFINITY;
  LAS float* sc = (LAS float*)(L + L_SC) + w * 512;
  auto k_iter = [&](int it, __amdgpu_buffer_rsrc_t rn, int soff) {
    float sv[NT];
#pragma unroll
    for (int t = 0; t < NT; ++t) {
      float a[3];
#pragma unroll
      for (int i = 0; i < 3; ++i) { a[i] = dot2(R[t][i][0], Q[i][0], 0.f); a[i] = dot2(R[t][i][1], Q[i][1], a[i]); a[i] = dot2(R[t][i][2], Q[i][2], a[i]); a[i] = dot2(R[t][i][3], Q[i][3], a[i]); }
      sv[t] = (a[0] + a[1]) + a[2];
      asm volatile("" : "+v"(sv[t]) : : "memory");
#pragma unroll
      for (int i = 0; i < 3; ++i) R[t][i] = ld16<AUX_NT>(rn, kvo + i * 128, soff + t * NWAVE * TILEB);
      SCHED_FENCE();
    }
#pragma unroll
    for (int t = 0; t < NT; ++t) sv[t] = oct_sum(sv[t]);
    mx = fmaxf(fmaxf(mx, fmaxf(sv[0], sv[1])), fmaxf(sv[2], sv[3]));
    if (c == 0) *(LAS v4f*)(sc + it * 32 + k * 4) = v4f{sv[0], sv[1], sv[2], sv[3]};
  };
  k_iter(0, rK, ITERB);

  {
    float ga[8], ua[8], ya[8];
#pragma unroll
    for (int e = 0; e < 8; ++e) { ga[e] = 0.f; ua[e] = 0.f; }
#pragma unroll
    for (int t = 0; t < 4; ++t) {
      const unsigned xl = *(LAS u16*)(L + L_XS + (w * 80 + t * 20 + (fr < 20 ? fr : 0)) * 2), xh = xl << 16;
#pragma unroll
      for (int d = 0; d < 4; ++d) {
        ga[2 * d] = dot2(W[t][d], xl, ga[2 * d]); ga[2 * d + 1] = dot2(W[t][d], xh, ga[2 * d + 1]);
        ua[2 * d] = dot2(W[4 + t][d], xl, ua[2 * d]); ua[2 * d + 1] = dot2(W[4 + t][d], xh, ua[2 * d + 1]);
      }
    }
    if (lane < 60) {
      LAS v4f* p = (LAS v4f*)(L + L_FP + (w * 60 + lane) * 64);
      p[0] = v4f{ga[0], ga[1], ga[2], ga[3]}; p[1] = v4f{ga[4], ga[5], ga[6], ga[7]};
      p[2] = v4f{ua[0], ua[1], ua[2], ua[3]}; p[3] = v4f{ua[4], ua[5], ua[6], ua[7]};
    }
    __syncthreads();
    if (tid < 384) {
      const int ww = tid & 7, m = (tid >> 3) & 1, u = tid >> 4;
      const LAS float* p = (const LAS float*)(L + L_FP) + (ww * 60 + (u >> 3)) * 16 + m * 8 + (u & 7);
      float a = 0.f;
#pragma unroll
      for (int r = 0; r < 20; ++r) a += p[r * 48];
      a = oct_sum(a);
      const float o = dpp<XOR8>(a);
      const float hv = a / (1.f + __expf(-a)) * o * (1.f - gate);
      if ((tid & 15) == 0) *(LAS float*)(L + L_MISC + 64 + u * 4) = hv;
    }
    __syncthreads();
#pragma unroll
    for (int e = 0; e < 8; ++e) ya[e] = 0.f;
#pragma unroll
    for (int t = 0; t < 4; ++t) {
      const float hv = *(LAS float*)(L + L_MISC + 64 + (t * 6 + (yr < 6 ? yr : 0)) * 4);
#pragma unroll
      for (int d = 0; d < 4; ++d) { ya[2 * d] = fmaf(hv, bflo(W[8 + t][d]), ya[2 * d]); ya[2 * d + 1] = fmaf(hv, bfhi(W[8 + t][d]), ya[2 * d + 1]); }
    }
    if (lane < 60) {
      LAS v4f* p = (LAS v4f*)(L + L_FP + (w * 480 + yr * 80 + yc * 8) * 4);
      p[0] = v4f{ya[0], ya[1], ya[2], ya[3]}; p[1] = v4f{ya[4], ya[5], ya[6], ya[7]};
    }
  }
  SCHED_FENCE();
  {
    const __amdgpu_buffer_rsrc_t ro = rsrc(wo + (size_t)h * DH * DM, DH * DM * 2);
    const int vo = yr * (DM * 2) + w * 160 + yc * 16 + (lane < 60 ? 0 : 1 << 24);
#pragma unroll
    for (int t = 0; t < 32; ++t) W[t] = ld16<0>(ro, vo, t * 6 * (DM * 2));
  }
  SCHED_FENCE();
  k_iter(1, rK, 2 * ITERB);
  for (int it = 2; it < NIT - 1; ++it) k_iter(it, rK, (it + 1) * ITERB);
  k_iter(NIT - 1, rV, 0);

  mx = fmaxf(mx, dpp<XOR8>(mx)); mx = max16_32(mx);
  const float mw = mx * CS;
  LDS_FENCE();
  {
    LAS v4f* p = (LAS v4f*)(sc + lane * 8);
    v4f a = p[0], bb = p[1];
    float l = 0.f;
#pragma unroll
    for (int e = 0; e < 4; ++e) { a[e] = ex2(fmaf(a[e], CS, -mw)); bb[e] = ex2(fmaf(bb[e], CS, -mw)); l += a[e] + bb[e]; }
    p[0] = a; p[1] = bb;
    l = wave_sum(l);
    if (lane == 0) __builtin_amdgcn_raw_buffer_store_b128(v4u{__float_as_uint(mw), tagA, __float_as_uint(l), tagB}, rws, WS_ML + (((b * NH + h) * 2 + s) * 8 + w) * 16, 0, AUX_SC1);
  }
#pragma unroll
  for (int u = 0; u < 16; ++u)
#pragma unroll
    for (int d = 0; d < 4; ++d) {
      const unsigned a = W[2 * u][d], bb = W[2 * u + 1][d];
      W[2 * u][d] = __builtin_amdgcn_perm(bb, a, 0x05040100u);
      W[2 * u + 1][d] = __builtin_amdgcn_perm(bb, a, 0x07060302u);
    }
  LDS_FENCE();

  v2f acc2[12];
#pragma unroll
  for (int e = 0; e < 12; ++e) acc2[e] = v2f{0.f, 0.f};
#define PIN_ACC() asm volatile("" : "+v"(acc2[0]), "+v"(acc2[1]), "+v"(acc2[2]), "+v"(acc2[3]), "+v"(acc2[4]), "+v"(acc2[5]), "+v"(acc2[6]), "+v"(acc2[7]), \
                                    "+v"(acc2[8]), "+v"(acc2[9]), "+v"(acc2[10]), "+v"(acc2[11]) : : "memory")
  auto v_iter = [&](int it, bool refill) {
    const v4f p4 = *(LAS v4f*)(sc + it * 32 + k * 4);
#pragma unroll
    for (int t = 0; t < NT; ++t) {
      const v2f pp = v2f{p4[t], p4[t]};
#pragma unroll
      for (int i = 0; i < 3; ++i)
#pragma unroll
        for (int d = 0; d < 4; ++d) acc2[i * 4 + d] = __builtin_elementwise_fma(pp, v2f{bflo(R[t][i][d]), bfhi(R[t][i][d])}, acc2[i * 4 + d]);
      PIN_ACC();
      if (refill) {
#pragma unroll
        for (int i = 0; i < 3; ++i) R[t][i] = ld16<AUX_NT>(rV, kvo + i * 128, (it + 1) * ITERB + t * NWAVE * TILEB);
      }
      SCHED_FENCE();
    }
  };
  for (int it = 0; it < NIT - 1; ++it) v_iter(it, true);
  const int mloff = WS_ML + ((b * NH + h) * 16 + (lane & 15)) * 16;
  v4u ml = ld16<AUX_SC1>(rws, mloff, 0);
  SCHED_FENCE();
  v_iter(NIT - 1, false);

  for (unsigned spins = 0; !__all(ml[1] == tagA && ml[3] == tagB); ) {
    if (++spins > SPIN_CAP) break;
    __builtin_amdgcn_s_sleep(2);
    asm volatile("" ::: "memory");
    ml = ld16<AUX_SC1>(rws, mloff, 0);
  }
  {
    const float m_i = __uint_as_float(ml[0]), l_i = __uint_as_float(ml[2]);
    float M = m_i;
    M = fmaxf(M, dpp<XOR1>(M)); M = fmaxf(M, dpp<XOR2>(M)); M = fmaxf(M, dpp<XOR7>(M)); M = fmaxf(M, dpp<XOR8>(M));
    float Ls = l_i * ex2(m_i - M);
    Ls = oct_sum(Ls); Ls += dpp<XOR8>(Ls);
    const float f = ex2(mw - M) / Ls * gate;
    float acc[24];
#pragma unroll
    for (int e = 0; e < 24; ++e) acc[e] = xoct_sum(acc2[e >> 1][e & 1]) * f;
    if (k == 0) {
#pragma unroll
      for (int i = 0; i < 3; ++i) {
        LAS v4f* p = (LAS v4f*)(L + L_QP + (w * 192 + i * 64 + c * 8) * 4);
        p[0] = v4f{acc[i * 8], acc[i * 8 + 1], acc[i * 8 + 2], acc[i * 8 + 3]}; p[1] = v4f{acc[i * 8 + 4], acc[i * 8 + 5], acc[i * 8 + 6], acc[i * 8 + 7]};
      }
    }
  }
  __syncthreads();
  int tid2 = tid; asm volatile("" : "+v"(tid2));
  if (tid2 < 96) {
    const int r = tid2 >> 4, u = tid2 & 15;
    float a0 = 0.f, a1 = 0.f;
#pragma unroll
    for (int ww = 0; ww < 8; ++ww) { const LAS float* p = (const LAS float*)(L + L_QP) + ww * 192 + u * 12 + r; a0 += p[0]; a1 += p[6]; }
    *(LAS unsigned*)(L + L_OB + tid2 * 4) = cvtpk(a0, a1);
  }
  __syncthreads();
  {
    int ln = lane; asm volatile("" : "+v"(ln));
    const int yr = ln < 60 ? ln / 10 : 0, yc = ln - (ln / 10) * 10;
    LAS float* yrp = (LAS float*)(L + L_FP) + w * 480;
    float ya[8];
    { const LAS v4f* p = (const LAS v4f*)(yrp + yr * 80 + yc * 8); const v4f a = p[0], bb = p[1];
#pragma unroll
      for (int e = 0; e < 4; ++e) { ya[e] = a[e]; ya[4 + e] = bb[e]; } }
    const LAS v4u* op = (const LAS v4u*)(L + L_OB + yr * 64);
#pragma unroll
    for (int u4 = 0; u4 < 4; ++u4) {
      const v4u o4 = op[u4];
#pragma unroll
      for (int e = 0; e < 4; ++e) {
        const int u = u4 * 4 + e;
#pragma unroll
        for (int d = 0; d < 4; ++d) { ya[2 * d] = dot2(W[2 * u][d], o4[e], ya[2 * d]); ya[2 * d + 1] = dot2(W[2 * u + 1][d], o4[e], ya[2 * d + 1]); }
      }
    }
    if (lane < 60) { LAS v4f* p = (LAS v4f*)(yrp + yr * 80 + yc * 8); p[0] = v4f{ya[0], ya[1], ya[2], ya[3]}; p[1] = v4f{ya[4], ya[5], ya[6], ya[7]}; }
    LDS_FENCE();
    if (lane < 40) {
      float y0 = 0.f, y1 = 0.f;
#pragma unroll
      for (int r = 0; r < 6; ++r) { const v2f v = *(LAS v2f*)(yrp + r * 80 + lane * 2); y0 += v[0]; y1 += v[1]; }
      const int cons = w * 8 + lane / 5, g5 = lane % 5;
      __builtin_amdgcn_raw_buffer_store_b128(v4u{__float_as_uint(y0), tagA, __float_as_uint(y1), tagB}, rws, WS_INBOX + (((b * 64 + cons) * 64 + j) * 5 + g5) * 16, 0, AUX_SC1);
    }
  }
  if (w == 0) {
    const int io = WS_INBOX + ((b * 64 + j) * 64 + lane) * 80;
    v4u g5[5];
    for (unsigned spins = 0;;) {
      bool ok = true;
      asm volatile("" ::: "memory");
#pragma unroll
      for (int e = 0; e < 5; ++e) { g5[e] = ld16<AUX_SC1>(rws, io + e * 16, 0); }
#pragma unroll
      for (int e = 0; e < 5; ++e) ok = ok && g5[e][1] == tagA && g5[e][3] == tagB;
      if (__all(ok) || ++spins > SPIN_CAP) break;
      __builtin_amdgcn_s_sleep(2);
    }
    float y[10];
#pragma unroll
    for (int e = 0; e < 5; ++e) { y[2 * e] = wave_sum(__uint_as_float(g5[e][0])); y[2 * e + 1] = wave_sum(__uint_as_float(g5[e][2])); }
    unsigned o = cvtpk(y[0], y[1]);
#pragma unroll
    for (int e = 1; e < 5; ++e) { const unsigned v = cvtpk(y[2 * e], y[2 * e + 1]); o = (lane == e) ? v : o; }
    if (lane < 5) *(GAS unsigned*)(out + b * DM + j * 10 + lane * 2) = o;
    if (j == 0 && lane == 0) __hip_atomic_store((GAS u64*)(ws + WS_EPOCH + b * 256), ((u64)tagB << 32) | tagA, __ATOMIC_RELAXED, __HIP_MEMORY_SCOPE_AGENT);
  }
}
}

extern "C" void kernel_launch(void* const* d_in, const int* in_sizes, int n_in, void* d_out, int out_size, void* d_ws, size_t ws_size,
                              hipStream_t stream) {
  (void)in_sizes; (void)n_in; (void)out_size; (void)ws_size;
  hipLaunchKernelGGL(fused_decode, dim3(NWG), dim3(NTHR), 0, stream, (const u16*)d_in[0], (const u16*)d_in[1], (const u16*)d_in[2], (const u16*)d_in[3],
                     (const u16*)d_in[4], (const u16*)d_in[6], (const u16*)d_in[7], (const u16*)d_in[8], (const u16*)d_in[9], (u16*)d_out, (unsigned char*)d_ws);
}
```

```cpp
#include <hip/hip_runtime.h>
#include <hip/hip_bf16.h>
#include <stdint.h>

#define GAS __attribute__((address_space(1)))
#define LAS __attribute__((address_space(3)))
typedef unsigned v4u __attribute__((ext_vector_type(4)));
typedef unsigned v2u __attribute__((ext_vector_type(2)));
typedef float v4f __attribute__((ext_vector_type(4)));
typedef float v2f __attribute__((ext_vector_type(2)));
typedef __bf16 bf16x2 __attribute__((ext_vector_type(2)));
typedef unsigned long long u64;
typedef unsigned short u16;

namespace {
constexpr int NB = 4, NH = 32, SK = 8192, DH = 192, DM = 640, FF = 1536;
constexpr int NWG = 256, NWAVE = 8, NTHR = NWAVE * 64;
constexpr int KEYS_WG = SK / 2;
constexpr int ROWB = DH * 2;
constexpr int TILEB = 8 * ROWB;
constexpr int NT = 4;
constexpr int GRPB = NT * TILEB;
constexpr int NG = KEYS_WG * ROWB / GRPB;
constexpr int RSRC3 = 0x00020000;
constexpr int AUX_NT = 2, AUX_SC1 = 16;
constexpr int OOB = 1 << 28;
constexpr float CS = 0.07216878364870322f * 1.4426950408889634f;

constexpr int WS_EPOCH = 0;
constexpr int WS_ML = 4096;
constexpr int WS_INBOX = 65536;
constexpr int WS_QX = WS_INBOX + NB * 64 * 64 * 80;
constexpr int WS_BYTES = WS_QX + NH * NB * 8 * 128 * 16;

constexpr int L_XS = 0;
constexpr int L_QP = 1280;
constexpr int L_QB = L_QP + 8 * 192 * 4;
constexpr int L_OB = L_QB + 384;
constexpr int L_MISC = L_OB + 384;
constexpr int L_YF = L_MISC + 512;
constexpr int L_SC = L_YF + 2560;
constexpr int L_FP = L_SC + NG * 32 * 4;
constexpr int L_BYTES = L_FP + 22 * 4 * 192 * 4;

constexpr unsigned SPIN_CAP = 1u << 17;

__device__ __forceinline__ float dot2(unsigned a, unsigned b, float c) {
  return __builtin_amdgcn_fdot2_f32_bf16(__builtin_bit_cast(bf16x2, a), __builtin_bit_cast(bf16x2, b), c, false);
}
template <int CTRL> __device__ __forceinline__ float dpp(float x) {
  return __builtin_bit_cast(float, __builtin_amdgcn_mov_dpp(__builtin_bit_cast(int, x), CTRL, 0xf, 0xf, true));
}
constexpr int XOR1 = 0xB1, XOR2 = 0x4E, XOR7 = 0x141, XOR8 = 0x128;
__device__ __forceinline__ float sum16_32(float x) {
  auto s = __builtin_amdgcn_permlane16_swap(__float_as_uint(x), __float_as_uint(x), false, false);
  x = __uint_as_float(s[0]) + __uint_as_float(s[1]);
  auto t = __builtin_amdgcn_permlane32_swap(__float_as_uint(x), __float_as_uint(x), false, false);
  return __uint_as_float(t[0]) + __uint_as_float(t[1]);
}
__device__ __forceinline__ float max16_32(float x) {
  auto s = __builtin_amdgcn_permlane16_swap(__float_as_uint(x), __float_as_uint(x), false, false);
  x = fmaxf(__uint_as_float(s[0]), __uint_as_float(s[1]));
  auto t = __builtin_amdgcn_permlane32_swap(__float_as_uint(x), __float_as_uint(x), false, false);
  return fmaxf(__uint_as_float(t[0]), __uint_as_float(t[1]));
}
__device__ __forceinline__ float oct_sum(float x) { x += dpp<XOR1>(x); x += dpp<XOR2>(x); x += dpp<XOR7>(x); return x; }
__device__ __forceinline__ float xoct_sum(float x) { x += dpp<XOR8>(x); return sum16_32(x); }
__device__ __forceinline__ float wave_sum(float x) { return xoct_sum(oct_sum(x)); }
__device__ __forceinline__ unsigned cvtpk(float lo, float hi) { unsigned r; asm("v_cvt_pk_bf16_f32 %0, %1, %2" : "=v"(r) : "v"(lo), "v"(hi)); return r; }
__device__ __forceinline__ float bflo(unsigned u) { return __uint_as_float(u << 16); }
__device__ __forceinline__ float bfhi(unsigned u) { return __uint_as_float(u & 0xffff0000u); }
__device__ __forceinline__ float ex2(float x) { return __builtin_amdgcn_exp2f(x); }
__device__ __forceinline__ __amdgpu_buffer_rsrc_t rsrc(const void* p, int bytes) { return __builtin_amdgcn_make_buffer_rsrc((void*)p, (short)0, bytes, RSRC3); }
template <int AUX> __device__ __forceinline__ v4u ld16(__amdgpu_buffer_rsrc_t r, int voff, int soff) { return __builtin_amdgcn_raw_buffer_load_b128(r, voff, soff, AUX); }
template <int AUX> __device__ __forceinline__ v2u ld8(__amdgpu_buffer_rsrc_t r, int voff, int soff) { return __builtin_amdgcn_raw_buffer_load_b64(r, voff, soff, AUX); }
#define STAMP(i) do { } while (0)
#define SCHED_FENCE() __builtin_amdgcn_sched_barrier(0)
#define LDS_FENCE() asm volatile("s_waitcnt lgkmcnt(0)" ::: "memory")
#define MEM_FENCE() asm volatile("" ::: "memory")
__device__ __forceinline__ int fresh_lane() { int l; asm volatile("v_mbcnt_lo_u32_b32 %0, -1, 0\n\tv_mbcnt_hi_u32_b32 %0, -1, %0" : "=v"(l)); return l; }

__global__ void __launch_bounds__(NTHR, 2)
fused_decode(const u16* __restrict__ x, const u16* __restrict__ Kc, const u16* __restrict__ Vc, const u16* __restrict__ wq,
             const u16* __restrict__ wo, const u16* __restrict__ w1, const u16* __restrict__ w3, const u16* __restrict__ w2,
             const u16* __restrict__ gw, u16* __restrict__ out, unsigned char* __restrict__ ws) {
  __shared__ __attribute__((aligned(16))) unsigned char lds_raw[L_BYTES];
  LAS unsigned char* L = (LAS unsigned char*)lds_raw;
  const int tid = threadIdx.x, lane = tid & 63, w = __builtin_amdgcn_readfirstlane(tid >> 6);
  const int k = lane >> 3, c = lane & 7;
  const int blk = blockIdx.x, xg = blk & 7, idx = blk >> 3;
  const int h = xg * 4 + (idx & 3), sh = idx >> 2, b = sh >> 1, s = sh & 1;
  const int j = h * 2 + s;
  STAMP(0);
  const __amdgpu_buffer_rsrc_t rws = rsrc(ws, WS_BYTES);
  const u64 epoch = __hip_atomic_load((GAS u64*)(ws + WS_EPOCH), __ATOMIC_RELAXED, __HIP_MEMORY_SCOPE_AGENT);
  const int qr = tid / 24, qc = tid - qr * 24;
  v2u xq[4]; v4u wqv[4];
  {
    const __amdgpu_buffer_rsrc_t rx = rsrc(x, NB * DM * 2);
    const __amdgpu_buffer_rsrc_t rq = rsrc(wq + h * DH, (DM * NH * DH - h * DH) * 2);
    const int oob = tid < 480 ? 0 : OOB;
#pragma unroll
    for (int e = 0; e < 4; ++e) wqv[e] = ld16<AUX_NT>(rq, (sh * 80 + qr * 4) * (NH * DH * 2) + qc * 16 + oob, e * (NH * DH * 2));
#pragma unroll
    for (int e = 0; e < 4; ++e) xq[e] = ld8<AUX_NT>(rx, (sh * 80 + qr * 4) * 2 + e * (DM * 2) + oob, 0);
  }
  LAS int* ctr = (LAS int*)(L + L_MISC + 4);
  if (tid == 0) *ctr = 21;
  __syncthreads();
  const size_t kvoff = ((size_t)(b * NH + h) * SK + (size_t)s * KEYS_WG) * DH;
  const u16* Kb = Kc + kvoff; const u16* Vb = Vc + kvoff;
  const int kvo = k * ROWB + c * 16;
  v4u R[NT][3];
  v4u W[32];
  {
    const __amdgpu_buffer_rsrc_t r0 = rsrc(Kb, w == 0 ? 0 : KEYS_WG * ROWB);
#pragma unroll
    for (int t = 0; t < NT; ++t)
#pragma unroll
      for (int i = 0; i < 3; ++i) R[t][i] = ld16<AUX_NT>(r0, kvo + i * 128, ((w + 7) & 7) % 7 * GRPB + t * TILEB);
  }
  SCHED_FENCE();
  STAMP(1);
  const unsigned tagA = (unsigned)epoch + 1u, tagB = (unsigned)(epoch >> 32) + 1u;
  {
    unsigned lo01[4], hi01[4], lo23[4], hi23[4];
#pragma unroll
    for (int d = 0; d < 4; ++d) {
      lo01[d] = __builtin_amdgcn_perm(wqv[1][d], wqv[0][d], 0x05040100u); hi01[d] = __builtin_amdgcn_perm(wqv[1][d], wqv[0][d], 0x07060302u);
      lo23[d] = __builtin_amdgcn_perm(wqv[3][d], wqv[2][d], 0x05040100u); hi23[d] = __builtin_amdgcn_perm(wqv[3][d], wqv[2][d], 0x07060302u);
    }
#pragma unroll
    for (int bb = 0; bb < 4; ++bb) {
      float a[8];
#pragma unroll
      for (int d = 0; d < 4; ++d) { a[2 * d] = dot2(lo23[d], xq[bb][1], dot2(lo01[d], xq[bb][0], 0.f)); a[2 * d + 1] = dot2(hi23[d], xq[bb][1], dot2(hi01[d], xq[bb][0], 0.f)); }
      LAS v4f* p = (LAS v4f*)(L + L_FP + ((qr * 4 + bb) * 192 + qc * 8) * 4);
      p[0] = v4f{a[0], a[1], a[2], a[3]}; p[1] = v4f{a[4], a[5], a[6], a[7]};
    }
  }
  __syncthreads();
  STAMP(2);
  if (tid < 384) {
    const int bb = tid / 96, p = tid - bb * 96;
    float a0 = 0.f, a1 = 0.f;
#pragma unroll
    for (int r = 0; r < 20; ++r) { const v2f v = *(LAS v2f*)(L + L_FP + ((r * 4 + bb) * 192 + p * 2) * 4); a0 += v[0]; a1 += v[1]; }
    __builtin_amdgcn_raw_buffer_store_b128(v4u{__float_as_uint(a0), tagA, __float_as_uint(a1), tagB}, rws, WS_QX + (((h * 4 + bb) * 8 + sh) * 128 + p) * 16, 0, AUX_SC1);
  }
  v4u Q[3];
  float mx = -INFINITY;
  LAS float* sc = (LAS float*)(L + L_SC);
  const bool lane0 = lane == 0, oct0 = c == 0;
  const int scoff = k * 16;
  auto claim_issue = [&]() -> int { int v = 0; if (lane0) v = __hip_atomic_fetch_add(ctr, 1, __ATOMIC_RELAXED, __HIP_MEMORY_SCOPE_WORKGROUP); return v; };
  auto grp_rsrc = [&](int g) { return rsrc(g < NG ? Kb : Vb, g < 2 * NG ? KEYS_WG * ROWB : 0); };
  auto k_tile = [&](const v4u& r0, const v4u& r1, const v4u& r2) -> float {
    float a0 = dot2(r0[0], Q[0][0], 0.f), a1 = dot2(r1[0], Q[1][0], 0.f), a2 = dot2(r2[0], Q[2][0], 0.f);
#pragma unroll
    for (int d = 1; d < 4; ++d) { a0 = dot2(r0[d], Q[0][d], a0); a1 = dot2(r1[d], Q[1][d], a1); a2 = dot2(r2[d], Q[2][d], a2); }
    return (a0 + a1) + a2;
  };
  auto k_finish = [&](float (&sv)[NT], int g) {
#pragma unroll
    for (int t = 0; t < NT; ++t) sv[t] = oct_sum(sv[t]);
    mx = fmaxf(fmaxf(mx, fmaxf(sv[0], sv[1])), fmaxf(sv[2], sv[3]));
    if (oct0) *(LAS v4f*)(L + L_SC + g * 128 + scoff) = v4f{sv[0], sv[1], sv[2], sv[3]};
  };
  int cur, nxt;
  auto k_iter = [&]() {
    const int cl = claim_issue();
    const __amdgpu_buffer_rsrc_t rn = grp_rsrc(nxt);
    const int so = (nxt & (NG - 1)) * GRPB;
    float sv[NT];
#pragma unroll
    for (int t = 0; t < NT; ++t) {
      sv[t] = k_tile(R[t][0], R[t][1], R[t][2]);
      asm volatile("" : "+v"(sv[t]) : : "memory");
#pragma unroll
      for (int i = 0; i < 3; ++i) R[t][i] = ld16<AUX_NT>(rn, kvo + i * 128, so + t * TILEB);
      SCHED_FENCE();
    }
    k_finish(sv, cur);
    cur = nxt; nxt = __builtin_amdgcn_readfirstlane(cl);
  };

  if (w == 0) {
    {
    const int qo = WS_QX + (h * 4 + b) * 8 * 2048 + lane * 16;
    v4u ga[8], gb[8];
    for (unsigned spins = 0;;) {
      MEM_FENCE();
#pragma unroll
      for (int p = 0; p < 8; ++p) { ga[p] = ld16<AUX_SC1>(rws, qo + p * 2048, 0); gb[p] = ld16<AUX_SC1>(rws, qo + p * 2048 + 1024 + (lane < 32 ? 0 : OOB), 0); }
      bool ok = true;
#pragma unroll
      for (int p = 0; p < 8; ++p) ok = ok & (ga[p][1] == tagA) & (ga[p][3] == tagB) & ((lane >= 32) | ((gb[p][1] == tagA) & (gb[p][3] == tagB)));
      if (__all(ok) || ++spins > SPIN_CAP) break;
      __builtin_amdgcn_s_sleep(1);
    }
    float a0 = 0.f, a1 = 0.f, b0 = 0.f, b1 = 0.f;
#pragma unroll
    for (int p = 0; p < 8; ++p) { a0 += __uint_as_float(ga[p][0]); a1 += __uint_as_float(ga[p][2]); b0 += __uint_as_float(gb[p][0]); b1 += __uint_as_float(gb[p][2]); }
    *(LAS unsigned*)(L + L_QB + lane * 4) = cvtpk(a0, a1);
    if (lane < 32) *(LAS unsigned*)(L + L_QB + 256 + lane * 4) = cvtpk(b0, b1);
    }
    __syncthreads();
    STAMP(3);
#pragma unroll
    for (int i = 0; i < 3; ++i) Q[i] = *(LAS v4u*)(L + L_QB + (c + 8 * i) * 16);
    const int lane = fresh_lane();
    const __amdgpu_buffer_rsrc_t rx = rsrc(x + b * DM, DM * 2), rg = rsrc(gw, DM * 2);
    const v4u gx0 = ld16<AUX_NT>(rx, lane * 16, 0), gx1 = ld16<AUX_NT>(rx, 1024 + lane * 16, 0);
    const v4u gg0 = ld16<AUX_NT>(rg, lane * 16, 0), gg1 = ld16<AUX_NT>(rg, 1024 + lane * 16, 0);
    const int fr = lane / 3, fc = lane - fr * 3;
    const int vo = fr * (FF * 2) + j * 48 + fc * 16 + (lane < 60 ? 0 : OOB);
    {
      const __amdgpu_buffer_rsrc_t r1 = rsrc(w1, DM * FF * 2);
#pragma unroll
      for (int t = 0; t < 32; ++t) W[t] = ld16<AUX_NT>(r1, vo, t * 20 * (FF * 2));
    }
    SCHED_FENCE();
    *(LAS v4u*)(L + L_XS + lane * 16) = gx0;
    if (lane < 16) *(LAS v4u*)(L + L_XS + 1024 + lane * 16) = gx1;
    float d = 0.f;
#pragma unroll
    for (int e = 0; e < 4; ++e) { d = dot2(gx0[e], gg0[e], d); d = dot2(gx1[e], gg1[e], d); }
    d = wave_sum(d);
    const float gate = 1.f / (1.f + __expf(-d));
    if (lane == 0) *(LAS float*)(L + L_MISC) = gate;
    LDS_FENCE();
    float ga[8], ua[8];
#pragma unroll
    for (int e = 0; e < 8; ++e) { ga[e] = 0.f; ua[e] = 0.f; }
    const LAS u16* xs = (const LAS u16*)(L + L_XS) + (fr < 20 ? fr : 0);
#pragma unroll
    for (int t = 0; t < 32; ++t) {
      const unsigned xl = xs[t * 20], xh = xl << 16;
#pragma unroll
      for (int dd = 0; dd < 4; ++dd) { ga[2 * dd] = dot2(W[t][dd], xl, ga[2 * dd]); ga[2 * dd + 1] = dot2(W[t][dd], xh, ga[2 * dd + 1]); }
    }
    asm volatile("" : "+v"(ga[0]), "+v"(ga[1]), "+v"(ga[2]), "+v"(ga[3]), "+v"(ga[4]), "+v"(ga[5]), "+v"(ga[6]), "+v"(ga[7]) : : "memory");
    {
      const __amdgpu_buffer_rsrc_t r3 = rsrc(w3, DM * FF * 2);
#pragma unroll
      for (int t = 0; t < 32; ++t) W[t] = ld16<AUX_NT>(r3, vo, t * 20 * (FF * 2));
    }
    SCHED_FENCE();
#pragma unroll
    for (int t = 0; t < 32; ++t) {
      const unsigned xl = xs[t * 20], xh = xl << 16;
#pragma unroll
      for (int dd = 0; dd < 4; ++dd) { ua[2 * dd] = dot2(W[t][dd], xl, ua[2 * dd]); ua[2 * dd + 1] = dot2(W[t][dd], xh, ua[2 * dd + 1]); }
    }
    asm volatile("" : "+v"(ua[0]), "+v"(ua[1]), "+v"(ua[2]), "+v"(ua[3]), "+v"(ua[4]), "+v"(ua[5]), "+v"(ua[6]), "+v"(ua[7]) : : "memory");
    {
      const __amdgpu_buffer_rsrc_t r2 = rsrc(w2 + j * 24 * DM, 24 * DM * 2);
#pragma unroll
      for (int t = 0; t < 24; ++t) W[t] = ld16<AUX_NT>(r2, lane * 16, t * (DM * 2));
#pragma unroll
      for (int t = 0; t < 6; ++t) W[24 + t] = ld16<AUX_NT>(r2, (lane >> 4) * (DM * 2) + 1024 + (lane & 15) * 16, t * 4 * (DM * 2));
    }
    SCHED_FENCE();
    if (lane < 60) {
      LAS v4f* p = (LAS v4f*)(L + L_FP + lane * 64);
      p[0] = v4f{ga[0], ga[1], ga[2], ga[3]}; p[1] = v4f{ga[4], ga[5], ga[6], ga[7]};
      p[2] = v4f{ua[0], ua[1], ua[2], ua[3]}; p[3] = v4f{ua[4], ua[5], ua[6], ua[7]};
    }
    LDS_FENCE();
    {
      const int u = lane < 24 ? lane : (lane < 48 ? lane - 24 : 0), m = lane >= 24 ? 1 : 0;
      const LAS float* p = (const LAS float*)(L + L_FP) + (u >> 3) * 16 + m * 8 + (u & 7);
      float a = 0.f;
#pragma unroll
      for (int r = 0; r < 20; ++r) a += p[r * 48];
      LAS float* sx = (LAS float*)(L + L_MISC + 192);
      if (lane < 48) sx[lane] = a;
      LDS_FENCE();
      if (lane < 24) { const float up = sx[24 + lane]; *(LAS float*)(L + L_MISC + 64 + lane * 4) = a / (1.f + __expf(-a)) * up * (1.f - gate); }
      LDS_FENCE();
    }
    {
      v2f ya[4], yb[4];
#pragma unroll
      for (int e = 0; e < 4; ++e) { ya[e] = v2f{0.f, 0.f}; yb[e] = v2f{0.f, 0.f}; }
      const LAS float* hs = (const LAS float*)(L + L_MISC + 64);
#pragma unroll
      for (int t = 0; t < 24; ++t) {
        const float hv = hs[t];
#pragma unroll
        for (int dd = 0; dd < 4; ++dd) ya[dd] = __builtin_elementwise_fma(v2f{hv, hv}, v2f{bflo(W[t][dd]), bfhi(W[t][dd])}, ya[dd]);
        asm volatile("" : "+v"(ya[0]), "+v"(ya[1]), "+v"(ya[2]), "+v"(ya[3]));
        SCHED_FENCE();
      }
#pragma unroll
      for (int t = 0; t < 6; ++t) {
        const float hv = hs[t * 4 + (lane >> 4)];
#pragma unroll
        for (int dd = 0; dd < 4; ++dd) yb[dd] = __builtin_elementwise_fma(v2f{hv, hv}, v2f{bflo(W[24 + t][dd]), bfhi(W[24 + t][dd])}, yb[dd]);
        asm volatile("" : "+v"(yb[0]), "+v"(yb[1]), "+v"(yb[2]), "+v"(yb[3]));
        SCHED_FENCE();
      }
#pragma unroll
      for (int e = 0; e < 4; ++e) { yb[e][0] = sum16_32(yb[e][0]); yb[e][1] = sum16_32(yb[e][1]); }
      LAS v4f* p = (LAS v4f*)(L + L_YF + lane * 32);
      p[0] = v4f{ya[0][0], ya[0][1], ya[1][0], ya[1][1]}; p[1] = v4f{ya[2][0], ya[2][1], ya[3][0], ya[3][1]};
      if (lane < 16) { LAS v4f* p2 = (LAS v4f*)(L + L_YF + 2048 + lane * 32); p2[0] = v4f{yb[0][0], yb[0][1], yb[1][0], yb[1][1]}; p2[1] = v4f{yb[2][0], yb[2][1], yb[3][0], yb[3][1]}; }
    }
    MEM_FENCE();
    cur = __builtin_amdgcn_readfirstlane(claim_issue());
    const __amdgpu_buffer_rsrc_t rn = grp_rsrc(cur);
#pragma unroll
    for (int t = 0; t < NT; ++t)
#pragma unroll
      for (int i = 0; i < 3; ++i) R[t][i] = ld16<AUX_NT>(rn, kvo + i * 128, (cur & (NG - 1)) * GRPB + t * TILEB);
    nxt = __builtin_amdgcn_readfirstlane(claim_issue());
  } else {
    {
    const __amdgpu_buffer_rsrc_t r0 = rsrc(Kb, KEYS_WG * ROWB);
#pragma unroll
    for (int g = 0; g < 2; ++g)
#pragma unroll
      for (int t = 0; t < NT; ++t)
#pragma unroll
        for (int i = 0; i < 3; ++i) W[g * 12 + t * 3 + i] = ld16<AUX_NT>(r0, kvo + i * 128, (6 + g * 7 + w) * GRPB + t * TILEB);
    SCHED_FENCE();
    }
    __syncthreads();
    STAMP(3);
#pragma unroll
    for (int i = 0; i < 3; ++i) Q[i] = *(LAS v4u*)(L + L_QB + (c + 8 * i) * 16);
    cur = w - 1;
    nxt = __builtin_amdgcn_readfirstlane(claim_issue());
    k_iter();
#pragma unroll
    for (int g = 0; g < 2; ++g) {
      float sv[NT];
#pragma unroll
      for (int t = 0; t < NT; ++t) sv[t] = k_tile(W[g * 12 + t * 3], W[g * 12 + t * 3 + 1], W[g * 12 + t * 3 + 2]);
      k_finish(sv, 6 + g * 7 + w);
    }
    for (int n = 2 * (w - 1); n > 0 && cur < NG; --n) k_iter();
  }
  STAMP(4);
  SCHED_FENCE();
  {
    const int lane = fresh_lane();
    const int yr = lane / 10, yc = lane - yr * 10;
    const __amdgpu_buffer_rsrc_t ro = rsrc(wo + (size_t)h * DH * DM, DH * DM * 2);
    const int vo = yr * (DM * 2) + w * 160 + yc * 16 + (lane < 60 ? 0 : OOB);
#pragma unroll
    for (int t = 0; t < 32; ++t) W[t] = ld16<AUX_NT>(ro, vo, t * 6 * (DM * 2));
  }
  SCHED_FENCE();
  STAMP(5);
  if (cur < NG) k_iter();
  while (cur < NG) k_iter();

  STAMP(6);
  mx = fmaxf(mx, dpp<XOR8>(mx)); mx = max16_32(mx);
  if (lane0) *(LAS float*)(L + L_MISC + 32 + w * 4) = mx;
  __syncthreads();
  float mw;
  {
    const v4f m0 = *(LAS v4f*)(L + L_MISC + 32), m1 = *(LAS v4f*)(L + L_MISC + 48);
    mw = fmaxf(fmaxf(fmaxf(m0[0], m0[1]), fmaxf(m0[2], m0[3])), fmaxf(fmaxf(m1[0], m1[1]), fmaxf(m1[2], m1[3]))) * CS;
    const int lane = fresh_lane();
    const LAS v4f* p = (const LAS v4f*)(sc + w * 512 + lane * 8);
    const v4f a = p[0], bb = p[1];
    float l = 0.f;
#pragma unroll
    for (int e = 0; e < 4; ++e) l += ex2(fmaf(a[e], CS, -mw)) + ex2(fmaf(bb[e], CS, -mw));
    l = wave_sum(l);
    if (lane == 0) __builtin_amdgcn_raw_buffer_store_b128(v4u{__float_as_uint(mw), tagA, __float_as_uint(l), tagB}, rws, WS_ML + (((b * NH + h) * 2 + s) * 8 + w) * 16, 0, AUX_SC1);
  }
  STAMP(11);

  v2f acc2[12];
#pragma unroll
  for (int e = 0; e < 12; ++e) acc2[e] = v2f{0.f, 0.f};
#define PIN_ACC() asm volatile("" : "+v"(acc2[0]), "+v"(acc2[1]), "+v"(acc2[2]), "+v"(acc2[3]), "+v"(acc2[4]), "+v"(acc2[5]), "+v"(acc2[6]), "+v"(acc2[7]), \
                                    "+v"(acc2[8]), "+v"(acc2[9]), "+v"(acc2[10]), "+v"(acc2[11]) : : "memory")
  auto v_tile = [&](int t, float p) {
    const v2f pp = v2f{p, p};
#pragma unroll
    for (int i = 0; i < 3; ++i)
#pragma unroll
      for (int d = 0; d < 4; ++d) acc2[i * 4 + d] = __builtin_elementwise_fma(pp, v2f{bflo(R[t][i][d]), bfhi(R[t][i][d])}, acc2[i * 4 + d]);
    PIN_ACC();
  };
  while (nxt < 2 * NG) {
    const int cl = claim_issue();
    const v4f s4 = *(LAS v4f*)(L + L_SC + (cur - NG) * 128 + scoff);
    const __amdgpu_buffer_rsrc_t rn = rsrc(Vb, KEYS_WG * ROWB);
    const int so = (nxt - NG) * GRPB;
#pragma unroll
    for (int t = 0; t < NT; ++t) {
      v_tile(t, ex2(fmaf(s4[t], CS, -mw)));
#pragma unroll
      for (int i = 0; i < 3; ++i) R[t][i] = ld16<AUX_NT>(rn, kvo + i * 128, so + t * TILEB);
      SCHED_FENCE();
    }
    cur = nxt; nxt = __builtin_amdgcn_readfirstlane(cl);
  }
  const int mloff = WS_ML + ((b * NH + h) * 16 + (fresh_lane() & 15)) * 16;
  v4u ml = ld16<AUX_SC1>(rws, mloff, 0);
  SCHED_FENCE();
  {
    const v4f s4 = *(LAS v4f*)(L + L_SC + (cur - NG) * 128 + scoff);
#pragma unroll
    for (int t = 0; t < NT; ++t) {
#pragma unroll
      for (int u = 4 * t; u < 4 * t + 4; ++u) {
#pragma unroll
        for (int d = 0; d < 4; ++d) {
          const unsigned a = W[2 * u][d], bb = W[2 * u + 1][d];
          W[2 * u][d] = __builtin_amdgcn_perm(bb, a, 0x05040100u); W[2 * u + 1][d] = __builtin_amdgcn_perm(bb, a, 0x07060302u);
        }
        asm volatile("" : "+v"(W[2 * u]), "+v"(W[2 * u + 1]));
        SCHED_FENCE();
      }
      v_tile(t, ex2(fmaf(s4[t], CS, -mw)));
      SCHED_FENCE();
    }
  }

  STAMP(7);
  for (unsigned spins = 0; !__all((ml[1] == tagA) & (ml[3] == tagB)); ) {
    if (++spins > SPIN_CAP) break;
    __builtin_amdgcn_s_sleep(2);
    MEM_FENCE();
    ml = ld16<AUX_SC1>(rws, mloff, 0);
  }
  {
    const float m_i = __uint_as_float(ml[0]), l_i = __uint_as_float(ml[2]);
    float M = m_i;
    M = fmaxf(M, dpp<XOR8>(M));
    float Ls = l_i * ex2(m_i - M);
    Ls = oct_sum(Ls); Ls += dpp<XOR8>(Ls);
    const float gate = *(LAS float*)(L + L_MISC);
    const float f = ex2(mw - M) / Ls * gate;
    float acc[24];
#pragma unroll
    for (int e = 0; e < 24; ++e) acc[e] = xoct_sum(acc2[e >> 1][e & 1]) * f;
    const int c = fresh_lane();
    if (c < 8) {
#pragma unroll
      for (int i = 0; i < 3; ++i) {
        LAS v4f* p = (LAS v4f*)(L + L_QP + (w * 192 + i * 64 + c * 8) * 4);
        p[0] = v4f{acc[i * 8], acc[i * 8 + 1], acc[i * 8 + 2], acc[i * 8 + 3]}; p[1] = v4f{acc[i * 8 + 4], acc[i * 8 + 5], acc[i * 8 + 6], acc[i * 8 + 7]};
      }
    }
  }
  STAMP(8);
  __syncthreads();
  const int tid2 = w * 64 + fresh_lane();
  if (tid2 < 96) {
    const int r = tid2 >> 4, u = tid2 & 15;
    float a0 = 0.f, a1 = 0.f;
#pragma unroll
    for (int ww = 0; ww < 8; ++ww) { const LAS float* p = (const LAS float*)(L + L_QP) + ww * 192 + u * 12 + r; a0 += p[0]; a1 += p[6]; }
    *(LAS unsigned*)(L + L_OB + tid2 * 4) = cvtpk(a0, a1);
  }
  __syncthreads();
  {
    const int ln = fresh_lane();
    const int yr = ln < 60 ? ln / 10 : 0, yc = ln - (ln / 10) * 10;
    LAS float* yrp = (LAS float*)(L + L_FP) + w * 480;
    float ya[8];
#pragma unroll
    for (int e = 0; e < 8; ++e) ya[e] = 0.f;
    const LAS v4u* op = (const LAS v4u*)(L + L_OB + yr * 64);
#pragma unroll
    for (int u4 = 0; u4 < 4; ++u4) {
      const v4u o4 = op[u4];
#pragma unroll
      for (int e = 0; e < 4; ++e) {
        const int u = u4 * 4 + e;
#pragma unroll
        for (int d = 0; d < 4; ++d) { ya[2 * d] = dot2(W[2 * u][d], o4[e], ya[2 * d]); ya[2 * d + 1] = dot2(W[2 * u + 1][d], o4[e], ya[2 * d + 1]); }
      }
    }
    if (ln < 60) { LAS v4f* p = (LAS v4f*)(yrp + yr * 80 + yc * 8); p[0] = v4f{ya[0], ya[1], ya[2], ya[3]}; p[1] = v4f{ya[4], ya[5], ya[6], ya[7]}; }
    LDS_FENCE();
    if (ln < 40) {
      v2f y = *(LAS v2f*)(L + L_YF + (w * 80 + ln * 2) * 4);
#pragma unroll
      for (int r = 0; r < 6; ++r) y += *(LAS v2f*)(yrp + r * 80 + ln * 2);
      const int cons = w * 8 + ln / 5, g5 = ln % 5;
      __builtin_amdgcn_raw_buffer_store_b128(v4u{__float_as_uint(y[0]), tagA, __float_as_uint(y[1]), tagB}, rws, WS_INBOX + (((b * 64 + cons) * 64 + j) * 5 + g5) * 16, 0, AUX_SC1);
    }
  }
  STAMP(9);
  if (w == 0) {
    const int lane = fresh_lane();
    const int io = WS_INBOX + ((b * 64 + j) * 64 + lane) * 80;
    v4u g5[5];
    for (unsigned spins = 0;;) {
      bool ok = true;
      MEM_FENCE();
#pragma unroll
      for (int e = 0; e < 5; ++e) { g5[e] = ld16<AUX_SC1>(rws, io + e * 16, 0); }
#pragma unroll
      for (int e = 0; e < 5; ++e) ok = ok & (g5[e][1] == tagA) & (g5[e][3] == tagB);
      if (__all(ok) || ++spins > SPIN_CAP) break;
      __builtin_amdgcn_s_sleep(2);
    }
    float y[10];
#pragma unroll
    for (int e = 0; e < 5; ++e) { y[2 * e] = wave_sum(__uint_as_float(g5[e][0])); y[2 * e + 1] = wave_sum(__uint_as_float(g5[e][2])); }
    unsigned o = cvtpk(y[0], y[1]);
#pragma unroll
    for (int e = 1; e < 5; ++e) { const unsigned v = cvtpk(y[2 * e], y[2 * e + 1]); o = (lane == e) ? v : o; }
    if (lane < 5) *(GAS unsigned*)(out + b * DM + j * 10 + lane * 2) = o;
    if (b == 0 && j == 0 && lane == 0) __hip_atomic_store((GAS u64*)(ws + WS_EPOCH), ((u64)tagB << 32) | tagA, __ATOMIC_RELAXED, __HIP_MEMORY_SCOPE_AGENT);
  }
}
}

extern "C" void kernel_launch(void* const* d_in, const int* in_sizes, int n_in, void* d_out, int out_size, void* d_ws, size_t ws_size,
                              hipStream_t stream) {
  (void)in_sizes; (void)n_in; (void)out_size; (void)ws_size;
  hipLaunchKernelGGL(fused_decode, dim3(NWG), dim3(NTHR), 0, stream, (const u16*)d_in[0], (const u16*)d_in[1], (const u16*)d_in[2], (const u16*)d_in[3],
                     (const u16*)d_in[4], (const u16*)d_in[6], (const u16*)d_in[7], (const u16*)d_in[8], (const u16*)d_in[9], (u16*)d_out, (unsigned char*)d_ws);
}
```

```cpp
#include <hip/hip_runtime.h>
#include <hip/hip_bf16.h>
#include <stdint.h>

#define GAS __attribute__((address_space(1)))
#define LAS __attribute__((address_space(3)))
typedef unsigned v4u __attribute__((ext_vector_type(4)));
typedef unsigned v2u __attribute__((ext_vector_type(2)));
typedef float v4f __attribute__((ext_vector_type(4)));
typedef float v2f __attribute__((ext_vector_type(2)));
typedef __bf16 bf16x2 __attribute__((ext_vector_type(2)));
typedef unsigned long long u64;
typedef unsigned short u16;

namespace {
constexpr int NB = 4, NH = 32, SK = 8192, DH = 192, DM = 640, FF = 1536;
constexpr int NWG = 256, NWAVE = 8, NTHR = NWAVE * 64;
constexpr int KEYS_WG = SK / 2;
constexpr int ROWB = DH * 2;
constexpr int TILEB = 8 * ROWB;
constexpr int GRPB = 2 * TILEB;
constexpr int KVB = KEYS_WG * ROWB;
constexpr int NGW = KVB / GRPB / NWAVE;
constexpr int RSRC3 = 0x00020000;
#define POLL_AUX 16
#define STORE_AUX 16
constexpr int AUX_NT = 2, AUX_SC1 = STORE_AUX;
constexpr int OOB = 1 << 28;
constexpr float CS = 0.07216878364870322f * 1.4426950408889634f;
#define TAU16 (180 * 16)
#define PHW 0

constexpr int WS_EPOCH = 0;
constexpr int WS_ML = 4096;
constexpr int WS_INBOX = 65536;
constexpr int WS_QX = WS_INBOX + NB * 64 * 64 * 80;
constexpr int WS_FX = WS_QX + NH * NB * 8 * 128 * 16;
constexpr int WS_BYTES = WS_FX + 8 * NB * 8 * 384 * 16;

constexpr int L_XS = 0;
constexpr int L_QP = 1280;
constexpr int L_QB = L_QP + 8 * 192 * 4;
constexpr int L_OB = L_QB + 384;
constexpr int L_MISC = L_OB + 384;
constexpr int L_G = L_MISC + 256;
constexpr int L_YF = L_G + 3072;
constexpr int L_SC = L_YF + 8 * 480 * 4;
constexpr int L_FP2 = L_YF;
constexpr int L_FP = L_YF + 22 * 4 * 192 * 4;
constexpr int L_TR = L_FP + 22 * 4 * 192 * 4;
constexpr int L_BYTES = L_TR;

constexpr unsigned SPIN_CAP = 1u << 17;

__device__ __forceinline__ float dot2(unsigned a, unsigned b, float c) {
  return __builtin_amdgcn_fdot2_f32_bf16(__builtin_bit_cast(bf16x2, a), __builtin_bit_cast(bf16x2, b), c, false);
}
template <int CTRL> __device__ __forceinline__ float dpp(float x) {
  return __builtin_bit_cast(float, __builtin_amdgcn_mov_dpp(__builtin_bit_cast(int, x), CTRL, 0xf, 0xf, true));
}
constexpr int XOR1 = 0xB1, XOR2 = 0x4E, XOR7 = 0x141, XOR8 = 0x128;
__device__ __forceinline__ float sum16_32(float x) {
  auto s = __builtin_amdgcn_permlane16_swap(__float_as_uint(x), __float_as_uint(x), false, false);
  x = __uint_as_float(s[0]) + __uint_as_float(s[1]);
  auto t = __builtin_amdgcn_permlane32_swap(__float_as_uint(x), __float_as_uint(x), false, false);
  return __uint_as_float(t[0]) + __uint_as_float(t[1]);
}
__device__ __forceinline__ float max16_32(float x) {
  auto s = __builtin_amdgcn_permlane16_swap(__float_as_uint(x), __float_as_uint(x), false, false);
  x = fmaxf(__uint_as_float(s[0]), __uint_as_float(s[1]));
  auto t = __builtin_amdgcn_permlane32_swap(__float_as_uint(x), __float_as_uint(x), false, false);
  return fmaxf(__uint_as_float(t[0]), __uint_as_float(t[1]));
}
__device__ __forceinline__ float oct_sum(float x) { x += dpp<XOR1>(x); x += dpp<XOR2>(x); x += dpp<XOR7>(x); return x; }
__device__ __forceinline__ float xoct_sum(float x) { x += dpp<XOR8>(x); return sum16_32(x); }
__device__ __forceinline__ float wave_sum(float x) { return xoct_sum(oct_sum(x)); }
__device__ __forceinline__ unsigned cvtpk(float lo, float hi) { unsigned r; asm("v_cvt_pk_bf16_f32 %0, %1, %2" : "=v"(r) : "v"(lo), "v"(hi)); return r; }
__device__ __forceinline__ float bflo(unsigned u) { return __uint_as_float(u << 16); }
__device__ __forceinline__ float bfhi(unsigned u) { return __uint_as_float(u & 0xffff0000u); }
__device__ __forceinline__ float ex2(float x) { return __builtin_amdgcn_exp2f(x); }
__device__ __forceinline__ __amdgpu_buffer_rsrc_t rsrc(const void* p, int bytes) { return __builtin_amdgcn_make_buffer_rsrc((void*)p, (short)0, bytes, RSRC3); }
template <int AUX> __device__ __forceinline__ v4u ld16(__amdgpu_buffer_rsrc_t r, int voff, int soff) { return __builtin_amdgcn_raw_buffer_load_b128(r, voff, soff, AUX); }
template <int AUX> __device__ __forceinline__ v2u ld8(__amdgpu_buffer_rsrc_t r, int voff, int soff) { return __builtin_amdgcn_raw_buffer_load_b64(r, voff, soff, AUX); }
__device__ __forceinline__ unsigned now32() { return (unsigned)__builtin_amdgcn_s_memrealtime(); }
#define STAMP(i) do { } while (0)
#define TR(id) do { } while (0)
#define SCHED_FENCE() __builtin_amdgcn_sched_barrier(0)
#define LDS_FENCE() asm volatile("s_waitcnt lgkmcnt(0)" ::: "memory")
#define MEM_FENCE() asm volatile("" ::: "memory")
__device__ __forceinline__ v4u poll16(__amdgpu_buffer_rsrc_t r, int voff) { asm volatile("" : "+v"(voff) : : "memory"); return __builtin_amdgcn_raw_buffer_load_b128(r, voff, 0, POLL_AUX); }
__device__ __forceinline__ int fresh_lane() { int l; asm volatile("v_mbcnt_lo_u32_b32 %0, -1, 0\n\tv_mbcnt_hi_u32_b32 %0, -1, %0" : "=v"(l)); return l; }

__device__ __forceinline__ void dma16(__amdgpu_buffer_rsrc_t r, int voff, int soff, unsigned lds) {
  unsigned keep;
  asm volatile("s_mov_b32 %0, m0\n\ts_mov_b32 m0, %3\n\ts_nop 0\n\tbuffer_load_dwordx4 %1, %2, %4 offen nt lds\n\ts_mov_b32 m0, %0"
               : "=&s"(keep) : "v"(voff), "s"(r), "s"(lds), "s"(soff) : "memory");
}
struct Grp { v4u r[2][3]; };

__global__ void __launch_bounds__(NTHR, 2)
fused_decode(const u16* __restrict__ x, const u16* __restrict__ Kc, const u16* __restrict__ Vc, const u16* __restrict__ wq,
             const u16* __restrict__ wo, const u16* __restrict__ w1, const u16* __restrict__ w3, const u16* __restrict__ w2,
             const u16* __restrict__ gw, u16* __restrict__ out, unsigned char* ws) {
  __shared__ __attribute__((aligned(16))) unsigned char lds_raw[L_BYTES];
  LAS unsigned char* L = (LAS unsigned char*)lds_raw;
  const int tid = threadIdx.x, lane = tid & 63, w = __builtin_amdgcn_readfirstlane(tid >> 6);
  const int k = lane >> 3, c = lane & 7;
  const int blk = blockIdx.x, xg = blk & 7, idx = blk >> 3;
  const int h = xg * 4 + (idx & 3), sh = idx >> 2, b = sh >> 1, s = sh & 1;
  const int j = h * 2 + s;
  STAMP(0);
  unsigned T0 = now32() + 100 + (PHW ? w * (TAU16 / 8) / 16 : idx * (TAU16 / 32) / 16);
  const __amdgpu_buffer_rsrc_t rws = rsrc(ws, WS_BYTES);
  const u64 epoch = __hip_atomic_load((GAS u64*)(ws + WS_EPOCH), __ATOMIC_RELAXED, __HIP_MEMORY_SCOPE_AGENT);
  const int qr = tid / 24, qc = tid - qr * 24;
  v2u xq[4]; v4u wqv[4], fwa, fwb; unsigned fx4[4];
  {
    const __amdgpu_buffer_rsrc_t rx = rsrc(x, NB * DM * 2);
    const __amdgpu_buffer_rsrc_t rq = rsrc(wq + h * DH, (DM * NH * DH - h * DH) * 2);
    const int oob = tid < 480 ? 0 : OOB;
#pragma unroll
    for (int e = 0; e < 4; ++e) wqv[e] = ld16<AUX_NT>(rq, (sh * 80 + qr * 4) * (NH * DH * 2) + qc * 16 + oob, e * (NH * DH * 2));
#pragma unroll
    for (int e = 0; e < 4; ++e) xq[e] = ld8<AUX_NT>(rx, (sh * 80 + qr * 4) * 2 + e * (DM * 2) + oob, 0);
    const int row0 = (idx >> 1) * 40 + qr * 2;
    const __amdgpu_buffer_rsrc_t r13 = rsrc(((idx & 1) ? w3 : w1) + xg * 192, (DM * FF - xg * 192) * 2);
    fwa = ld16<AUX_NT>(r13, row0 * (FF * 2) + qc * 16 + oob, 0); fwb = ld16<AUX_NT>(r13, row0 * (FF * 2) + qc * 16 + oob, FF * 2);
#pragma unroll
    for (int e = 0; e < 4; ++e) fx4[e] = __builtin_amdgcn_raw_buffer_load_b32(rx, row0 * 2 + e * (DM * 2) + oob, 0, AUX_NT);
  }
  const size_t kvoff = ((size_t)(b * NH + h) * SK + (size_t)s * KEYS_WG) * DH;
  const u16* Kb = Kc + kvoff; const u16* Vb = Vc + kvoff;
  const int kvo = w * GRPB + k * ROWB + c * 16;
  auto pace = [&](int m) { const unsigned tt = T0 + ((unsigned)(m * TAU16) >> 4); while ((int)(now32() - tt) < 0) __builtin_amdgcn_s_sleep(1); };
  int wo_t = -1;
  __amdgpu_buffer_rsrc_t ro = rsrc(wo + (size_t)h * DH * DM, DH * DM * 2);
  int wo_vo = 0;
  auto issue = [&](v4u& r00, v4u& r01, v4u& r02, v4u& r10, v4u& r11, v4u& r12, int m) {
    TR(1);
    pace(m);
    TR(2);
    if (wo_t >= 0) dma16(ro, wo_vo, wo_t * 6 * (DM * 2), (unsigned)(L_FP + w * 8192 + (wo_t & 7) * 1024));
    const __amdgpu_buffer_rsrc_t r = rsrc(m < NGW ? Kb : Vb, KVB);
    const int so = (m & (NGW - 1)) * (NWAVE * GRPB);
    r00 = ld16<AUX_NT>(r, kvo, so); r01 = ld16<AUX_NT>(r, kvo + 128, so); r02 = ld16<AUX_NT>(r, kvo + 256, so);
    r10 = ld16<AUX_NT>(r, kvo, so + TILEB); r11 = ld16<AUX_NT>(r, kvo + 128, so + TILEB); r12 = ld16<AUX_NT>(r, kvo + 256, so + TILEB);
    SCHED_FENCE();
    TR(3);
  };
#define ISSUE(G, m) issue(G.r[0][0], G.r[0][1], G.r[0][2], G.r[1][0], G.r[1][1], G.r[1][2], m)
#define ISSUE_W(o, m) issue(W[o], W[o + 1], W[o + 2], W[o + 3], W[o + 4], W[o + 5], m)
  Grp A, B;
  v4u W[32];
  ISSUE(A, 0);
  ISSUE(B, 1);
  STAMP(1);
  const unsigned tagA = (unsigned)epoch + 1u, tagB = (unsigned)(epoch >> 32) + 1u;
  {
    unsigned lo01[4], hi01[4], lo23[4], hi23[4];
#pragma unroll
    for (int d = 0; d < 4; ++d) {
      lo01[d] = __builtin_amdgcn_perm(wqv[1][d], wqv[0][d], 0x05040100u); hi01[d] = __builtin_amdgcn_perm(wqv[1][d], wqv[0][d], 0x07060302u);
      lo23[d] = __builtin_amdgcn_perm(wqv[3][d], wqv[2][d], 0x05040100u); hi23[d] = __builtin_amdgcn_perm(wqv[3][d], wqv[2][d], 0x07060302u);
    }
#pragma unroll
    for (int bb = 0; bb < 4; ++bb) {
      float a[8];
#pragma unroll
      for (int d = 0; d < 4; ++d) { a[2 * d] = dot2(lo23[d], xq[bb][1], dot2(lo01[d], xq[bb][0], 0.f)); a[2 * d + 1] = dot2(hi23[d], xq[bb][1], dot2(hi01[d], xq[bb][0], 0.f)); }
      LAS v4f* p = (LAS v4f*)(L + L_FP + ((qr * 4 + bb) * 192 + qc * 8) * 4);
      p[0] = v4f{a[0], a[1], a[2], a[3]}; p[1] = v4f{a[4], a[5], a[6], a[7]};
    }
  }
  {
    unsigned lo[4], hi[4];
#pragma unroll
    for (int d = 0; d < 4; ++d) { lo[d] = __builtin_amdgcn_perm(fwb[d], fwa[d], 0x05040100u); hi[d] = __builtin_amdgcn_perm(fwb[d], fwa[d], 0x07060302u); }
#pragma unroll
    for (int bb = 0; bb < 4; ++bb) {
      LAS v4f* p = (LAS v4f*)(L + L_FP2 + ((qr * 4 + bb) * 192 + qc * 8) * 4);
      p[0] = v4f{dot2(lo[0], fx4[bb], 0.f), dot2(hi[0], fx4[bb], 0.f), dot2(lo[1], fx4[bb], 0.f), dot2(hi[1], fx4[bb], 0.f)};
      p[1] = v4f{dot2(lo[2], fx4[bb], 0.f), dot2(hi[2], fx4[bb], 0.f), dot2(lo[3], fx4[bb], 0.f), dot2(hi[3], fx4[bb], 0.f)};
    }
  }
  TR(9);
  __syncthreads();
  TR(10);
  STAMP(2);
  if (tid < 384) {
    const int bb = tid / 96, p = tid - bb * 96;
    float a0 = 0.f, a1 = 0.f;
#pragma unroll
    for (int r = 0; r < 20; ++r) { const v2f v = *(LAS v2f*)(L + L_FP + ((r * 4 + bb) * 192 + p * 2) * 4); a0 += v[0]; a1 += v[1]; }
    __builtin_amdgcn_raw_buffer_store_b128(v4u{__float_as_uint(a0), tagA, __float_as_uint(a1), tagB}, rws, WS_QX + (((h * 4 + bb) * 8 + sh) * 128 + p) * 16, 0, AUX_SC1);
    a0 = 0.f; a1 = 0.f;
#pragma unroll
    for (int r = 0; r < 20; ++r) { const v2f v = *(LAS v2f*)(L + L_FP2 + ((r * 4 + bb) * 192 + p * 2) * 4); a0 += v[0]; a1 += v[1]; }
    __builtin_amdgcn_raw_buffer_store_b128(v4u{__float_as_uint(a0), tagA, __float_as_uint(a1), tagB}, rws,
                                           WS_FX + (((((xg * 4 + bb) * 8 + p / 12) * 2 + (idx & 1)) * 16 + (idx >> 1)) * 12 + p % 12) * 16, 0, AUX_SC1);
  }
  SCHED_FENCE();
  TR(11);
  ISSUE_W(0, 2);
  ISSUE_W(6, 3);
  ISSUE_W(12, 4);
  TR(12);
  if (w == 0) {
    const int qo = WS_QX + (h * 4 + b) * 8 * 2048 + lane * 16;
    v4u ga[8], gb[8];
    for (unsigned spins = 0;;) {
#pragma unroll
      for (int p = 0; p < 8; ++p) { ga[p] = poll16(rws, qo + p * 2048); gb[p] = poll16(rws, qo + p * 2048 + 1024 + (lane < 32 ? 0 : OOB)); }
      bool ok = true;
#pragma unroll
      for (int p = 0; p < 8; ++p) ok = ok & (ga[p][1] == tagA) & (ga[p][3] == tagB) & ((lane >= 32) | ((gb[p][1] == tagA) & (gb[p][3] == tagB)));
      TR(17);
      if (__all(ok) || ++spins > SPIN_CAP) break;
      __builtin_amdgcn_s_sleep(1);
    }
    TR(16);
    float a0 = 0.f, a1 = 0.f, b0 = 0.f, b1 = 0.f;
#pragma unroll
    for (int p = 0; p < 8; ++p) { a0 += __uint_as_float(ga[p][0]); a1 += __uint_as_float(ga[p][2]); b0 += __uint_as_float(gb[p][0]); b1 += __uint_as_float(gb[p][2]); }
    *(LAS unsigned*)(L + L_QB + lane * 4) = cvtpk(a0, a1);
    if (lane < 32) *(LAS unsigned*)(L + L_QB + 256 + lane * 4) = cvtpk(b0, b1);
  }
  TR(13);
  ISSUE_W(18, 5);
  ISSUE_W(24, 6);
  __syncthreads();
  TR(14);
  STAMP(3);
  { const unsigned t = now32() - ((7 * TAU16) >> 4); if ((int)(t - T0) > 0) T0 = t; }
  v4u Q[3];
#pragma unroll
  for (int i = 0; i < 3; ++i) Q[i] = *(LAS v4u*)(L + L_QB + (c + 8 * i) * 16);

  float mx = -INFINITY;
  const bool oct0 = c == 0;
  const int scoff = L_SC + w * 2048 + k * 8;
  auto k_tile = [&](const v4u& r0, const v4u& r1, const v4u& r2) -> float {
    float a0 = dot2(r0[0], Q[0][0], 0.f), a1 = dot2(r1[0], Q[1][0], 0.f), a2 = dot2(r2[0], Q[2][0], 0.f);
#pragma unroll
    for (int d = 1; d < 4; ++d) { a0 = dot2(r0[d], Q[0][d], a0); a1 = dot2(r1[d], Q[1][d], a1); a2 = dot2(r2[d], Q[2][d], a2); }
    return (a0 + a1) + a2;
  };
  auto k_put = [&](float s0, float s1, int n) {
    s0 = oct_sum(s0); s1 = oct_sum(s1);
    mx = fmaxf(mx, fmaxf(s0, s1));
    if (oct0) *(LAS v2f*)(L + scoff + n * 64) = v2f{s0, s1};
  };
#define K_STEP(G, n, m) do { float s0_ = k_tile(G.r[0][0], G.r[0][1], G.r[0][2]), s1_ = k_tile(G.r[1][0], G.r[1][1], G.r[1][2]); \
    asm volatile("" : "+v"(s0_), "+v"(s1_) : : "memory"); ISSUE(G, m); k_put(s0_, s1_, n); } while (0)
  K_STEP(A, 0, 7);
#pragma unroll
  for (int g = 0; g < 5; ++g) k_put(k_tile(W[g * 6], W[g * 6 + 1], W[g * 6 + 2]), k_tile(W[g * 6 + 3], W[g * 6 + 4], W[g * 6 + 5]), 2 + g);
  STAMP(4);
  K_STEP(B, 1, 8);
  STAMP(5);
  {
    const int lane = fresh_lane(), tid2 = w * 64 + lane;
    const int yr = lane / 10, yc = lane - yr * 10;
    const int fxo = WS_FX + ((xg * 4 + b) * 8 + (j & 7)) * 6144 + tid2 * 16 + (tid2 < 384 ? 0 : OOB);
    v4u fx = poll16(rws, fxo);
    const __amdgpu_buffer_rsrc_t r2 = rsrc(w2, FF * DM * 2), rg = rsrc(gw, w == 0 ? DM * 2 : 0), rx0 = rsrc(x + b * DM, w == 0 ? DM * 2 : 0);
    const int vo2 = (j * 24 + yr) * (DM * 2) + w * 160 + yc * 16 + (lane < 60 ? 0 : OOB);
#pragma unroll
    for (int t = 0; t < 4; ++t) W[8 + t] = ld16<AUX_NT>(r2, vo2, t * 6 * (DM * 2));
    const v4u gx0 = ld16<AUX_NT>(rx0, lane * 16, 0), gx1 = ld16<AUX_NT>(rx0, 1024 + lane * 16, 0);
    const v4u gg0 = ld16<AUX_NT>(rg, lane * 16, 0), gg1 = ld16<AUX_NT>(rg, 1024 + lane * 16, 0);
    SCHED_FENCE();
    TR(30);
    K_STEP(A, 7, 9); K_STEP(B, 8, 10);
    TR(31);
    for (unsigned spins = 0; !__all((tid2 >= 384) | ((fx[1] == tagA) & (fx[3] == tagB))); ) {
      if (++spins > SPIN_CAP) break;
      __builtin_amdgcn_s_sleep(2);
      fx = poll16(rws, fxo);
    }
    TR(32);
    if (tid2 < 384) *(LAS v2f*)(L + L_G + ((tid2 / 12) * 24 + (tid2 % 12) * 2) * 4) = v2f{__uint_as_float(fx[0]), __uint_as_float(fx[2])};
    float gate = 0.f;
    if (w == 0) {
      float d = 0.f;
#pragma unroll
      for (int e = 0; e < 4; ++e) { d = dot2(gx0[e], gg0[e], d); d = dot2(gx1[e], gg1[e], d); }
      gate = 1.f / (1.f + __expf(-wave_sum(d)));
      if (lane == 0) *(LAS float*)(L + L_MISC) = gate;
    }
    __syncthreads();
    TR(33);
    if (tid2 < 24) {
      const LAS float* g = (const LAS float*)(L + L_G) + tid2;
      float ga = 0.f, up = 0.f;
#pragma unroll
      for (int r = 0; r < 16; ++r) { ga += g[r * 24]; up += g[(16 + r) * 24]; }
      *(LAS float*)(L + L_MISC + 64 + tid2 * 4) = ga / (1.f + __expf(-ga)) * up * (1.f - gate);
    }
    __syncthreads();
    TR(34);
    v2f ya[4];
#pragma unroll
    for (int e = 0; e < 4; ++e) ya[e] = v2f{0.f, 0.f};
#pragma unroll
    for (int t = 0; t < 4; ++t) {
      const float hv = *(LAS float*)(L + L_MISC + 64 + (t * 6 + (yr < 6 ? yr : 0)) * 4);
#pragma unroll
      for (int d = 0; d < 4; ++d) ya[d] = __builtin_elementwise_fma(v2f{hv, hv}, v2f{bflo(W[8 + t][d]), bfhi(W[8 + t][d])}, ya[d]);
    }
    if (lane < 60) {
      LAS v4f* p = (LAS v4f*)(L + L_YF + (w * 480 + yr * 80 + yc * 8) * 4);
      p[0] = v4f{ya[0][0], ya[0][1], ya[1][0], ya[1][1]}; p[1] = v4f{ya[2][0], ya[2][1], ya[3][0], ya[3][1]};
    }
    wo_vo = yr * (DM * 2) + w * 160 + yc * 16 + (lane < 60 ? 0 : OOB);
  }
  MEM_FENCE();
  TR(35);
  K_STEP(A, 9, 11); K_STEP(B, 10, 12);
  STAMP(13);
#define WO_COPY(t0) do { asm volatile("s_waitcnt vmcnt(12)" ::: "memory"); const int ln_ = fresh_lane(); \
    _Pragma("unroll") for (int t = 0; t < 8; ++t) W[t0 + t] = *(LAS v4u*)(L + L_FP + w * 8192 + t * 1024 + ln_ * 16); \
    asm volatile("" : "+v"(W[t0]), "+v"(W[t0 + 1]), "+v"(W[t0 + 2]), "+v"(W[t0 + 3]), "+v"(W[t0 + 4]), "+v"(W[t0 + 5]), "+v"(W[t0 + 6]), "+v"(W[t0 + 7]) : : "memory"); } while (0)
  int n;
#pragma unroll 1
  for (n = 11; n < 21; n += 2) {
    wo_t = n < 19 ? n - 11 : -1; K_STEP(A, n, n + 2);
    wo_t = n < 19 ? n - 10 : -1; K_STEP(B, n + 1, n + 3);
  }
  WO_COPY(0);
#pragma unroll 1
  for (; n < 31; n += 2) {
    wo_t = n < 29 ? n - 13 : -1; K_STEP(A, n, n + 2);
    wo_t = n < 29 ? n - 12 : -1; K_STEP(B, n + 1, n + 3);
  }
  WO_COPY(8);
  wo_t = -1;
  K_STEP(A, 31, 33);

  STAMP(6);
  mx = fmaxf(mx, dpp<XOR8>(mx)); mx = max16_32(mx);
  const float mw = mx * CS;
  LDS_FENCE();
  {
    const int lane = fresh_lane();
    LAS v4f* p = (LAS v4f*)(L + L_SC + w * 2048 + lane * 32);
    v4f a = p[0], bb = p[1];
    float l = 0.f;
#pragma unroll
    for (int e = 0; e < 4; ++e) { a[e] = ex2(fmaf(a[e], CS, -mw)); bb[e] = ex2(fmaf(bb[e], CS, -mw)); l += a[e] + bb[e]; }
    p[0] = a; p[1] = bb;
    l = wave_sum(l);
    if (lane == 0) __builtin_amdgcn_raw_buffer_store_b128(v4u{__float_as_uint(mw), tagA, __float_as_uint(l), tagB}, rws, WS_ML + (((b * NH + h) * 2 + s) * 8 + w) * 16, 0, AUX_SC1);
  }
  LDS_FENCE();
  STAMP(11);

  v2f acc2[12];
#pragma unroll
  for (int e = 0; e < 12; ++e) acc2[e] = v2f{0.f, 0.f};
#define PIN_ACC() asm volatile("" : "+v"(acc2[0]), "+v"(acc2[1]), "+v"(acc2[2]), "+v"(acc2[3]), "+v"(acc2[4]), "+v"(acc2[5]), "+v"(acc2[6]), "+v"(acc2[7]), \
                                    "+v"(acc2[8]), "+v"(acc2[9]), "+v"(acc2[10]), "+v"(acc2[11]) : : "memory")
  auto v_tile = [&](const v4u& r0, const v4u& r1, const v4u& r2, float p) {
    const v2f pp = v2f{p, p};
#pragma unroll
    for (int d = 0; d < 4; ++d) {
      acc2[d] = __builtin_elementwise_fma(pp, v2f{bflo(r0[d]), bfhi(r0[d])}, acc2[d]);
      acc2[4 + d] = __builtin_elementwise_fma(pp, v2f{bflo(r1[d]), bfhi(r1[d])}, acc2[4 + d]);
      acc2[8 + d] = __builtin_elementwise_fma(pp, v2f{bflo(r2[d]), bfhi(r2[d])}, acc2[8 + d]);
    }
    PIN_ACC();
  };
#define V_USE(G, n) do { const v2f p2_ = *(LAS v2f*)(L + scoff + (n) * 64); v_tile(G.r[0][0], G.r[0][1], G.r[0][2], p2_[0]); v_tile(G.r[1][0], G.r[1][1], G.r[1][2], p2_[1]); } while (0)
#pragma unroll 1
  for (n = 0; n < 10; n += 2) {
    V_USE(B, n); wo_t = n < 8 ? n + 16 : -1; ISSUE(B, NGW + n + 2);
    V_USE(A, n + 1); wo_t = n < 8 ? n + 17 : -1; ISSUE(A, NGW + n + 3);
  }
  WO_COPY(16);
#pragma unroll 1
  for (; n < 20; n += 2) {
    V_USE(B, n); wo_t = n < 18 ? n + 14 : -1; ISSUE(B, NGW + n + 2);
    V_USE(A, n + 1); wo_t = n < 18 ? n + 15 : -1; ISSUE(A, NGW + n + 3);
  }
  WO_COPY(24);
  wo_t = -1;
#pragma unroll 1
  for (; n < NGW - 2; n += 2) { V_USE(B, n); ISSUE(B, NGW + n + 2); V_USE(A, n + 1); ISSUE(A, NGW + n + 3); }
  const int mloff = WS_ML + ((b * NH + h) * 16 + (fresh_lane() & 15)) * 16;
  v4u ml = poll16(rws, mloff);
  SCHED_FENCE();
#define PAIR_WO(u0, u1) do { _Pragma("unroll") for (int u = u0; u < u1; ++u) { \
      _Pragma("unroll") for (int d = 0; d < 4; ++d) { const unsigned a = W[2 * u][d], bb = W[2 * u + 1][d]; \
        W[2 * u][d] = __builtin_amdgcn_perm(bb, a, 0x05040100u); W[2 * u + 1][d] = __builtin_amdgcn_perm(bb, a, 0x07060302u); } \
      asm volatile("" : "+v"(W[2 * u]), "+v"(W[2 * u + 1])); SCHED_FENCE(); } } while (0)
  PAIR_WO(0, 8);
  V_USE(B, NGW - 2);
  SCHED_FENCE();
  PAIR_WO(8, 16);
  V_USE(A, NGW - 1);

  STAMP(7);
  for (unsigned spins = 0; !__all((ml[1] == tagA) & (ml[3] == tagB)); ) {
    if (++spins > SPIN_CAP) break;
    __builtin_amdgcn_s_sleep(2);
    ml = poll16(rws, mloff);
  }
  {
    const float m_i = __uint_as_float(ml[0]), l_i = __uint_as_float(ml[2]);
    float M = m_i;
    M = fmaxf(M, dpp<XOR1>(M)); M = fmaxf(M, dpp<XOR2>(M)); M = fmaxf(M, dpp<XOR7>(M)); M = fmaxf(M, dpp<XOR8>(M));
    float Ls = l_i * ex2(m_i - M);
    Ls = oct_sum(Ls); Ls += dpp<XOR8>(Ls);
    const float gate = *(LAS float*)(L + L_MISC);
    const float f = ex2(mw - M) / Ls * gate;
    float acc[24];
#pragma unroll
    for (int e = 0; e < 24; ++e) acc[e] = xoct_sum(acc2[e >> 1][e & 1]) * f;
    const int c = fresh_lane();
    if (c < 8) {
#pragma unroll
      for (int i = 0; i < 3; ++i) {
        LAS v4f* p = (LAS v4f*)(L + L_QP + (w * 192 + i * 64 + c * 8) * 4);
        p[0] = v4f{acc[i * 8], acc[i * 8 + 1], acc[i * 8 + 2], acc[i * 8 + 3]}; p[1] = v4f{acc[i * 8 + 4], acc[i * 8 + 5], acc[i * 8 + 6], acc[i * 8 + 7]};
      }
    }
  }
  STAMP(8);
  __syncthreads();
  const int tid2 = w * 64 + fresh_lane();
  if (tid2 < 96) {
    const int r = tid2 >> 4, u = tid2 & 15;
    float a0 = 0.f, a1 = 0.f;
#pragma unroll
    for (int ww = 0; ww < 8; ++ww) { const LAS float* p = (const LAS float*)(L + L_QP) + ww * 192 + u * 12 + r; a0 += p[0]; a1 += p[6]; }
    *(LAS unsigned*)(L + L_OB + tid2 * 4) = cvtpk(a0, a1);
  }
  __syncthreads();
  {
    const int ln = fresh_lane();
    const int yr = ln < 60 ? ln / 10 : 0, yc = ln - (ln / 10) * 10;
    LAS float* yrp = (LAS float*)(L + L_FP) + w * 480;
    float ya[8];
    { const LAS v4f* p = (const LAS v4f*)(L + L_YF + (w * 480 + yr * 80 + yc * 8) * 4); const v4f a = p[0], bb = p[1];
#pragma unroll
      for (int e = 0; e < 4; ++e) { ya[e] = a[e]; ya[4 + e] = bb[e]; } }
    const LAS v4u* op = (const LAS v4u*)(L + L_OB + yr * 64);
#pragma unroll
    for (int u4 = 0; u4 < 4; ++u4) {
      const v4u o4 = op[u4];
#pragma unroll
      for (int e = 0; e < 4; ++e) {
        const int u = u4 * 4 + e;
#pragma unroll
        for (int d = 0; d < 4; ++d) { ya[2 * d] = dot2(W[2 * u][d], o4[e], ya[2 * d]); ya[2 * d + 1] = dot2(W[2 * u + 1][d], o4[e], ya[2 * d + 1]); }
      }
    }
    if (ln < 60) { LAS v4f* p = (LAS v4f*)(yrp + yr * 80 + yc * 8); p[0] = v4f{ya[0], ya[1], ya[2], ya[3]}; p[1] = v4f{ya[4], ya[5], ya[6], ya[7]}; }
    LDS_FENCE();
    if (ln < 40) {
      v2f y = v2f{0.f, 0.f};
#pragma unroll
      for (int r = 0; r < 6; ++r) y += *(LAS v2f*)(yrp + r * 80 + ln * 2);
      const int cons = w * 8 + ln / 5, g5 = ln % 5;
      __builtin_amdgcn_raw_buffer_store_b128(v4u{__float_as_uint(y[0]), tagA, __float_as_uint(y[1]), tagB}, rws, WS_INBOX + (((b * 64 + cons) * 64 + j) * 5 + g5) * 16, 0, AUX_SC1);
    }
  }
  STAMP(9);
  if (w == 0) {
    const int lane = fresh_lane();
    const int io = WS_INBOX + ((b * 64 + j) * 64 + lane) * 80;
    v4u g5[5];
    for (unsigned spins = 0;;) {
      bool ok = true;
#pragma unroll
      for (int e = 0; e < 5; ++e) g5[e] = poll16(rws, io + e * 16);
#pragma unroll
      for (int e = 0; e < 5; ++e) ok = ok & (g5[e][1] == tagA) & (g5[e][3] == tagB);
      if (__all(ok) || ++spins > SPIN_CAP) break;
      __builtin_amdgcn_s_sleep(2);
    }
    float y[10];
#pragma unroll
    for (int e = 0; e < 5; ++e) { y[2 * e] = wave_sum(__uint_as_float(g5[e][0])); y[2 * e + 1] = wave_sum(__uint_as_float(g5[e][2])); }
    unsigned o = cvtpk(y[0], y[1]);
#pragma unroll
    for (int e = 1; e < 5; ++e) { const unsigned v = cvtpk(y[2 * e], y[2 * e + 1]); o = (lane == e) ? v : o; }
    if (lane < 5) *(GAS unsigned*)(out + b * DM + j * 10 + lane * 2) = o;
    if (b == 0 && j == 0 && lane == 0) __hip_atomic_store((GAS u64*)(ws + WS_EPOCH), ((u64)tagB << 32) | tagA, __ATOMIC_RELAXED, __HIP_MEMORY_SCOPE_AGENT);
  }
}
}

extern "C" void kernel_launch(void* const* d_in, const int* in_sizes, int n_in, void* d_out, int out_size, void* d_ws, size_t ws_size,
                              hipStream_t stream) {
  (void)in_sizes; (void)n_in; (void)out_size; (void)ws_size;
  hipLaunchKernelGGL(fused_decode, dim3(NWG), dim3(NTHR), 0, stream, (const u16*)d_in[0], (const u16*)d_in[1], (const u16*)d_in[2], (const u16*)d_in[3],
                     (const u16*)d_in[4], (const u16*)d_in[6], (const u16*)d_in[7], (const u16*)d_in[8], (const u16*)d_in[9], (u16*)d_out, (unsigned char*)d_ws);
}
```

```cpp
#include <hip/hip_runtime.h>
#include <hip/hip_bf16.h>
#include <stdint.h>

#define GAS __attribute__((address_space(1)))
#define LAS __attribute__((address_space(3)))
typedef unsigned v4u __attribute__((ext_vector_type(4)));
typedef unsigned v2u __attribute__((ext_vector_type(2)));
typedef float v4f __attribute__((ext_vector_type(4)));
typedef float v2f __attribute__((ext_vector_type(2)));
typedef __bf16 bf16x2 __attribute__((ext_vector_type(2)));
typedef unsigned long long u64;
typedef unsigned short u16;

namespace {
constexpr int NB = 4, NH = 32, SK = 8192, DH = 192, DM = 640, FF = 1536;
constexpr int NWG = 256, NWAVE = 8, NTHR = NWAVE * 64;
constexpr int KEYS_WG = SK / 2;
constexpr int ROWB = DH * 2;
constexpr int TILEB = 8 * ROWB;
constexpr int GRPB = 2 * TILEB;
constexpr int KVB = KEYS_WG * ROWB;
constexpr int NGW = KVB / GRPB / NWAVE;
constexpr int RSRC3 = 0x00020000;
#define POLL_AUX 16
#define STORE_AUX 16
constexpr int AUX_NT = 2, AUX_SC1 = STORE_AUX;
constexpr int OOB = 1 << 28;
constexpr float CS = 0.07216878364870322f * 1.4426950408889634f;
#define TAU16 (180 * 16)
#define PHW 0

constexpr int WS_EPOCH = 0;
constexpr int WS_ML = 4096;
constexpr int WS_INBOX = 65536;
constexpr int WS_QX = WS_INBOX + NB * 64 * 64 * 80;
constexpr int WS_FX = WS_QX + NH * NB * 8 * 128 * 16;
constexpr int WS_BYTES = WS_FX + 8 * NB * 8 * 384 * 16;

constexpr int L_XS = 0;
constexpr int L_QP = 1280;
constexpr int L_QB = L_QP + 8 * 192 * 4;
constexpr int L_OB = L_QB + 384;
constexpr int L_MISC = L_OB + 384;
constexpr int L_G = L_MISC + 256;
constexpr int L_YF = L_G + 3072;
constexpr int L_SC = L_YF + 8 * 480 * 4;
constexpr int L_FP2 = L_YF;
constexpr int L_FP = L_YF + 22 * 4 * 192 * 4;
constexpr int L_TR = L_FP + 22 * 4 * 192 * 4;
constexpr int L_BYTES = L_TR;

constexpr unsigned SPIN_CAP = 1u << 17;

__device__ __forceinline__ float dot2(unsigned a, unsigned b, float c) {
  return __builtin_amdgcn_fdot2_f32_bf16(__builtin_bit_cast(bf16x2, a), __builtin_bit_cast(bf16x2, b), c, false);
}
template <int CTRL> __device__ __forceinline__ float dpp(float x) {
  return __builtin_bit_cast(float, __builtin_amdgcn_mov_dpp(__builtin_bit_cast(int, x), CTRL, 0xf, 0xf, true));
}
constexpr int XOR1 = 0xB1, XOR2 = 0x4E, XOR7 = 0x141, XOR8 = 0x128;
__device__ __forceinline__ float sum16_32(float x) {
  auto s = __builtin_amdgcn_permlane16_swap(__float_as_uint(x), __float_as_uint(x), false, false);
  x = __uint_as_float(s[0]) + __uint_as_float(s[1]);
  auto t = __builtin_amdgcn_permlane32_swap(__float_as_uint(x), __float_as_uint(x), false, false);
  return __uint_as_float(t[0]) + __uint_as_float(t[1]);
}
__device__ __forceinline__ float max16_32(float x) {
  auto s = __builtin_amdgcn_permlane16_swap(__float_as_uint(x), __float_as_uint(x), false, false);
  x = fmaxf(__uint_as_float(s[0]), __uint_as_float(s[1]));
  auto t = __builtin_amdgcn_permlane32_swap(__float_as_uint(x), __float_as_uint(x), false, false);
  return fmaxf(__uint_as_float(t[0]), __uint_as_float(t[1]));
}
__device__ __forceinline__ float oct_sum(float x) { x += dpp<XOR1>(x); x += dpp<XOR2>(x); x += dpp<XOR7>(x); return x; }
__device__ __forceinline__ float xoct_sum(float x) { x += dpp<XOR8>(x); return sum16_32(x); }
__device__ __forceinline__ float wave_sum(float x) { return xoct_sum(oct_sum(x)); }
__device__ __forceinline__ unsigned cvtpk(float lo, float hi) { unsigned r; asm("v_cvt_pk_bf16_f32 %0, %1, %2" : "=v"(r) : "v"(lo), "v"(hi)); return r; }
__device__ __forceinline__ float bflo(unsigned u) { return __uint_as_float(u << 16); }
__device__ __forceinline__ float bfhi(unsigned u) { return __uint_as_float(u & 0xffff0000u); }
__device__ __forceinline__ float ex2(float x) { return __builtin_amdgcn_exp2f(x); }
__device__ __forceinline__ __amdgpu_buffer_rsrc_t rsrc(const void* p, int bytes) { return __builtin_amdgcn_make_buffer_rsrc((void*)p, (short)0, bytes, RSRC3); }
template <int AUX> __device__ __forceinline__ v4u ld16(__amdgpu_buffer_rsrc_t r, int voff, int soff) { return __builtin_amdgcn_raw_buffer_load_b128(r, voff, soff, AUX); }
template <int AUX> __device__ __forceinline__ v2u ld8(__amdgpu_buffer_rsrc_t r, int voff, int soff) { return __builtin_amdgcn_raw_buffer_load_b64(r, voff, soff, AUX); }
__device__ __forceinline__ unsigned now32() { return (unsigned)__builtin_amdgcn_s_memrealtime(); }
#define STAMP(i) do { } while (0)
#define TR(id) do { } while (0)
#define SCHED_FENCE() __builtin_amdgcn_sched_barrier(0)
#define LDS_FENCE() asm volatile("s_waitcnt lgkmcnt(0)" ::: "memory")
#define MEM_FENCE() asm volatile("" ::: "memory")
__device__ __forceinline__ v4u poll16(__amdgpu_buffer_rsrc_t r, int voff) { asm volatile("" : "+v"(voff) : : "memory"); return __builtin_amdgcn_raw_buffer_load_b128(r, voff, 0, POLL_AUX); }
__device__ __forceinline__ int fresh_lane() { int l; asm volatile("v_mbcnt_lo_u32_b32 %0, -1, 0\n\tv_mbcnt_hi_u32_b32 %0, -1, %0" : "=v"(l)); return l; }

__device__ __forceinline__ void dma16(__amdgpu_buffer_rsrc_t r, int voff, int soff, unsigned lds) {
  unsigned keep;
  asm volatile("s_mov_b32 %0, m0\n\ts_mov_b32 m0, %3\n\ts_nop 0\n\tbuffer_load_dwordx4 %1, %2, %4 offen nt lds\n\ts_mov_b32 m0, %0"
               : "=&s"(keep) : "v"(voff), "s"(r), "s"(lds), "s"(soff) : "memory");
}
struct Grp { v4u r[2][3]; };

__global__ void __launch_bounds__(NTHR, 2)
fused_decode(const u16* __restrict__ x, const u16* __restrict__ Kc, const u16* __restrict__ Vc, const u16* __restrict__ wq,
             const u16* __restrict__ wo, const u16* __restrict__ w1, const u16* __restrict__ w3, const u16* __restrict__ w2,
             const u16* __restrict__ gw, u16* __restrict__ out, unsigned char* ws) {
  __shared__ __attribute__((aligned(16))) unsigned char lds_raw[L_BYTES];
  LAS unsigned char* L = (LAS unsigned char*)lds_raw;
  const int tid = threadIdx.x, lane = tid & 63, w = __builtin_amdgcn_readfirstlane(tid >> 6);
  const int k = lane >> 3, c = lane & 7;
  const int blk = blockIdx.x, xg = blk & 7, idx = blk >> 3;
  const int h = xg * 4 + (idx & 3), sh = idx >> 2, b = sh >> 1, s = sh & 1;
  const int j = h * 2 + s;
  STAMP(0);
#define PHASE 0
#define T0OFF 80
  unsigned T0 = now32() + T0OFF + idx * PHASE * (TAU16 / 32) / (16 * 32);
  const __amdgpu_buffer_rsrc_t rws = rsrc(ws, WS_BYTES);
  const u64 epoch = __hip_atomic_load((GAS u64*)(ws + WS_EPOCH), __ATOMIC_RELAXED, __HIP_MEMORY_SCOPE_AGENT);
  const int qr = tid / 24, qc = tid - qr * 24;
  v2u xq[4]; v4u wqv[4], fwa, fwb; unsigned fx4[4];
  {
    const __amdgpu_buffer_rsrc_t rx = rsrc(x, NB * DM * 2);
    const __amdgpu_buffer_rsrc_t rq = rsrc(wq + h * DH, (DM * NH * DH - h * DH) * 2);
    const int oob = tid < 480 ? 0 : OOB;
#pragma unroll
    for (int e = 0; e < 4; ++e) wqv[e] = ld16<AUX_NT>(rq, (sh * 80 + qr * 4) * (NH * DH * 2) + qc * 16 + oob, e * (NH * DH * 2));
#pragma unroll
    for (int e = 0; e < 4; ++e) xq[e] = ld8<AUX_NT>(rx, (sh * 80 + qr * 4) * 2 + e * (DM * 2) + oob, 0);
    const int row0 = (idx >> 1) * 40 + qr * 2;
    const __amdgpu_buffer_rsrc_t r13 = rsrc(((idx & 1) ? w3 : w1) + xg * 192, (DM * FF - xg * 192) * 2);
    fwa = ld16<AUX_NT>(r13, row0 * (FF * 2) + qc * 16 + oob, 0); fwb = ld16<AUX_NT>(r13, row0 * (FF * 2) + qc * 16 + oob, FF * 2);
#pragma unroll
    for (int e = 0; e < 4; ++e) fx4[e] = __builtin_amdgcn_raw_buffer_load_b32(rx, row0 * 2 + e * (DM * 2) + oob, 0, AUX_NT);
  }
  const size_t kvoff = ((size_t)(b * NH + h) * SK + (size_t)s * KEYS_WG) * DH;
  const u16* Kb = Kc + kvoff; const u16* Vb = Vc + kvoff;
  const int kvo = w * GRPB + k * ROWB + c * 16;
  auto pace = [&](int m) { const unsigned tt = T0 + ((unsigned)(m * TAU16) >> 4); while ((int)(now32() - tt) < 0) __builtin_amdgcn_s_sleep(1); };
  int wo_t = -1;
  __amdgpu_buffer_rsrc_t ro = rsrc(wo + (size_t)h * DH * DM, DH * DM * 2);
  int wo_vo = 0;
  auto issue = [&](v4u& r00, v4u& r01, v4u& r02, v4u& r10, v4u& r11, v4u& r12, int m) {
    TR(1);
    pace(m);
    TR(2);
    if (wo_t >= 0) dma16(ro, wo_vo, wo_t * 6 * (DM * 2), (unsigned)(L_FP + w * 8192 + (wo_t & 7) * 1024));
    const __amdgpu_buffer_rsrc_t r = rsrc(m < NGW ? Kb : Vb, KVB);
    const int so = (m & (NGW - 1)) * (NWAVE * GRPB);
    r00 = ld16<AUX_NT>(r, kvo, so); r01 = ld16<AUX_NT>(r, kvo + 128, so); r02 = ld16<AUX_NT>(r, kvo + 256, so);
    r10 = ld16<AUX_NT>(r, kvo, so + TILEB); r11 = ld16<AUX_NT>(r, kvo + 128, so + TILEB); r12 = ld16<AUX_NT>(r, kvo + 256, so + TILEB);
    SCHED_FENCE();
    TR(3);
  };
#define ISSUE(G, m) issue(G.r[0][0], G.r[0][1], G.r[0][2], G.r[1][0], G.r[1][1], G.r[1][2], m)
#define ISSUE_W(o, m) issue(W[o], W[o + 1], W[o + 2], W[o + 3], W[o + 4], W[o + 5], m)
  Grp A, B;
  v4u W[32];
  ISSUE(A, 0);
  ISSUE(B, 1);
  STAMP(1);
  const unsigned tagA = (unsigned)epoch + 1u, tagB = (unsigned)(epoch >> 32) + 1u;
  {
    unsigned lo01[4], hi01[4], lo23[4], hi23[4];
#pragma unroll
    for (int d = 0; d < 4; ++d) {
      lo01[d] = __builtin_amdgcn_perm(wqv[1][d], wqv[0][d], 0x05040100u); hi01[d] = __builtin_amdgcn_perm(wqv[1][d], wqv[0][d], 0x07060302u);
      lo23[d] = __builtin_amdgcn_perm(wqv[3][d], wqv[2][d], 0x05040100u); hi23[d] = __builtin_amdgcn_perm(wqv[3][d], wqv[2][d], 0x07060302u);
    }
#pragma unroll
    for (int bb = 0; bb < 4; ++bb) {
      float a[8];
#pragma unroll
      for (int d = 0; d < 4; ++d) { a[2 * d] = dot2(lo23[d], xq[bb][1], dot2(lo01[d], xq[bb][0], 0.f)); a[2 * d + 1] = dot2(hi23[d], xq[bb][1], dot2(hi01[d], xq[bb][0], 0.f)); }
      LAS v4f* p = (LAS v4f*)(L + L_FP + ((qr * 4 + bb) * 192 + qc * 8) * 4);
      p[0] = v4f{a[0], a[1], a[2], a[3]}; p[1] = v4f{a[4], a[5], a[6], a[7]};
    }
  }
  {
    unsigned lo[4], hi[4];
#pragma unroll
    for (int d = 0; d < 4; ++d) { lo[d] = __builtin_amdgcn_perm(fwb[d], fwa[d], 0x05040100u); hi[d] = __builtin_amdgcn_perm(fwb[d], fwa[d], 0x07060302u); }
#pragma unroll
    for (int bb = 0; bb < 4; ++bb) {
      LAS v4f* p = (LAS v4f*)(L + L_FP2 + ((qr * 4 + bb) * 192 + qc * 8) * 4);
      p[0] = v4f{dot2(lo[0], fx4[bb], 0.f), dot2(hi[0], fx4[bb], 0.f), dot2(lo[1], fx4[bb], 0.f), dot2(hi[1], fx4[bb], 0.f)};
      p[1] = v4f{dot2(lo[2], fx4[bb], 0.f), dot2(hi[2], fx4[bb], 0.f), dot2(lo[3], fx4[bb], 0.f), dot2(hi[3], fx4[bb], 0.f)};
    }
  }
  TR(9);
  __syncthreads();
  TR(10);
  STAMP(2);
  if (tid < 384) {
    const int bb = tid / 96, p = tid - bb * 96;
    float a0 = 0.f, a1 = 0.f;
#pragma unroll
    for (int r = 0; r < 20; ++r) { const v2f v = *(LAS v2f*)(L + L_FP + ((r * 4 + bb) * 192 + p * 2) * 4); a0 += v[0]; a1 += v[1]; }
    __builtin_amdgcn_raw_buffer_store_b128(v4u{__float_as_uint(a0), tagA, __float_as_uint(a1), tagB}, rws, WS_QX + (((h * 4 + bb) * 8 + sh) * 128 + p) * 16, 0, AUX_SC1);
    a0 = 0.f; a1 = 0.f;
#pragma unroll
    for (int r = 0; r < 20; ++r) { const v2f v = *(LAS v2f*)(L + L_FP2 + ((r * 4 + bb) * 192 + p * 2) * 4); a0 += v[0]; a1 += v[1]; }
    __builtin_amdgcn_raw_buffer_store_b128(v4u{__float_as_uint(a0), tagA, __float_as_uint(a1), tagB}, rws,
                                           WS_FX + (((((xg * 4 + bb) * 8 + p / 12) * 2 + (idx & 1)) * 16 + (idx >> 1)) * 12 + p % 12) * 16, 0, AUX_SC1);
  }
  SCHED_FENCE();
  TR(11);
  ISSUE_W(0, 2);
  ISSUE_W(6, 3);
  ISSUE_W(12, 4);
  TR(12);
  if (w == 0) {
    const int qo = WS_QX + (h * 4 + b) * 8 * 2048 + lane * 16;
    v4u ga[8], gb[8];
    for (unsigned spins = 0;;) {
#pragma unroll
      for (int p = 0; p < 8; ++p) { ga[p] = poll16(rws, qo + p * 2048); gb[p] = poll16(rws, qo + p * 2048 + 1024 + (lane < 32 ? 0 : OOB)); }
      bool ok = true;
#pragma unroll
      for (int p = 0; p < 8; ++p) ok = ok & (ga[p][1] == tagA) & (ga[p][3] == tagB) & ((lane >= 32) | ((gb[p][1] == tagA) & (gb[p][3] == tagB)));
      TR(17);
      if (__all(ok) || ++spins > SPIN_CAP) break;
      __builtin_amdgcn_s_sleep(1);
    }
    TR(16);
    float a0 = 0.f, a1 = 0.f, b0 = 0.f, b1 = 0.f;
#pragma unroll
    for (int p = 0; p < 8; ++p) { a0 += __uint_as_float(ga[p][0]); a1 += __uint_as_float(ga[p][2]); b0 += __uint_as_float(gb[p][0]); b1 += __uint_as_float(gb[p][2]); }
    *(LAS unsigned*)(L + L_QB + lane * 4) = cvtpk(a0, a1);
    if (lane < 32) *(LAS unsigned*)(L + L_QB + 256 + lane * 4) = cvtpk(b0, b1);
  }
  TR(13);
  ISSUE_W(18, 5);
  ISSUE_W(24, 6);
  __syncthreads();
  TR(14);
  STAMP(3);
#define SLACK 3
  { const unsigned t = now32() - (((7 + SLACK) * TAU16) >> 4); if ((int)(t - T0) > 0) T0 = t; }
  v4u Q[3];
#pragma unroll
  for (int i = 0; i < 3; ++i) Q[i] = *(LAS v4u*)(L + L_QB + (c + 8 * i) * 16);

  float mx = -INFINITY;
  const bool oct0 = c == 0;
  const int scoff = L_SC + w * 2048 + k * 8;
  auto k_tile = [&](const v4u& r0, const v4u& r1, const v4u& r2) -> float {
    float a0 = dot2(r0[0], Q[0][0], 0.f), a1 = dot2(r1[0], Q[1][0], 0.f), a2 = dot2(r2[0], Q[2][0], 0.f);
#pragma unroll
    for (int d = 1; d < 4; ++d) { a0 = dot2(r0[d], Q[0][d], a0); a1 = dot2(r1[d], Q[1][d], a1); a2 = dot2(r2[d], Q[2][d], a2); }
    return (a0 + a1) + a2;
  };
  auto k_put = [&](float s0, float s1, int n) {
    s0 = oct_sum(s0); s1 = oct_sum(s1);
    mx = fmaxf(mx, fmaxf(s0, s1));
    if (oct0) *(LAS v2f*)(L + scoff + n * 64) = v2f{s0, s1};
  };
#define K_STEP(G, n, m) do { float s0_ = k_tile(G.r[0][0], G.r[0][1], G.r[0][2]), s1_ = k_tile(G.r[1][0], G.r[1][1], G.r[1][2]); \
    asm volatile("" : "+v"(s0_), "+v"(s1_) : : "memory"); ISSUE(G, m); k_put(s0_, s1_, n); } while (0)
  K_STEP(A, 0, 7);
#pragma unroll
  for (int g = 0; g < 5; ++g) k_put(k_tile(W[g * 6], W[g * 6 + 1], W[g * 6 + 2]), k_tile(W[g * 6 + 3], W[g * 6 + 4], W[g * 6 + 5]), 2 + g);
  STAMP(4);
  K_STEP(B, 1, 8);
  STAMP(5);
  {
    const int lane = fresh_lane(), tid2 = w * 64 + lane;
    const int yr = lane / 10, yc = lane - yr * 10;
    const int fxo = WS_FX + ((xg * 4 + b) * 8 + (j & 7)) * 6144 + tid2 * 16 + (tid2 < 384 ? 0 : OOB);
    v4u fx = poll16(rws, fxo);
    const __amdgpu_buffer_rsrc_t r2 = rsrc(w2, FF * DM * 2), rg = rsrc(gw, w == 0 ? DM * 2 : 0), rx0 = rsrc(x + b * DM, w == 0 ? DM * 2 : 0);
    const int vo2 = (j * 24 + yr) * (DM * 2) + w * 160 + yc * 16 + (lane < 60 ? 0 : OOB);
#pragma unroll
    for (int t = 0; t < 4; ++t) W[8 + t] = ld16<AUX_NT>(r2, vo2, t * 6 * (DM * 2));
    const v4u gx0 = ld16<AUX_NT>(rx0, lane * 16, 0), gx1 = ld16<AUX_NT>(rx0, 1024 + lane * 16, 0);
    const v4u gg0 = ld16<AUX_NT>(rg, lane * 16, 0), gg1 = ld16<AUX_NT>(rg, 1024 + lane * 16, 0);
    SCHED_FENCE();
    TR(30);
    K_STEP(A, 7, 9); K_STEP(B, 8, 10);
    TR(31);
    for (unsigned spins = 0; !__all((tid2 >= 384) | ((fx[1] == tagA) & (fx[3] == tagB))); ) {
      if (++spins > SPIN_CAP) break;
      __builtin_amdgcn_s_sleep(2);
      fx = poll16(rws, fxo);
    }
    TR(32);
    if (tid2 < 384) *(LAS v2f*)(L + L_G + ((tid2 / 12) * 24 + (tid2 % 12) * 2) * 4) = v2f{__uint_as_float(fx[0]), __uint_as_float(fx[2])};
    float gate = 0.f;
    if (w == 0) {
      float d = 0.f;
#pragma unroll
      for (int e = 0; e < 4; ++e) { d = dot2(gx0[e], gg0[e], d); d = dot2(gx1[e], gg1[e], d); }
      gate = 1.f / (1.f + __expf(-wave_sum(d)));
      if (lane == 0) *(LAS float*)(L + L_MISC) = gate;
    }
    __syncthreads();
    TR(33);
    if (tid2 < 24) {
      const LAS float* g = (const LAS float*)(L + L_G) + tid2;
      float ga = 0.f, up = 0.f;
#pragma unroll
      for (int r = 0; r < 16; ++r) { ga += g[r * 24]; up += g[(16 + r) * 24]; }
      *(LAS float*)(L + L_MISC + 64 + tid2 * 4) = ga / (1.f + __expf(-ga)) * up * (1.f - gate);
    }
    __syncthreads();
    TR(34);
    v2f ya[4];
#pragma unroll
    for (int e = 0; e < 4; ++e) ya[e] = v2f{0.f, 0.f};
#pragma unroll
    for (int t = 0; t < 4; ++t) {
      const float hv = *(LAS float*)(L + L_MISC + 64 + (t * 6 + (yr < 6 ? yr : 0)) * 4);
#pragma unroll
      for (int d = 0; d < 4; ++d) ya[d] = __builtin_elementwise_fma(v2f{hv, hv}, v2f{bflo(W[8 + t][d]), bfhi(W[8 + t][d])}, ya[d]);
    }
    if (lane < 60) {
      LAS v4f* p = (LAS v4f*)(L + L_YF + (w * 480 + yr * 80 + yc * 8) * 4);
      p[0] = v4f{ya[0][0], ya[0][1], ya[1][0], ya[1][1]}; p[1] = v4f{ya[2][0], ya[2][1], ya[3][0], ya[3][1]};
    }
    wo_vo = yr * (DM * 2) + w * 160 + yc * 16 + (lane < 60 ? 0 : OOB);
  }
  MEM_FENCE();
  TR(35);
  K_STEP(A, 9, 11); K_STEP(B, 10, 12);
  STAMP(13);
#define WO_COPY(t0) do { asm volatile("s_waitcnt vmcnt(12)" ::: "memory"); const int ln_ = fresh_lane(); \
    _Pragma("unroll") for (int t = 0; t < 8; ++t) W[t0 + t] = *(LAS v4u*)(L + L_FP + w * 8192 + t * 1024 + ln_ * 16); \
    asm volatile("" : "+v"(W[t0]), "+v"(W[t0 + 1]), "+v"(W[t0 + 2]), "+v"(W[t0 + 3]), "+v"(W[t0 + 4]), "+v"(W[t0 + 5]), "+v"(W[t0 + 6]), "+v"(W[t0 + 7]) : : "memory"); } while (0)
  int n;
#pragma unroll 1
  for (n = 11; n < 21; n += 2) {
    wo_t = n < 19 ? n - 11 : -1; K_STEP(A, n, n + 2);
    wo_t = n < 19 ? n - 10 : -1; K_STEP(B, n + 1, n + 3);
  }
  WO_COPY(0);
#pragma unroll 1
  for (; n < 31; n += 2) {
    wo_t = n < 29 ? n - 13 : -1; K_STEP(A, n, n + 2);
    wo_t = n < 29 ? n - 12 : -1; K_STEP(B, n + 1, n + 3);
  }
  WO_COPY(8);
  wo_t = -1;
  K_STEP(A, 31, 33);

  STAMP(6);
  mx = fmaxf(mx, dpp<XOR8>(mx)); mx = max16_32(mx);
  const float mw = mx * CS;
  LDS_FENCE();
  {
    const int lane = fresh_lane();
    LAS v4f* p = (LAS v4f*)(L + L_SC + w * 2048 + lane * 32);
    v4f a = p[0], bb = p[1];
    float l = 0.f;
#pragma unroll
    for (int e = 0; e < 4; ++e) { a[e] = ex2(fmaf(a[e], CS, -mw)); bb[e] = ex2(fmaf(bb[e], CS, -mw)); l += a[e] + bb[e]; }
    p[0] = a; p[1] = bb;
    l = wave_sum(l);
    if (lane == 0) __builtin_amdgcn_raw_buffer_store_b128(v4u{__float_as_uint(mw), tagA, __float_as_uint(l), tagB}, rws, WS_ML + (((b * NH + h) * 2 + s) * 8 + w) * 16, 0, AUX_SC1);
  }
  LDS_FENCE();
  STAMP(11);

  v2f acc2[12];
#pragma unroll
  for (int e = 0; e < 12; ++e) acc2[e] = v2f{0.f, 0.f};
#define PIN_ACC() asm volatile("" : "+v"(acc2[0]), "+v"(acc2[1]), "+v"(acc2[2]), "+v"(acc2[3]), "+v"(acc2[4]), "+v"(acc2[5]), "+v"(acc2[6]), "+v"(acc2[7]), \
                                    "+v"(acc2[8]), "+v"(acc2[9]), "+v"(acc2[10]), "+v"(acc2[11]) : : "memory")
  auto v_tile = [&](const v4u& r0, const v4u& r1, const v4u& r2, float p) {
    const v2f pp = v2f{p, p};
#pragma unroll
    for (int d = 0; d < 4; ++d) {
      acc2[d] = __builtin_elementwise_fma(pp, v2f{bflo(r0[d]), bfhi(r0[d])}, acc2[d]);
      acc2[4 + d] = __builtin_elementwise_fma(pp, v2f{bflo(r1[d]), bfhi(r1[d])}, acc2[4 + d]);
      acc2[8 + d] = __builtin_elementwise_fma(pp, v2f{bflo(r2[d]), bfhi(r2[d])}, acc2[8 + d]);
    }
    PIN_ACC();
  };
#define V_USE(G, n) do { const v2f p2_ = *(LAS v2f*)(L + scoff + (n) * 64); v_tile(G.r[0][0], G.r[0][1], G.r[0][2], p2_[0]); v_tile(G.r[1][0], G.r[1][1], G.r[1][2], p2_[1]); } while (0)
#pragma unroll 1
  for (n = 0; n < 10; n += 2) {
    V_USE(B, n); wo_t = n < 8 ? n + 16 : -1; ISSUE(B, NGW + n + 2);
    V_USE(A, n + 1); wo_t = n < 8 ? n + 17 : -1; ISSUE(A, NGW + n + 3);
  }
  WO_COPY(16);
#pragma unroll 1
  for (; n < 20; n += 2) {
    V_USE(B, n); wo_t = n < 18 ? n + 14 : -1; ISSUE(B, NGW + n + 2);
    V_USE(A, n + 1); wo_t = n < 18 ? n + 15 : -1; ISSUE(A, NGW + n + 3);
  }
  WO_COPY(24);
  wo_t = -1;
#pragma unroll 1
  for (; n < NGW - 2; n += 2) { V_USE(B, n); ISSUE(B, NGW + n + 2); V_USE(A, n + 1); ISSUE(A, NGW + n + 3); }
  const int mloff = WS_ML + ((b * NH + h) * 16 + (fresh_lane() & 15)) * 16;
  v4u ml = poll16(rws, mloff);
  SCHED_FENCE();
#define PAIR_WO(u0, u1) do { _Pragma("unroll") for (int u = u0; u < u1; ++u) { \
      _Pragma("unroll") for (int d = 0; d < 4; ++d) { const unsigned a = W[2 * u][d], bb = W[2 * u + 1][d]; \
        W[2 * u][d] = __builtin_amdgcn_perm(bb, a, 0x05040100u); W[2 * u + 1][d] = __builtin_amdgcn_perm(bb, a, 0x07060302u); } \
      asm volatile("" : "+v"(W[2 * u]), "+v"(W[2 * u + 1])); SCHED_FENCE(); } } while (0)
  PAIR_WO(0, 8);
  V_USE(B, NGW - 2);
  SCHED_FENCE();
  PAIR_WO(8, 16);
  V_USE(A, NGW - 1);

  STAMP(7);
  for (unsigned spins = 0; !__all((ml[1] == tagA) & (ml[3] == tagB)); ) {
    if (++spins > SPIN_CAP) break;
    __builtin_amdgcn_s_sleep(2);
    ml = poll16(rws, mloff);
  }
  {
    const float m_i = __uint_as_float(ml[0]), l_i = __uint_as_float(ml[2]);
    float M = m_i;
    M = fmaxf(M, dpp<XOR1>(M)); M = fmaxf(M, dpp<XOR2>(M)); M = fmaxf(M, dpp<XOR7>(M)); M = fmaxf(M, dpp<XOR8>(M));
    float Ls = l_i * ex2(m_i - M);
    Ls = oct_sum(Ls); Ls += dpp<XOR8>(Ls);
    const float gate = *(LAS float*)(L + L_MISC);
    const float f = ex2(mw - M) / Ls * gate;
    float acc[24];
#pragma unroll
    for (int e = 0; e < 24; ++e) acc[e] = xoct_sum(acc2[e >> 1][e & 1]) * f;
    const int c = fresh_lane();
    if (c < 8) {
#pragma unroll
      for (int i = 0; i < 3; ++i) {
        LAS v4f* p = (LAS v4f*)(L + L_QP + (w * 192 + i * 64 + c * 8) * 4);
        p[0] = v4f{acc[i * 8], acc[i * 8 + 1], acc[i * 8 + 2], acc[i * 8 + 3]}; p[1] = v4f{acc[i * 8 + 4], acc[i * 8 + 5], acc[i * 8 + 6], acc[i * 8 + 7]};
      }
    }
  }
  STAMP(8);
  __syncthreads();
  const int tid2 = w * 64 + fresh_lane();
  if (tid2 < 96) {
    const int r = tid2 >> 4, u = tid2 & 15;
    float a0 = 0.f, a1 = 0.f;
#pragma unroll
    for (int ww = 0; ww < 8; ++ww) { const LAS float* p = (const LAS float*)(L + L_QP) + ww * 192 + u * 12 + r; a0 += p[0]; a1 += p[6]; }
    *(LAS unsigned*)(L + L_OB + tid2 * 4) = cvtpk(a0, a1);
  }
  __syncthreads();
  {
    const int ln = fresh_lane();
    const int yr = ln < 60 ? ln / 10 : 0, yc = ln - (ln / 10) * 10;
    LAS float* yrp = (LAS float*)(L + L_FP) + w * 480;
    float ya[8];
    { const LAS v4f* p = (const LAS v4f*)(L + L_YF + (w * 480 + yr * 80 + yc * 8) * 4); const v4f a = p[0], bb = p[1];
#pragma unroll
      for (int e = 0; e < 4; ++e) { ya[e] = a[e]; ya[4 + e] = bb[e]; } }
    const LAS v4u* op = (const LAS v4u*)(L + L_OB + yr * 64);
#pragma unroll
    for (int u4 = 0; u4 < 4; ++u4) {
      const v4u o4 = op[u4];
#pragma unroll
      for (int e = 0; e < 4; ++e) {
        const int u = u4 * 4 + e;
#pragma unroll
        for (int d = 0; d < 4; ++d) { ya[2 * d] = dot2(W[2 * u][d], o4[e], ya[2 * d]); ya[2 * d + 1] = dot2(W[2 * u + 1][d], o4[e], ya[2 * d + 1]); }
      }
    }
    if (ln < 60) { LAS v4f* p = (LAS v4f*)(yrp + yr * 80 + yc * 8); p[0] = v4f{ya[0], ya[1], ya[2], ya[3]}; p[1] = v4f{ya[4], ya[5], ya[6], ya[7]}; }
    LDS_FENCE();
    if (ln < 40) {
      v2f y = v2f{0.f, 0.f};
#pragma unroll
      for (int r = 0; r < 6; ++r) y += *(LAS v2f*)(yrp + r * 80 + ln * 2);
      const int cons = w * 8 + ln / 5, g5 = ln % 5;
      __builtin_amdgcn_raw_buffer_store_b128(v4u{__float_as_uint(y[0]), tagA, __float_as_uint(y[1]), tagB}, rws, WS_INBOX + (((b * 64 + cons) * 64 + j) * 5 + g5) * 16, 0, AUX_SC1);
    }
  }
  STAMP(9);
  if (w == 0) {
    const int lane = fresh_lane();
    const int io = WS_INBOX + ((b * 64 + j) * 64 + lane) * 80;
    v4u g5[5];
    for (unsigned spins = 0;;) {
      bool ok = true;
#pragma unroll
      for (int e = 0; e < 5; ++e) g5[e] = poll16(rws, io + e * 16);
#pragma unroll
      for (int e = 0; e < 5; ++e) ok = ok & (g5[e][1] == tagA) & (g5[e][3] == tagB);
      if (__all(ok) || ++spins > SPIN_CAP) break;
      __builtin_amdgcn_s_sleep(2);
    }
    float y[10];
#pragma unroll
    for (int e = 0; e < 5; ++e) { y[2 * e] = wave_sum(__uint_as_float(g5[e][0])); y[2 * e + 1] = wave_sum(__uint_as_float(g5[e][2])); }
    unsigned o = cvtpk(y[0], y[1]);
#pragma unroll
    for (int e = 1; e < 5; ++e) { const unsigned v = cvtpk(y[2 * e], y[2 * e + 1]); o = (lane == e) ? v : o; }
    if (lane < 5) *(GAS unsigned*)(out + b * DM + j * 10 + lane * 2) = o;
    if (b == 0 && j == 0 && lane == 0) __hip_atomic_store((GAS u64*)(ws + WS_EPOCH), ((u64)tagB << 32) | tagA, __ATOMIC_RELAXED, __HIP_MEMORY_SCOPE_AGENT);
  }
}
}

extern "C" void kernel_launch(void* const* d_in, const int* in_sizes, int n_in, void* d_out, int out_size, void* d_ws, size_t ws_size,
                              hipStream_t stream) {
  (void)in_sizes; (void)n_in; (void)out_size; (void)ws_size;
  hipLaunchKernelGGL(fused_decode, dim3(NWG), dim3(NTHR), 0, stream, (const u16*)d_in[0], (const u16*)d_in[1], (const u16*)d_in[2], (const u16*)d_in[3],
                     (const u16*)d_in[4], (const u16*)d_in[6], (const u16*)d_in[7], (const u16*)d_in[8], (const u16*)d_in[9], (u16*)d_out, (unsigned char*)d_ws);
}
```

```cpp
#include <hip/hip_runtime.h>
#include <hip/hip_bf16.h>
#include <stdint.h>

#define GAS __attribute__((address_space(1)))
#define LAS __attribute__((address_space(3)))
typedef unsigned v4u __attribute__((ext_vector_type(4)));
typedef unsigned v2u __attribute__((ext_vector_type(2)));
typedef float v4f __attribute__((ext_vector_type(4)));
typedef float v2f __attribute__((ext_vector_type(2)));
typedef __bf16 bf16x2 __attribute__((ext_vector_type(2)));
typedef unsigned long long u64;
typedef unsigned short u16;

namespace {
constexpr int NB = 4, NH = 32, SK = 8192, DH = 192, DM = 640, FF = 1536;
constexpr int NWG = 256, NWAVE = 8, NTHR = NWAVE * 64;
constexpr int KEYS_WG = SK / 2;
constexpr int ROWB = DH * 2;
constexpr int TILEB = 8 * ROWB;
constexpr int GRPB = 2 * TILEB;
constexpr int KVB = KEYS_WG * ROWB;
constexpr int NGW = KVB / GRPB / NWAVE;
constexpr int RSRC3 = 0x00020000;
#define POLL_AUX 16
#define STORE_AUX 16
constexpr int AUX_NT = 2, AUX_SC1 = STORE_AUX;
constexpr int OOB = 1 << 28;
constexpr float CS = 0.07216878364870322f * 1.4426950408889634f;
#define TAU16 (178 * 16)
#define VDEF_FROM 112
#define VDEF_TO 256
#define XHEAD16 (TAU16 * 10 / 10)
#define XPOLL16 (TAU16 * 3 / 10)
#define XFFN16 (TAU16 * 3 / 10)
#define XDMA16 (TAU16 * 3 / 100)
#define PHW 0

constexpr int WS_EPOCH = 0;
constexpr int WS_ML = 4096;
constexpr int WS_INBOX = 65536;
constexpr int WS_QX = WS_INBOX + NB * 64 * 64 * 80;
constexpr int WS_FX = WS_QX + NH * NB * 8 * 128 * 16;
constexpr int WS_BYTES = WS_FX + 8 * NB * 8 * 384 * 16;

constexpr int L_XS = 0;
constexpr int L_QP = 5120;
constexpr int QPS = 196;
constexpr int L_QB = L_QP + 8 * QPS * 4;
constexpr int L_OB = L_QB + 384;
constexpr int L_MISC = L_OB + 384;
constexpr int L_G = L_MISC + 256;
constexpr int L_YF = L_G + 3072;
constexpr int L_SC = L_YF + 8 * 480 * 4;
constexpr int L_FP2 = L_YF;
constexpr int L_FP = L_YF + 22 * 4 * 192 * 4;
constexpr int L_TR = L_FP + 22 * 4 * 192 * 4;
constexpr int L_BYTES = L_TR;

constexpr unsigned SPIN_CAP = 1u << 17;

__device__ __forceinline__ float dot2(unsigned a, unsigned b, float c) {
  return __builtin_amdgcn_fdot2_f32_bf16(__builtin_bit_cast(bf16x2, a), __builtin_bit_cast(bf16x2, b), c, false);
}
template <int CTRL> __device__ __forceinline__ float dpp(float x) {
  return __builtin_bit_cast(float, __builtin_amdgcn_mov_dpp(__builtin_bit_cast(int, x), CTRL, 0xf, 0xf, true));
}
constexpr int XOR1 = 0xB1, XOR2 = 0x4E, XOR7 = 0x141, XOR8 = 0x128;
__device__ __forceinline__ float sum16_32(float x) {
  auto s = __builtin_amdgcn_permlane16_swap(__float_as_uint(x), __float_as_uint(x), false, false);
  x = __uint_as_float(s[0]) + __uint_as_float(s[1]);
  auto t = __builtin_amdgcn_permlane32_swap(__float_as_uint(x), __float_as_uint(x), false, false);
  return __uint_as_float(t[0]) + __uint_as_float(t[1]);
}
__device__ __forceinline__ float max16_32(float x) {
  auto s = __builtin_amdgcn_permlane16_swap(__float_as_uint(x), __float_as_uint(x), false, false);
  x = fmaxf(__uint_as_float(s[0]), __uint_as_float(s[1]));
  auto t = __builtin_amdgcn_permlane32_swap(__float_as_uint(x), __float_as_uint(x), false, false);
  return fmaxf(__uint_as_float(t[0]), __uint_as_float(t[1]));
}
__device__ __forceinline__ float oct_sum(float x) { x += dpp<XOR1>(x); x += dpp<XOR2>(x); x += dpp<XOR7>(x); return x; }
__device__ __forceinline__ float xoct_sum(float x) { x += dpp<XOR8>(x); return sum16_32(x); }
__device__ __forceinline__ float wave_sum(float x) { return xoct_sum(oct_sum(x)); }
__device__ __forceinline__ unsigned cvtpk(float lo, float hi) { unsigned r; asm("v_cvt_pk_bf16_f32 %0, %1, %2" : "=v"(r) : "v"(lo), "v"(hi)); return r; }
__device__ __forceinline__ float bflo(unsigned u) { return __uint_as_float(u << 16); }
__device__ __forceinline__ float bfhi(unsigned u) { return __uint_as_float(u & 0xffff0000u); }
__device__ __forceinline__ float ex2(float x) { return __builtin_amdgcn_exp2f(x); }
__device__ __forceinline__ __amdgpu_buffer_rsrc_t rsrc(const void* p, int bytes) { return __builtin_amdgcn_make_buffer_rsrc((void*)p, (short)0, bytes, RSRC3); }
template <int AUX> __device__ __forceinline__ v4u ld16(__amdgpu_buffer_rsrc_t r, int voff, int soff) { return __builtin_amdgcn_raw_buffer_load_b128(r, voff, soff, AUX); }
template <int AUX> __device__ __forceinline__ v2u ld8(__amdgpu_buffer_rsrc_t r, int voff, int soff) { return __builtin_amdgcn_raw_buffer_load_b64(r, voff, soff, AUX); }
__device__ __forceinline__ unsigned now32() { return (unsigned)__builtin_amdgcn_s_memrealtime(); }
#define STAMP(i) do { } while (0)
#define TR(id) do { } while (0)
#define SCHED_FENCE() __builtin_amdgcn_sched_barrier(0)
#define LDS_FENCE() asm volatile("s_waitcnt lgkmcnt(0)" ::: "memory")
#define MEM_FENCE() asm volatile("" ::: "memory")
__device__ __forceinline__ v4u poll16(__amdgpu_buffer_rsrc_t r, int voff) { asm volatile("" : "+v"(voff) : : "memory"); return __builtin_amdgcn_raw_buffer_load_b128(r, voff, 0, POLL_AUX); }
__device__ __forceinline__ int fresh_lane() { int l; asm volatile("v_mbcnt_lo_u32_b32 %0, -1, 0\n\tv_mbcnt_hi_u32_b32 %0, -1, %0" : "=v"(l)); return l; }

#define WO_POLICY "nt"
__device__ __forceinline__ void dma16(__amdgpu_buffer_rsrc_t r, int voff, int soff, unsigned lds) {
  unsigned keep;
  asm volatile("s_mov_b32 %0, m0\n\ts_mov_b32 m0, %3\n\ts_nop 0\n\tbuffer_load_dwordx4 %1, %2, %4 offen " WO_POLICY " lds\n\ts_mov_b32 m0, %0"
               : "=&s"(keep) : "v"(voff), "s"(r), "s"(lds), "s"(soff) : "memory");
}
#define W2_AUX AUX_NT
struct Grp { v4u r[2][3]; };

__global__ void __launch_bounds__(NTHR, 2)
fused_decode(const u16* __restrict__ x, const u16* __restrict__ Kc, const u16* __restrict__ Vc, const u16* __restrict__ wq,
             const u16* __restrict__ wo, const u16* __restrict__ w1, const u16* __restrict__ w3, const u16* __restrict__ w2,
             const u16* __restrict__ gw, u16* __restrict__ out, unsigned char* ws) {
  __shared__ __attribute__((aligned(16))) unsigned char lds_raw[L_BYTES];
  LAS unsigned char* L = (LAS unsigned char*)lds_raw;
  const int tid = threadIdx.x, lane = tid & 63, w = __builtin_amdgcn_readfirstlane(tid >> 6);
  const int k = lane >> 3, c = lane & 7;
  const int blk = blockIdx.x, xg = blk & 7, idx = blk >> 3;
  const int h = xg * 4 + (idx & 3), sh = idx >> 2, b = sh >> 1, s = sh & 1;
  const int j = h * 2 + s;
  STAMP(0);
#define PHASE 0
#define T0OFF 80
  unsigned T0 = now32() + T0OFF + idx * PHASE * (TAU16 / 32) / (16 * 32);
  const __amdgpu_buffer_rsrc_t rws = rsrc(ws, WS_BYTES);
  const v4u tiny = __builtin_nontemporal_load((const GAS v4u*)(tid == 320 ? (const unsigned char*)ws + WS_EPOCH : (const unsigned char*)x + (tid < 320 ? tid : 0) * 16));
  const int qr = tid / 24, qc = tid - qr * 24;
  v4u wqv[4], fwa, fwb;
  {
    const __amdgpu_buffer_rsrc_t rq = rsrc(wq + h * DH, (DM * NH * DH - h * DH) * 2);
    const int oob = tid < 480 ? 0 : OOB;
#pragma unroll
    for (int e = 0; e < 4; ++e) wqv[e] = ld16<AUX_NT>(rq, (sh * 80 + qr * 4) * (NH * DH * 2) + qc * 16 + oob, e * (NH * DH * 2));
    const int row0 = (idx >> 1) * 40 + qr * 2;
    const __amdgpu_buffer_rsrc_t r13 = rsrc(((idx & 1) ? w3 : w1) + xg * 192, (DM * FF - xg * 192) * 2);
    fwa = ld16<AUX_NT>(r13, row0 * (FF * 2) + qc * 16 + oob, 0); fwb = ld16<AUX_NT>(r13, row0 * (FF * 2) + qc * 16 + oob, FF * 2);
  }
  const size_t kvoff = ((size_t)(b * NH + h) * SK + (size_t)s * KEYS_WG) * DH;
  const u16* Kb = Kc + kvoff; const u16* Vb = Vc + kvoff;
  const int kvo = w * GRPB + k * ROWB + c * 16;
  unsigned tn = T0 << 4;
  auto pace = [&](unsigned step16) { while ((int)((now32() << 4) - tn) < 0) __builtin_amdgcn_s_sleep(1); tn += step16; };
  int wo_t = -1;
  __amdgpu_buffer_rsrc_t ro = rsrc(wo + (size_t)h * DH * DM, DH * DM * 2);
  int wo_vo = 0;
  auto issue = [&](v4u& r00, v4u& r01, v4u& r02, v4u& r10, v4u& r11, v4u& r12, int m) {
    TR(1);
    pace(TAU16 + (wo_t >= 0 ? XDMA16 : 0));
    TR(2);
    if (wo_t >= 0) dma16(ro, wo_vo, wo_t * 6 * (DM * 2), (unsigned)(L_FP + w * 8192 + (wo_t & 7) * 1024));
    const __amdgpu_buffer_rsrc_t r = rsrc(m < NGW ? Kb : Vb, KVB);
    const int so = (m & (NGW - 1)) * (NWAVE * GRPB);
    if (m >= NGW && (b * NH + h) * 2 + s >= VDEF_FROM && (b * NH + h) * 2 + s < VDEF_TO) {
      r00 = ld16<0>(r, kvo, so); r01 = ld16<0>(r, kvo + 128, so); r02 = ld16<0>(r, kvo + 256, so);
      r10 = ld16<0>(r, kvo, so + TILEB); r11 = ld16<0>(r, kvo + 128, so + TILEB); r12 = ld16<0>(r, kvo + 256, so + TILEB);
    } else
    {
    r00 = ld16<AUX_NT>(r, kvo, so); r01 = ld16<AUX_NT>(r, kvo + 128, so); r02 = ld16<AUX_NT>(r, kvo + 256, so);
    r10 = ld16<AUX_NT>(r, kvo, so + TILEB); r11 = ld16<AUX_NT>(r, kvo + 128, so + TILEB); r12 = ld16<AUX_NT>(r, kvo + 256, so + TILEB);
    }
    SCHED_FENCE();
    TR(3);
  };
#define ISSUE(G, m) issue(G.r[0][0], G.r[0][1], G.r[0][2], G.r[1][0], G.r[1][1], G.r[1][2], m)
#define ISSUE_W(o, m) issue(W[o], W[o + 1], W[o + 2], W[o + 3], W[o + 4], W[o + 5], m)
  Grp A, B;
  v4u W[32];
  ISSUE(A, 0);
  ISSUE(B, 1);
  tn += XHEAD16;
  STAMP(1);
  if (tid <= 320) *(LAS v4u*)(L + (tid < 320 ? L_XS + tid * 16 : L_MISC + 16)) = tiny;
  __syncthreads();
  const unsigned tagA = __builtin_amdgcn_readfirstlane(*(LAS unsigned*)(L + L_MISC + 16)) + 1u, tagB = __builtin_amdgcn_readfirstlane(*(LAS unsigned*)(L + L_MISC + 20)) + 1u;
  v2u xq[4]; unsigned fx4[4];
  { const int qrc = qr < 20 ? qr : 0;
#pragma unroll
    for (int e = 0; e < 4; ++e) { xq[e] = *(LAS v2u*)(L + L_XS + e * (DM * 2) + (sh * 80 + qrc * 4) * 2); fx4[e] = *(LAS unsigned*)(L + L_XS + e * (DM * 2) + ((idx >> 1) * 40 + qrc * 2) * 2); } }
  {
    unsigned lo01[4], hi01[4], lo23[4], hi23[4];
#pragma unroll
    for (int d = 0; d < 4; ++d) {
      lo01[d] = __builtin_amdgcn_perm(wqv[1][d], wqv[0][d], 0x05040100u); hi01[d] = __builtin_amdgcn_perm(wqv[1][d], wqv[0][d], 0x07060302u);
      lo23[d] = __builtin_amdgcn_perm(wqv[3][d], wqv[2][d], 0x05040100u); hi23[d] = __builtin_amdgcn_perm(wqv[3][d], wqv[2][d], 0x07060302u);
    }
#pragma unroll
    for (int bb = 0; bb < 4; ++bb) {
      float a[8];
#pragma unroll
      for (int d = 0; d < 4; ++d) { a[2 * d] = dot2(lo23[d], xq[bb][1], dot2(lo01[d], xq[bb][0], 0.f)); a[2 * d + 1] = dot2(hi23[d], xq[bb][1], dot2(hi01[d], xq[bb][0], 0.f)); }
      LAS v4f* p = (LAS v4f*)(L + L_FP + ((qr * 4 + bb) * 192 + qc * 8) * 4);
      p[0] = v4f{a[0], a[1], a[2], a[3]}; p[1] = v4f{a[4], a[5], a[6], a[7]};
    }
  }
  {
    unsigned lo[4], hi[4];
#pragma unroll
    for (int d = 0; d < 4; ++d) { lo[d] = __builtin_amdgcn_perm(fwb[d], fwa[d], 0x05040100u); hi[d] = __builtin_amdgcn_perm(fwb[d], fwa[d], 0x07060302u); }
#pragma unroll
    for (int bb = 0; bb < 4; ++bb) {
      LAS v4f* p = (LAS v4f*)(L + L_FP2 + ((qr * 4 + bb) * 192 + qc * 8) * 4);
      p[0] = v4f{dot2(lo[0], fx4[bb], 0.f), dot2(hi[0], fx4[bb], 0.f), dot2(lo[1], fx4[bb], 0.f), dot2(hi[1], fx4[bb], 0.f)};
      p[1] = v4f{dot2(lo[2], fx4[bb], 0.f), dot2(hi[2], fx4[bb], 0.f), dot2(lo[3], fx4[bb], 0.f), dot2(hi[3], fx4[bb], 0.f)};
    }
  }
  TR(9);
  __syncthreads();
  TR(10);
  STAMP(2);
  if (tid < 384) {
    const int bb = tid / 96, p = tid - bb * 96;
    float a0 = 0.f, a1 = 0.f;
#pragma unroll
    for (int r = 0; r < 20; ++r) { const v2f v = *(LAS v2f*)(L + L_FP + ((r * 4 + bb) * 192 + p * 2) * 4); a0 += v[0]; a1 += v[1]; }
    __builtin_amdgcn_raw_buffer_store_b128(v4u{__float_as_uint(a0), tagA, __float_as_uint(a1), tagB}, rws, WS_QX + (((h * 4 + bb) * 8 + sh) * 128 + p) * 16, 0, AUX_SC1);
    a0 = 0.f; a1 = 0.f;
#pragma unroll
    for (int r = 0; r < 20; ++r) { const v2f v = *(LAS v2f*)(L + L_FP2 + ((r * 4 + bb) * 192 + p * 2) * 4); a0 += v[0]; a1 += v[1]; }
    __builtin_amdgcn_raw_buffer_store_b128(v4u{__float_as_uint(a0), tagA, __float_as_uint(a1), tagB}, rws,
                                           WS_FX + (((((xg * 4 + bb) * 8 + p / 12) * 2 + (idx & 1)) * 16 + (idx >> 1)) * 12 + p % 12) * 16, 0, AUX_SC1);
  }
  if (tid >= 384 && tid < 388)
    __builtin_amdgcn_raw_buffer_store_b128(v4u{T0, tagA, T0, tagB}, rws, WS_QX + (((h * 4 + (tid - 384)) * 8 + sh) * 128 + 96) * 16, 0, AUX_SC1);
  SCHED_FENCE();
  TR(11);
  ISSUE_W(0, 2);
  ISSUE_W(6, 3);
  ISSUE_W(12, 4);
  TR(12);
  if (w == 0) {
    const int qo = WS_QX + (h * 4 + b) * 8 * 2048 + lane * 16;
    v4u ga[8], gb[8];
    for (unsigned spins = 0;;) {
#pragma unroll
      for (int p = 0; p < 8; ++p) { ga[p] = poll16(rws, qo + p * 2048); gb[p] = poll16(rws, qo + p * 2048 + 1024 + (lane <= 32 ? 0 : OOB)); }
      bool ok = true;
#pragma unroll
      for (int p = 0; p < 8; ++p) ok = ok & (ga[p][1] == tagA) & (ga[p][3] == tagB) & ((lane > 32) | ((gb[p][1] == tagA) & (gb[p][3] == tagB)));
      TR(17);
      if (__all(ok) || ++spins > SPIN_CAP) break;
      __builtin_amdgcn_s_sleep(1);
    }
    TR(16);
    float a0 = 0.f, a1 = 0.f, b0 = 0.f, b1 = 0.f;
#pragma unroll
    for (int p = 0; p < 8; ++p) { a0 += __uint_as_float(ga[p][0]); a1 += __uint_as_float(ga[p][2]); b0 += __uint_as_float(gb[p][0]); b1 += __uint_as_float(gb[p][2]); }
    *(LAS unsigned*)(L + L_QB + lane * 4) = cvtpk(a0, a1);
    if (lane < 32) *(LAS unsigned*)(L + L_QB + 256 + lane * 4) = cvtpk(b0, b1);
    unsigned tl = gb[0][0];
#pragma unroll
    for (int p = 1; p < 8; ++p) tl = (int)(gb[p][0] - tl) > 0 ? gb[p][0] : tl;
    if (lane == 32) *(LAS unsigned*)(L + L_MISC + 8) = tl;
  }
  TR(13);
  tn += XPOLL16;
  ISSUE_W(18, 5);
  ISSUE_W(24, 6);
  __syncthreads();
  TR(14);
  STAMP(3);
  { const unsigned tl = __builtin_amdgcn_readfirstlane(*(LAS unsigned*)(L + L_MISC + 8)); if (tl - T0 < 1000u) tn += (tl - T0) << 4; }
#define SLACK 3
  { const unsigned t = (now32() << 4) - SLACK * TAU16; if ((int)(t - tn) > 0) tn = t; }
  v4u Q[3];
#pragma unroll
  for (int i = 0; i < 3; ++i) Q[i] = *(LAS v4u*)(L + L_QB + (c + 8 * i) * 16);

  float mx = -INFINITY;
  const bool oct0 = c == 0;
  const int scoff = L_SC + w * 2048 + k * 8;
  auto k_tile = [&](const v4u& r0, const v4u& r1, const v4u& r2) -> float {
    float a0 = dot2(r0[0], Q[0][0], 0.f), a1 = dot2(r1[0], Q[1][0], 0.f), a2 = dot2(r2[0], Q[2][0], 0.f);
#pragma unroll
    for (int d = 1; d < 4; ++d) { a0 = dot2(r0[d], Q[0][d], a0); a1 = dot2(r1[d], Q[1][d], a1); a2 = dot2(r2[d], Q[2][d], a2); }
    return (a0 + a1) + a2;
  };
  auto k_put = [&](float s0, float s1, int n) {
    s0 = oct_sum(s0); s1 = oct_sum(s1);
    mx = fmaxf(mx, fmaxf(s0, s1));
    if (oct0) *(LAS v2f*)(L + scoff + n * 64) = v2f{s0, s1};
  };
#define K_STEP(G, n, m) do { float s0_ = k_tile(G.r[0][0], G.r[0][1], G.r[0][2]), s1_ = k_tile(G.r[1][0], G.r[1][1], G.r[1][2]); \
    asm volatile("" : "+v"(s0_), "+v"(s1_) : : "memory"); ISSUE(G, m); k_put(s0_, s1_, n); } while (0)
  K_STEP(A, 0, 7);
#pragma unroll
  for (int g = 0; g < 5; ++g) k_put(k_tile(W[g * 6], W[g * 6 + 1], W[g * 6 + 2]), k_tile(W[g * 6 + 3], W[g * 6 + 4], W[g * 6 + 5]), 2 + g);
  STAMP(4);
  K_STEP(B, 1, 8);
  STAMP(5);
  {
    const int lane = fresh_lane(), tid2 = w * 64 + lane;
    const int yr = lane / 10, yc = lane - yr * 10;
    const int fxo = WS_FX + ((xg * 4 + b) * 8 + (j & 7)) * 6144 + tid2 * 16 + (tid2 < 384 ? 0 : OOB);
    v4u fx = poll16(rws, fxo);
    const __amdgpu_buffer_rsrc_t r2 = rsrc(w2, FF * DM * 2), rg = rsrc(gw, w == 0 ? DM * 2 : 0);
    const int vo2 = (j * 24 + yr) * (DM * 2) + w * 160 + yc * 16 + (lane < 60 ? 0 : OOB);
#pragma unroll
    for (int t = 0; t < 4; ++t) W[8 + t] = ld16<W2_AUX>(r2, vo2, t * 6 * (DM * 2));
    const v4u gg0 = ld16<AUX_NT>(rg, lane * 16, 0), gg1 = ld16<AUX_NT>(rg, 1024 + lane * 16, 0);
    SCHED_FENCE();
    TR(30);
    tn += XFFN16;
    K_STEP(A, 7, 9); K_STEP(B, 8, 10);
    TR(31);
    for (unsigned spins = 0; !__all((tid2 >= 384) | ((fx[1] == tagA) & (fx[3] == tagB))); ) {
      if (++spins > SPIN_CAP) break;
      __builtin_amdgcn_s_sleep(2);
      fx = poll16(rws, fxo);
    }
    TR(32);
    if (tid2 < 384) *(LAS v2f*)(L + L_G + ((tid2 / 12) * 24 + (tid2 % 12) * 2) * 4) = v2f{__uint_as_float(fx[0]), __uint_as_float(fx[2])};
    float gate = 0.f;
    if (w == 0) {
      const v4u gx0 = *(LAS v4u*)(L + L_XS + b * (DM * 2) + lane * 16), gx1 = *(LAS v4u*)(L + L_XS + b * (DM * 2) + 1024 + (lane & 15) * 16);
      float d = 0.f;
#pragma unroll
      for (int e = 0; e < 4; ++e) { d = dot2(gx0[e], gg0[e], d); d = dot2(gx1[e], gg1[e], d); }
      gate = 1.f / (1.f + __expf(-wave_sum(d)));
      if (lane == 0) *(LAS float*)(L + L_MISC) = gate;
    }
    __syncthreads();
    TR(33);
    if (tid2 < 24) {
      const LAS float* g = (const LAS float*)(L + L_G) + tid2;
      float ga = 0.f, up = 0.f;
#pragma unroll
      for (int r = 0; r < 16; ++r) { ga += g[r * 24]; up += g[(16 + r) * 24]; }
      *(LAS float*)(L + L_MISC + 64 + tid2 * 4) = ga / (1.f + __expf(-ga)) * up * (1.f - gate);
    }
    __syncthreads();
    TR(34);
    v2f ya[4];
#pragma unroll
    for (int e = 0; e < 4; ++e) ya[e] = v2f{0.f, 0.f};
#pragma unroll
    for (int t = 0; t < 4; ++t) {
      const float hv = *(LAS float*)(L + L_MISC + 64 + (t * 6 + (yr < 6 ? yr : 0)) * 4);
#pragma unroll
      for (int d = 0; d < 4; ++d) ya[d] = __builtin_elementwise_fma(v2f{hv, hv}, v2f{bflo(W[8 + t][d]), bfhi(W[8 + t][d])}, ya[d]);
    }
    if (lane < 60) {
      LAS v4f* p = (LAS v4f*)(L + L_YF + (w * 480 + yr * 80 + yc * 8) * 4);
      p[0] = v4f{ya[0][0], ya[0][1], ya[1][0], ya[1][1]}; p[1] = v4f{ya[2][0], ya[2][1], ya[3][0], ya[3][1]};
    }
    wo_vo = yr * (DM * 2) + w * 160 + yc * 16 + (lane < 60 ? 0 : OOB);
  }
  MEM_FENCE();
  TR(35);
  K_STEP(A, 9, 11); K_STEP(B, 10, 12);
  STAMP(13);
#define WO_COPY(t0) do { asm volatile("s_waitcnt vmcnt(12)" ::: "memory"); const int ln_ = fresh_lane(); \
    _Pragma("unroll") for (int t = 0; t < 8; t += 2) {     \
      const v4u a_ = *(LAS v4u*)(L + L_FP + w * 8192 + t * 1024 + ln_ * 16), b_ = *(LAS v4u*)(L + L_FP + w * 8192 + (t + 1) * 1024 + ln_ * 16); \
      _Pragma("unroll") for (int d = 0; d < 4; ++d) { W[t0 + t][d] = __builtin_amdgcn_perm(b_[d], a_[d], 0x05040100u); W[t0 + t + 1][d] = __builtin_amdgcn_perm(b_[d], a_[d], 0x07060302u); } } \
    asm volatile("" : "+v"(W[t0]), "+v"(W[t0 + 1]), "+v"(W[t0 + 2]), "+v"(W[t0 + 3]), "+v"(W[t0 + 4]), "+v"(W[t0 + 5]), "+v"(W[t0 + 6]), "+v"(W[t0 + 7]) : : "memory"); } while (0)
  int n;
#pragma unroll 1
  for (n = 11; n < 21; n += 2) {
    wo_t = n < 19 ? n - 11 : -1; K_STEP(A, n, n + 2);
    wo_t = n < 19 ? n - 10 : -1; K_STEP(B, n + 1, n + 3);
  }
  WO_COPY(0);
#pragma unroll 1
  for (; n < 31; n += 2) {
    wo_t = n < 29 ? n - 13 : -1; K_STEP(A, n, n + 2);
    wo_t = n < 29 ? n - 12 : -1; K_STEP(B, n + 1, n + 3);
  }
  WO_COPY(8);
  wo_t = -1;
  K_STEP(A, 31, 33);

  STAMP(6);
  mx = fmaxf(mx, dpp<XOR8>(mx)); mx = max16_32(mx);
  const float mw = mx * CS;
  LDS_FENCE();
  {
    const int lane = fresh_lane();
    LAS v4f* p = (LAS v4f*)(L + L_SC + w * 2048 + lane * 32);
    v4f a = p[0], bb = p[1];
    float l = 0.f;
#pragma unroll
    for (int e = 0; e < 4; ++e) { a[e] = ex2(fmaf(a[e], CS, -mw)); bb[e] = ex2(fmaf(bb[e], CS, -mw)); l += a[e] + bb[e]; }
    p[0] = a; p[1] = bb;
    l = wave_sum(l);
    if (lane == 0) __builtin_amdgcn_raw_buffer_store_b128(v4u{__float_as_uint(mw), tagA, __float_as_uint(l), tagB}, rws, WS_ML + (((b * NH + h) * 2 + s) * 8 + w) * 16, 0, AUX_SC1);
  }
  LDS_FENCE();
  STAMP(11);

  v2f acc2[12];
#pragma unroll
  for (int e = 0; e < 12; ++e) acc2[e] = v2f{0.f, 0.f};
#define PIN_ACC() asm volatile("" : "+v"(acc2[0]), "+v"(acc2[1]), "+v"(acc2[2]), "+v"(acc2[3]), "+v"(acc2[4]), "+v"(acc2[5]), "+v"(acc2[6]), "+v"(acc2[7]), \
                                    "+v"(acc2[8]), "+v"(acc2[9]), "+v"(acc2[10]), "+v"(acc2[11]) : : "memory")
  auto v_tile = [&](const v4u& r0, const v4u& r1, const v4u& r2, float p) {
    const v2f pp = v2f{p, p};
#pragma unroll
    for (int d = 0; d < 4; ++d) {
      acc2[d] = __builtin_elementwise_fma(pp, v2f{bflo(r0[d]), bfhi(r0[d])}, acc2[d]);
      acc2[4 + d] = __builtin_elementwise_fma(pp, v2f{bflo(r1[d]), bfhi(r1[d])}, acc2[4 + d]);
      acc2[8 + d] = __builtin_elementwise_fma(pp, v2f{bflo(r2[d]), bfhi(r2[d])}, acc2[8 + d]);
    }
    PIN_ACC();
  };
#define V_USE(G, n) do { const v2f p2_ = *(LAS v2f*)(L + scoff + (n) * 64); v_tile(G.r[0][0], G.r[0][1], G.r[0][2], p2_[0]); v_tile(G.r[1][0], G.r[1][1], G.r[1][2], p2_[1]); } while (0)
#pragma unroll 1
  for (n = 0; n < 10; n += 2) {
    V_USE(B, n); wo_t = n < 8 ? n + 16 : -1; ISSUE(B, NGW + n + 2);
    V_USE(A, n + 1); wo_t = n < 8 ? n + 17 : -1; ISSUE(A, NGW + n + 3);
  }
  WO_COPY(16);
#pragma unroll 1
  for (; n < 20; n += 2) {
    V_USE(B, n); wo_t = n < 18 ? n + 14 : -1; ISSUE(B, NGW + n + 2);
    V_USE(A, n + 1); wo_t = n < 18 ? n + 15 : -1; ISSUE(A, NGW + n + 3);
  }
  WO_COPY(24);
  wo_t = -1;
  const int mloff = WS_ML + ((b * NH + h) * 16 + (fresh_lane() & 15)) * 16;
  v4u ml = poll16(rws, mloff);
  V_USE(B, n); ISSUE(B, NGW + n + 2); V_USE(A, n + 1); ISSUE(A, NGW + n + 3);
  for (unsigned spins = 0; !__all((ml[1] == tagA) & (ml[3] == tagB)); ) {
    if (++spins > SPIN_CAP) break;
    __builtin_amdgcn_s_sleep(2);
    ml = poll16(rws, mloff);
  }
  float fw;
  {
    const float m_i = __uint_as_float(ml[0]), l_i = __uint_as_float(ml[2]);
    float M = m_i;
    M = fmaxf(M, dpp<XOR1>(M)); M = fmaxf(M, dpp<XOR2>(M)); M = fmaxf(M, dpp<XOR7>(M)); M = fmaxf(M, dpp<XOR8>(M));
    float Ls = l_i * ex2(m_i - M);
    Ls = oct_sum(Ls); Ls += dpp<XOR8>(Ls);
    fw = __uint_as_float(__builtin_amdgcn_readfirstlane(__float_as_uint(ex2(mw - M) / Ls * *(LAS float*)(L + L_MISC))));
  }
#pragma unroll 1
  for (n += 2; n < NGW - 2; n += 2) { V_USE(B, n); ISSUE(B, NGW + n + 2); V_USE(A, n + 1); ISSUE(A, NGW + n + 3); }
  V_USE(B, NGW - 2);
  V_USE(A, NGW - 1);

  STAMP(7);
  TR(40);
  {
    float b12[12], c6[6];
#pragma unroll
    for (int i = 0; i < 12; ++i) {
      auto r = __builtin_amdgcn_permlane32_swap(__float_as_uint(acc2[i >> 1][i & 1]), __float_as_uint(acc2[6 + (i >> 1)][i & 1]), false, false);
      b12[i] = __uint_as_float(r[0]) + __uint_as_float(r[1]);
    }
#pragma unroll
    for (int i = 0; i < 6; ++i) {
      auto r = __builtin_amdgcn_permlane16_swap(__float_as_uint(b12[i]), __float_as_uint(b12[i + 6]), false, false);
      c6[i] = __uint_as_float(r[0]) + __uint_as_float(r[1]);
    }
#pragma unroll
    for (int i = 0; i < 6; ++i) c6[i] = (c6[i] + dpp<XOR8>(c6[i])) * fw;
    const int ln = fresh_lane();
    if ((ln & 8) == 0) {
      const int v0 = (ln >> 4) * 6;
#pragma unroll
      for (int p = 0; p < 3; ++p) { const int v = v0 + 2 * p; *(LAS v2f*)(L + L_QP + (w * QPS + (v >> 3) * 64 + (ln & 7) * 8 + (v & 7)) * 4) = v2f{c6[2 * p], c6[2 * p + 1]}; }
    }
  }
  const int ln = fresh_lane();
  const int yr = ln < 60 ? ln / 10 : 0, yc = ln - (ln / 10) * 10;
  float ya[8];
  { const LAS v4f* p = (const LAS v4f*)(L + L_YF + (w * 480 + yr * 80 + yc * 8) * 4); const v4f a = p[0], bb = p[1];
#pragma unroll
    for (int e = 0; e < 4; ++e) { ya[e] = a[e]; ya[4 + e] = bb[e]; } }
  STAMP(8);
  TR(41);
  __syncthreads();
  TR(42);
  if (w < 6) {
    const int pi = w * 16 + (ln >> 2), r = pi >> 4, u = pi & 15, qq = ln & 3;
    const LAS float* p = (const LAS float*)(L + L_QP) + 2 * qq * QPS + u * 12 + r;
    float a0 = p[0] + p[QPS], a1 = p[6] + p[QPS + 6];
    a0 += dpp<XOR1>(a0); a1 += dpp<XOR1>(a1); a0 += dpp<XOR2>(a0); a1 += dpp<XOR2>(a1);
    if (qq == 0) *(LAS unsigned*)(L + L_OB + pi * 4) = cvtpk(a0, a1);
  }
  TR(43);
  __syncthreads();
  TR(44);
  {
    LAS float* yrp = (LAS float*)(L + L_FP) + w * 480;
    const LAS v4u* op = (const LAS v4u*)(L + L_OB + yr * 64);
#pragma unroll
    for (int u4 = 0; u4 < 4; ++u4) {
      const v4u o4 = op[u4];
#pragma unroll
      for (int e = 0; e < 4; ++e) {
        const int u = u4 * 4 + e;
#pragma unroll
        for (int d = 0; d < 4; ++d) { ya[2 * d] = dot2(W[2 * u][d], o4[e], ya[2 * d]); ya[2 * d + 1] = dot2(W[2 * u + 1][d], o4[e], ya[2 * d + 1]); }
      }
    }
    asm volatile("" : "+v"(ya[0]), "+v"(ya[1]), "+v"(ya[2]), "+v"(ya[3]), "+v"(ya[4]), "+v"(ya[5]), "+v"(ya[6]), "+v"(ya[7]));
    TR(45);
    if (ln < 60) { LAS v4f* p = (LAS v4f*)(yrp + yr * 80 + yc * 8); p[0] = v4f{ya[0], ya[1], ya[2], ya[3]}; p[1] = v4f{ya[4], ya[5], ya[6], ya[7]}; }
    LDS_FENCE();
    TR(46);
    if (ln < 40) {
      v2f y = v2f{0.f, 0.f};
#pragma unroll
      for (int r = 0; r < 6; ++r) y += *(LAS v2f*)(yrp + r * 80 + ln * 2);
      const int cons = w * 8 + ln / 5, g5 = ln % 5;
      __builtin_amdgcn_raw_buffer_store_b128(v4u{__float_as_uint(y[0]), tagA, __float_as_uint(y[1]), tagB}, rws, WS_INBOX + (((b * 64 + cons) * 64 + j) * 5 + g5) * 16, 0, AUX_SC1);
    }
  }
  STAMP(9);
  TR(47);
  if (w == 0) {
    const int io = WS_INBOX + ((b * 64 + j) * 64 + ln) * 80;
    v4u g5[5];
    for (unsigned spins = 0;;) {
      bool ok = true;
#pragma unroll
      for (int e = 0; e < 5; ++e) g5[e] = poll16(rws, io + e * 16);
#pragma unroll
      for (int e = 0; e < 5; ++e) ok = ok & (g5[e][1] == tagA) & (g5[e][3] == tagB);
      if (__all(ok) || ++spins > SPIN_CAP) break;
      __builtin_amdgcn_s_sleep(1);
    }
    TR(48);
    const unsigned yv[10] = {g5[0][0], g5[0][2], g5[1][0], g5[1][2], g5[4][0], g5[2][0], g5[2][2], g5[3][0], g5[3][2], g5[4][2]};
    float b5[5];
#pragma unroll
    for (int i = 0; i < 5; ++i) { auto r = __builtin_amdgcn_permlane32_swap(yv[i], yv[5 + i], false, false); b5[i] = __uint_as_float(r[0]) + __uint_as_float(r[1]); }
    float z[3];
    { auto r = __builtin_amdgcn_permlane16_swap(__float_as_uint(b5[0]), __float_as_uint(b5[2]), false, false); z[0] = __uint_as_float(r[0]) + __uint_as_float(r[1]); }
    { auto r = __builtin_amdgcn_permlane16_swap(__float_as_uint(b5[1]), __float_as_uint(b5[3]), false, false); z[1] = __uint_as_float(r[0]) + __uint_as_float(r[1]); }
    { auto r = __builtin_amdgcn_permlane16_swap(__float_as_uint(b5[4]), __float_as_uint(b5[4]), false, false); z[2] = __uint_as_float(r[0]) + __uint_as_float(r[1]); }
#pragma unroll
    for (int i = 0; i < 3; ++i) { z[i] += dpp<XOR1>(z[i]); z[i] += dpp<XOR2>(z[i]); z[i] += dpp<XOR7>(z[i]); z[i] += dpp<XOR8>(z[i]); }
    auto r89 = __builtin_amdgcn_permlane32_swap(__float_as_uint(z[2]), __float_as_uint(z[2]), false, false);
    const unsigned o03 = cvtpk(z[0], z[1]), o4 = cvtpk(__uint_as_float(r89[0]), __uint_as_float(r89[1]));
    if ((ln & 15) == 0 || ln == 1) *(GAS unsigned*)(out + b * DM + j * 10 + (ln == 1 ? 8 : (ln >> 4) * 2)) = ln == 1 ? o4 : o03;
    if (b == 0 && j == 0 && ln == 0) __hip_atomic_store((GAS u64*)(ws + WS_EPOCH), ((u64)tagB << 32) | tagA, __ATOMIC_RELAXED, __HIP_MEMORY_SCOPE_AGENT);
  }
  TR(49);
}
}

extern "C" void kernel_launch(void* const* d_in, const int* in_sizes, int n_in, void* d_out, int out_size, void* d_ws, size_t ws_size,
                              hipStream_t stream) {
  (void)in_sizes; (void)n_in; (void)out_size; (void)ws_size;
  hipLaunchKernelGGL(fused_decode, dim3(NWG), dim3(NTHR), 0, stream, (const u16*)d_in[0], (const u16*)d_in[1], (const u16*)d_in[2], (const u16*)d_in[3],
                     (const u16*)d_in[4], (const u16*)d_in[6], (const u16*)d_in[7], (const u16*)d_in[8], (const u16*)d_in[9], (u16*)d_out, (unsigned char*)d_ws);
}
```

```cpp
#include <hip/hip_runtime.h>
#include <hip/hip_bf16.h>
#include <stdint.h>

#define GAS __attribute__((address_space(1)))
#define LAS __attribute__((address_space(3)))
typedef unsigned v4u __attribute__((ext_vector_type(4)));
typedef unsigned v2u __attribute__((ext_vector_type(2)));
typedef float v4f __attribute__((ext_vector_type(4)));
typedef float v2f __attribute__((ext_vector_type(2)));
typedef __bf16 bf16x2 __attribute__((ext_vector_type(2)));
typedef unsigned long long u64;
typedef unsigned short u16;

namespace {
constexpr int NB = 4, NH = 32, SK = 8192, DH = 192, DM = 640, FF = 1536;
constexpr int NWG = 256, NWAVE = 8, NTHR = NWAVE * 64;
constexpr int KEYS_WG = SK / 2;
constexpr int ROWB = DH * 2;
constexpr int TILEB = 8 * ROWB;
constexpr int GRPB = 2 * TILEB;
constexpr int KVB = KEYS_WG * ROWB;
constexpr int NGW = KVB / GRPB / NWAVE;
constexpr int RSRC3 = 0x00020000;
#define POLL_AUX 16
#define STORE_AUX 16
constexpr int AUX_NT = 2, AUX_SC1 = STORE_AUX;
constexpr int OOB = 1 << 28;
constexpr float CS = 0.07216878364870322f * 1.4426950408889634f;
#define TAU16 (178 * 16)
#define FIXDUR 20000
#define FIXSEL (blk >= 248)
#define XHEAD16 (TAU16 * 10 / 10)
#define XPOLL16 (TAU16 * 3 / 10)
#define XFFN16 (TAU16 * 3 / 10)
#define XDMA16 (TAU16 * 3 / 100)
#define PHW 0

constexpr int WS_EPOCH = 0;
constexpr int WS_ML = 4096;
constexpr int WS_INBOX = 65536;
constexpr int WS_QX = WS_INBOX + NB * 64 * 64 * 80;
constexpr int WS_FX = WS_QX + NH * NB * 8 * 128 * 16;
constexpr int WS_BYTES = WS_FX + 8 * NB * 8 * 384 * 16;

constexpr int L_XS = 0;
constexpr int L_QP = 5120;
constexpr int QPS = 196;
constexpr int L_QB = L_QP + 8 * QPS * 4;
constexpr int L_OB = L_QB + 384;
constexpr int L_MISC = L_OB + 384;
constexpr int L_G = L_MISC + 256;
constexpr int L_YF = L_G + 3072;
constexpr int L_SC = L_YF + 8 * 480 * 4;
constexpr int L_FP2 = L_YF;
constexpr int L_FP = L_YF + 22 * 4 * 192 * 4;
constexpr int L_TR = L_FP + 22 * 4 * 192 * 4;
constexpr int L_BYTES = L_TR;

constexpr unsigned SPIN_CAP = 1u << 17;

__device__ __forceinline__ float dot2(unsigned a, unsigned b, float c) {
  return __builtin_amdgcn_fdot2_f32_bf16(__builtin_bit_cast(bf16x2, a), __builtin_bit_cast(bf16x2, b), c, false);
}
template <int CTRL> __device__ __forceinline__ float dpp(float x) {
  return __builtin_bit_cast(float, __builtin_amdgcn_mov_dpp(__builtin_bit_cast(int, x), CTRL, 0xf, 0xf, true));
}
constexpr int XOR1 = 0xB1, XOR2 = 0x4E, XOR7 = 0x141, XOR8 = 0x128;
__device__ __forceinline__ float sum16_32(float x) {
  auto s = __builtin_amdgcn_permlane16_swap(__float_as_uint(x), __float_as_uint(x), false, false);
  x = __uint_as_float(s[0]) + __uint_as_float(s[1]);
  auto t = __builtin_amdgcn_permlane32_swap(__float_as_uint(x), __float_as_uint(x), false, false);
  return __uint_as_float(t[0]) + __uint_as_float(t[1]);
}
__device__ __forceinline__ float max16_32(float x) {
  auto s = __builtin_amdgcn_permlane16_swap(__float_as_uint(x), __float_as_uint(x), false, false);
  x = fmaxf(__uint_as_float(s[0]), __uint_as_float(s[1]));
  auto t = __builtin_amdgcn_permlane32_swap(__float_as_uint(x), __float_as_uint(x), false, false);
  return fmaxf(__uint_as_float(t[0]), __uint_as_float(t[1]));
}
__device__ __forceinline__ float oct_sum(float x) { x += dpp<XOR1>(x); x += dpp<XOR2>(x); x += dpp<XOR7>(x); return x; }
__device__ __forceinline__ float xoct_sum(float x) { x += dpp<XOR8>(x); return sum16_32(x); }
__device__ __forceinline__ float wave_sum(float x) { return xoct_sum(oct_sum(x)); }
__device__ __forceinline__ unsigned cvtpk(float lo, float hi) { unsigned r; asm("v_cvt_pk_bf16_f32 %0, %1, %2" : "=v"(r) : "v"(lo), "v"(hi)); return r; }
__device__ __forceinline__ float bflo(unsigned u) { return __uint_as_float(u << 16); }
__device__ __forceinline__ float bfhi(unsigned u) { return __uint_as_float(u & 0xffff0000u); }
__device__ __forceinline__ float ex2(float x) { return __builtin_amdgcn_exp2f(x); }
__device__ __forceinline__ __amdgpu_buffer_rsrc_t rsrc(const void* p, int bytes) { return __builtin_amdgcn_make_buffer_rsrc((void*)p, (short)0, bytes, RSRC3); }
template <int AUX> __device__ __forceinline__ v4u ld16(__amdgpu_buffer_rsrc_t r, int voff, int soff) { return __builtin_amdgcn_raw_buffer_load_b128(r, voff, soff, AUX); }
template <int AUX> __device__ __forceinline__ v2u ld8(__amdgpu_buffer_rsrc_t r, int voff, int soff) { return __builtin_amdgcn_raw_buffer_load_b64(r, voff, soff, AUX); }
__device__ __forceinline__ unsigned now32() { return (unsigned)__builtin_amdgcn_s_memrealtime(); }
#define STAMP(i) do { } while (0)
#define TR(id) do { } while (0)
#define SCHED_FENCE() __builtin_amdgcn_sched_barrier(0)
#define LDS_FENCE() asm volatile("s_waitcnt lgkmcnt(0)" ::: "memory")
#define MEM_FENCE() asm volatile("" ::: "memory")
__device__ __forceinline__ v4u poll16(__amdgpu_buffer_rsrc_t r, int voff) { asm volatile("" : "+v"(voff) : : "memory"); return __builtin_amdgcn_raw_buffer_load_b128(r, voff, 0, POLL_AUX); }
__device__ __forceinline__ int fresh_lane() { int l; asm volatile("v_mbcnt_lo_u32_b32 %0, -1, 0\n\tv_mbcnt_hi_u32_b32 %0, -1, %0" : "=v"(l)); return l; }

#define WO_POLICY "nt"
__device__ __forceinline__ void dma16(__amdgpu_buffer_rsrc_t r, int voff, int soff, unsigned lds) {
  unsigned keep;
  asm volatile("s_mov_b32 %0, m0\n\ts_mov_b32 m0, %3\n\ts_nop 0\n\tbuffer_load_dwordx4 %1, %2, %4 offen " WO_POLICY " lds\n\ts_mov_b32 m0, %0"
               : "=&s"(keep) : "v"(voff), "s"(r), "s"(lds), "s"(soff) : "memory");
}
#define W2_AUX AUX_NT
struct Grp { v4u r[2][3]; };

__global__ void __launch_bounds__(NTHR, 2)
fused_decode(const u16* __restrict__ x, const u16* __restrict__ Kc, const u16* __restrict__ Vc, const u16* __restrict__ wq,
             const u16* __restrict__ wo, const u16* __restrict__ w1, const u16* __restrict__ w3, const u16* __restrict__ w2,
             const u16* __restrict__ gw, u16* __restrict__ out, unsigned char* ws) {
  __shared__ __attribute__((aligned(16))) unsigned char lds_raw[L_BYTES];
  LAS unsigned char* L = (LAS unsigned char*)lds_raw;
  const int tid = threadIdx.x, lane = tid & 63, w = __builtin_amdgcn_readfirstlane(tid >> 6);
  const int k = lane >> 3, c = lane & 7;
  const int blk = blockIdx.x, xg = blk & 7, idx = blk >> 3;
  const int h = xg * 4 + (idx & 3), sh = idx >> 2, b = sh >> 1, s = sh & 1;
  const int j = h * 2 + s;
  STAMP(0);
#define PHASE 0
#define T0OFF 80
  unsigned T0 = now32() + T0OFF + idx * PHASE * (TAU16 / 32) / (16 * 32);
  const __amdgpu_buffer_rsrc_t rws = rsrc(ws, WS_BYTES);
  const v4u tiny = __builtin_nontemporal_load((const GAS v4u*)(tid == 320 ? (const unsigned char*)ws + WS_EPOCH : (const unsigned char*)x + (tid < 320 ? tid : 0) * 16));
  const int qr = tid / 24, qc = tid - qr * 24;
  v4u wqv[4], fwa, fwb;
  {
    const __amdgpu_buffer_rsrc_t rq = rsrc(wq + h * DH, (DM * NH * DH - h * DH) * 2);
    const int oob = tid < 480 ? 0 : OOB;
#pragma unroll
    for (int e = 0; e < 4; ++e) wqv[e] = ld16<AUX_NT>(rq, (sh * 80 + qr * 4) * (NH * DH * 2) + qc * 16 + oob, e * (NH * DH * 2));
    const int row0 = (idx >> 1) * 40 + qr * 2;
    const __amdgpu_buffer_rsrc_t r13 = rsrc(((idx & 1) ? w3 : w1) + xg * 192, (DM * FF - xg * 192) * 2);
    fwa = ld16<AUX_NT>(r13, row0 * (FF * 2) + qc * 16 + oob, 0); fwb = ld16<AUX_NT>(r13, row0 * (FF * 2) + qc * 16 + oob, FF * 2);
  }
  const size_t kvoff = ((size_t)(b * NH + h) * SK + (size_t)s * KEYS_WG) * DH;
  const u16* Kb = Kc + kvoff; const u16* Vb = Vc + kvoff;
  const int kvo = w * GRPB + k * ROWB + c * 16;
  unsigned tn = T0 << 4;
  auto pace = [&](unsigned step16) { while ((int)((now32() << 4) - tn) < 0) __builtin_amdgcn_s_sleep(1); tn += step16; };
  int wo_t = -1;
  __amdgpu_buffer_rsrc_t ro = rsrc(wo + (size_t)h * DH * DM, DH * DM * 2);
  int wo_vo = 0;
  auto issue = [&](v4u& r00, v4u& r01, v4u& r02, v4u& r10, v4u& r11, v4u& r12, int m) {
    TR(1);
    pace(TAU16 + (wo_t >= 0 ? XDMA16 : 0));
    TR(2);
    if (wo_t >= 0) dma16(ro, wo_vo, wo_t * 6 * (DM * 2), (unsigned)(L_FP + w * 8192 + (wo_t & 7) * 1024));
    const __amdgpu_buffer_rsrc_t r = rsrc(m < NGW ? Kb : Vb, KVB);
    const int so = (m & (NGW - 1)) * (NWAVE * GRPB);
    r00 = ld16<AUX_NT>(r, kvo, so); r01 = ld16<AUX_NT>(r, kvo + 128, so); r02 = ld16<AUX_NT>(r, kvo + 256, so);
    r10 = ld16<AUX_NT>(r, kvo, so + TILEB); r11 = ld16<AUX_NT>(r, kvo + 128, so + TILEB); r12 = ld16<AUX_NT>(r, kvo + 256, so + TILEB);
    SCHED_FENCE();
    TR(3);
  };
#define ISSUE(G, m) issue(G.r[0][0], G.r[0][1], G.r[0][2], G.r[1][0], G.r[1][1], G.r[1][2], m)
#define ISSUE_W(o, m) issue(W[o], W[o + 1], W[o + 2], W[o + 3], W[o + 4], W[o + 5], m)
  Grp A, B;
  v4u W[32];
  ISSUE(A, 0);
  ISSUE(B, 1);
  tn += XHEAD16;
  STAMP(1);
  if (tid <= 320) *(LAS v4u*)(L + (tid < 320 ? L_XS + tid * 16 : L_MISC + 16)) = tiny;
  __syncthreads();
  const unsigned tagA = __builtin_amdgcn_readfirstlane(*(LAS unsigned*)(L + L_MISC + 16)) + 1u, tagB = __builtin_amdgcn_readfirstlane(*(LAS unsigned*)(L + L_MISC + 20)) + 1u;
  v2u xq[4]; unsigned fx4[4];
  { const int qrc = qr < 20 ? qr : 0;
#pragma unroll
    for (int e = 0; e < 4; ++e) { xq[e] = *(LAS v2u*)(L + L_XS + e * (DM * 2) + (sh * 80 + qrc * 4) * 2); fx4[e] = *(LAS unsigned*)(L + L_XS + e * (DM * 2) + ((idx >> 1) * 40 + qrc * 2) * 2); } }
  {
    unsigned lo01[4], hi01[4], lo23[4], hi23[4];
#pragma unroll
    for (int d = 0; d < 4; ++d) {
      lo01[d] = __builtin_amdgcn_perm(wqv[1][d], wqv[0][d], 0x05040100u); hi01[d] = __builtin_amdgcn_perm(wqv[1][d], wqv[0][d], 0x07060302u);
      lo23[d] = __builtin_amdgcn_perm(wqv[3][d], wqv[2][d], 0x05040100u); hi23[d] = __builtin_amdgcn_perm(wqv[3][d], wqv[2][d], 0x07060302u);
    }
#pragma unroll
    for (int bb = 0; bb < 4; ++bb) {
      float a[8];
#pragma unroll
      for (int d = 0; d < 4; ++d) { a[2 * d] = dot2(lo23[d], xq[bb][1], dot2(lo01[d], xq[bb][0], 0.f)); a[2 * d + 1] = dot2(hi23[d], xq[bb][1], dot2(hi01[d], xq[bb][0], 0.f)); }
      LAS v4f* p = (LAS v4f*)(L + L_FP + ((qr * 4 + bb) * 192 + qc * 8) * 4);
      p[0] = v4f{a[0], a[1], a[2], a[3]}; p[1] = v4f{a[4], a[5], a[6], a[7]};
    }
  }
  {
    unsigned lo[4], hi[4];
#pragma unroll
    for (int d = 0; d < 4; ++d) { lo[d] = __builtin_amdgcn_perm(fwb[d], fwa[d], 0x05040100u); hi[d] = __builtin_amdgcn_perm(fwb[d], fwa[d], 0x07060302u); }
#pragma unroll
    for (int bb = 0; bb < 4; ++bb) {
      LAS v4f* p = (LAS v4f*)(L + L_FP2 + ((qr * 4 + bb) * 192 + qc * 8) * 4);
      p[0] = v4f{dot2(lo[0], fx4[bb], 0.f), dot2(hi[0], fx4[bb], 0.f), dot2(lo[1], fx4[bb], 0.f), dot2(hi[1], fx4[bb], 0.f)};
      p[1] = v4f{dot2(lo[2], fx4[bb], 0.f), dot2(hi[2], fx4[bb], 0.f), dot2(lo[3], fx4[bb], 0.f), dot2(hi[3], fx4[bb], 0.f)};
    }
  }
  TR(9);
  __syncthreads();
  TR(10);
  STAMP(2);
  if (tid < 384) {
    const int bb = tid / 96, p = tid - bb * 96;
    float a0 = 0.f, a1 = 0.f;
#pragma unroll
    for (int r = 0; r < 20; ++r) { const v2f v = *(LAS v2f*)(L + L_FP + ((r * 4 + bb) * 192 + p * 2) * 4); a0 += v[0]; a1 += v[1]; }
    __builtin_amdgcn_raw_buffer_store_b128(v4u{__float_as_uint(a0), tagA, __float_as_uint(a1), tagB}, rws, WS_QX + (((h * 4 + bb) * 8 + sh) * 128 + p) * 16, 0, AUX_SC1);
    a0 = 0.f; a1 = 0.f;
#pragma unroll
    for (int r = 0; r < 20; ++r) { const v2f v = *(LAS v2f*)(L + L_FP2 + ((r * 4 + bb) * 192 + p * 2) * 4); a0 += v[0]; a1 += v[1]; }
    __builtin_amdgcn_raw_buffer_store_b128(v4u{__float_as_uint(a0), tagA, __float_as_uint(a1), tagB}, rws,
                                           WS_FX + (((((xg * 4 + bb) * 8 + p / 12) * 2 + (idx & 1)) * 16 + (idx >> 1)) * 12 + p % 12) * 16, 0, AUX_SC1);
  }
  if (tid >= 384 && tid < 388)
    __builtin_amdgcn_raw_buffer_store_b128(v4u{T0, tagA, T0, tagB}, rws, WS_QX + (((h * 4 + (tid - 384)) * 8 + sh) * 128 + 96) * 16, 0, AUX_SC1);
  SCHED_FENCE();
  TR(11);
  ISSUE_W(0, 2);
  ISSUE_W(6, 3);
  ISSUE_W(12, 4);
  TR(12);
  if (w == 0) {
    const int qo = WS_QX + (h * 4 + b) * 8 * 2048 + lane * 16;
    v4u ga[8], gb[8];
    for (unsigned spins = 0;;) {
#pragma unroll
      for (int p = 0; p < 8; ++p) { ga[p] = poll16(rws, qo + p * 2048); gb[p] = poll16(rws, qo + p * 2048 + 1024 + (lane <= 32 ? 0 : OOB)); }
      bool ok = true;
#pragma unroll
      for (int p = 0; p < 8; ++p) ok = ok & (ga[p][1] == tagA) & (ga[p][3] == tagB) & ((lane > 32) | ((gb[p][1] == tagA) & (gb[p][3] == tagB)));
      TR(17);
      if (__all(ok) || ++spins > SPIN_CAP) break;
      __builtin_amdgcn_s_sleep(1);
    }
    TR(16);
    float a0 = 0.f, a1 = 0.f, b0 = 0.f, b1 = 0.f;
#pragma unroll
    for (int p = 0; p < 8; ++p) { a0 += __uint_as_float(ga[p][0]); a1 += __uint_as_float(ga[p][2]); b0 += __uint_as_float(gb[p][0]); b1 += __uint_as_float(gb[p][2]); }
    *(LAS unsigned*)(L + L_QB + lane * 4) = cvtpk(a0, a1);
    if (lane < 32) *(LAS unsigned*)(L + L_QB + 256 + lane * 4) = cvtpk(b0, b1);
    unsigned tl = gb[0][0];
#pragma unroll
    for (int p = 1; p < 8; ++p) tl = (int)(gb[p][0] - tl) > 0 ? gb[p][0] : tl;
    if (lane == 32) *(LAS unsigned*)(L + L_MISC + 8) = tl;
  }
  TR(13);
  tn += XPOLL16;
  ISSUE_W(18, 5);
  ISSUE_W(24, 6);
  __syncthreads();
  TR(14);
  STAMP(3);
  { const unsigned tl = __builtin_amdgcn_readfirstlane(*(LAS unsigned*)(L + L_MISC + 8)); if (tl - T0 < 1000u) tn += (tl - T0) << 4; }
#define SLACK 3
  { const unsigned t = (now32() << 4) - SLACK * TAU16; if ((int)(t - tn) > 0) tn = t; }
  v4u Q[3];
#pragma unroll
  for (int i = 0; i < 3; ++i) Q[i] = *(LAS v4u*)(L + L_QB + (c + 8 * i) * 16);

  float mx = -INFINITY;
  const bool oct0 = c == 0;
  const int scoff = L_SC + w * 2048 + k * 8;
  auto k_tile = [&](const v4u& r0, const v4u& r1, const v4u& r2) -> float {
    float a0 = dot2(r0[0], Q[0][0], 0.f), a1 = dot2(r1[0], Q[1][0], 0.f), a2 = dot2(r2[0], Q[2][0], 0.f);
#pragma unroll
    for (int d = 1; d < 4; ++d) { a0 = dot2(r0[d], Q[0][d], a0); a1 = dot2(r1[d], Q[1][d], a1); a2 = dot2(r2[d], Q[2][d], a2); }
    return (a0 + a1) + a2;
  };
  auto k_put = [&](float s0, float s1, int n) {
    s0 = oct_sum(s0); s1 = oct_sum(s1);
    mx = fmaxf(mx, fmaxf(s0, s1));
    if (oct0) *(LAS v2f*)(L + scoff + n * 64) = v2f{s0, s1};
  };
#define K_STEP(G, n, m) do { float s0_ = k_tile(G.r[0][0], G.r[0][1], G.r[0][2]), s1_ = k_tile(G.r[1][0], G.r[1][1], G.r[1][2]); \
    asm volatile("" : "+v"(s0_), "+v"(s1_) : : "memory"); ISSUE(G, m); k_put(s0_, s1_, n); } while (0)
  K_STEP(A, 0, 7);
#pragma unroll
  for (int g = 0; g < 5; ++g) k_put(k_tile(W[g * 6], W[g * 6 + 1], W[g * 6 + 2]), k_tile(W[g * 6 + 3], W[g * 6 + 4], W[g * 6 + 5]), 2 + g);
  STAMP(4);
  K_STEP(B, 1, 8);
  STAMP(5);
  {
    const int lane = fresh_lane(), tid2 = w * 64 + lane;
    const int yr = lane / 10, yc = lane - yr * 10;
    const int fxo = WS_FX + ((xg * 4 + b) * 8 + (j & 7)) * 6144 + tid2 * 16 + (tid2 < 384 ? 0 : OOB);
    v4u fx = poll16(rws, fxo);
    const __amdgpu_buffer_rsrc_t r2 = rsrc(w2, FF * DM * 2), rg = rsrc(gw, w == 0 ? DM * 2 : 0);
    const int vo2 = (j * 24 + yr) * (DM * 2) + w * 160 + yc * 16 + (lane < 60 ? 0 : OOB);
#pragma unroll
    for (int t = 0; t < 4; ++t) W[8 + t] = ld16<W2_AUX>(r2, vo2, t * 6 * (DM * 2));
    const v4u gg0 = ld16<AUX_NT>(rg, lane * 16, 0), gg1 = ld16<AUX_NT>(rg, 1024 + lane * 16, 0);
    SCHED_FENCE();
    TR(30);
    tn += XFFN16;
    K_STEP(A, 7, 9); K_STEP(B, 8, 10);
    TR(31);
    for (unsigned spins = 0; !__all((tid2 >= 384) | ((fx[1] == tagA) & (fx[3] == tagB))); ) {
      if (++spins > SPIN_CAP) break;
      __builtin_amdgcn_s_sleep(2);
      fx = poll16(rws, fxo);
    }
    TR(32);
    if (tid2 < 384) *(LAS v2f*)(L + L_G + ((tid2 / 12) * 24 + (tid2 % 12) * 2) * 4) = v2f{__uint_as_float(fx[0]), __uint_as_float(fx[2])};
    float gate = 0.f;
    if (w == 0) {
      const v4u gx0 = *(LAS v4u*)(L + L_XS + b * (DM * 2) + lane * 16), gx1 = *(LAS v4u*)(L + L_XS + b * (DM * 2) + 1024 + (lane & 15) * 16);
      float d = 0.f;
#pragma unroll
      for (int e = 0; e < 4; ++e) { d = dot2(gx0[e], gg0[e], d); d = dot2(gx1[e], gg1[e], d); }
      gate = 1.f / (1.f + __expf(-wave_sum(d)));
      if (lane == 0) *(LAS float*)(L + L_MISC) = gate;
    }
    __syncthreads();
    TR(33);
    if (tid2 < 24) {
      const LAS float* g = (const LAS float*)(L + L_G) + tid2;
      float ga = 0.f, up = 0.f;
#pragma unroll
      for (int r = 0; r < 16; ++r) { ga += g[r * 24]; up += g[(16 + r) * 24]; }
      *(LAS float*)(L + L_MISC + 64 + tid2 * 4) = ga / (1.f + __expf(-ga)) * up * (1.f - gate);
    }
    __syncthreads();
    TR(34);
    v2f ya[4];
#pragma unroll
    for (int e = 0; e < 4; ++e) ya[e] = v2f{0.f, 0.f};
#pragma unroll
    for (int t = 0; t < 4; ++t) {
      const float hv = *(LAS float*)(L + L_MISC + 64 + (t * 6 + (yr < 6 ? yr : 0)) * 4);
#pragma unroll
      for (int d = 0; d < 4; ++d) ya[d] = __builtin_elementwise_fma(v2f{hv, hv}, v2f{bflo(W[8 + t][d]), bfhi(W[8 + t][d])}, ya[d]);
    }
    if (lane < 60) {
      LAS v4f* p = (LAS v4f*)(L + L_YF + (w * 480 + yr * 80 + yc * 8) * 4);
      p[0] = v4f{ya[0][0], ya[0][1], ya[1][0], ya[1][1]}; p[1] = v4f{ya[2][0], ya[2][1], ya[3][0], ya[3][1]};
    }
    wo_vo = yr * (DM * 2) + w * 160 + yc * 16 + (lane < 60 ? 0 : OOB);
  }
  MEM_FENCE();
  TR(35);
  K_STEP(A, 9, 11); K_STEP(B, 10, 12);
  STAMP(13);
#define WO_COPY(t0) do { asm volatile("s_waitcnt vmcnt(12)" ::: "memory"); const int ln_ = fresh_lane(); \
    _Pragma("unroll") for (int t = 0; t < 8; t += 2) {     \
      const v4u a_ = *(LAS v4u*)(L + L_FP + w * 8192 + t * 1024 + ln_ * 16), b_ = *(LAS v4u*)(L + L_FP + w * 8192 + (t + 1) * 1024 + ln_ * 16); \
      _Pragma("unroll") for (int d = 0; d < 4; ++d) { W[t0 + t][d] = __builtin_amdgcn_perm(b_[d], a_[d], 0x05040100u); W[t0 + t + 1][d] = __builtin_amdgcn_perm(b_[d], a_[d], 0x07060302u); } } \
    asm volatile("" : "+v"(W[t0]), "+v"(W[t0 + 1]), "+v"(W[t0 + 2]), "+v"(W[t0 + 3]), "+v"(W[t0 + 4]), "+v"(W[t0 + 5]), "+v"(W[t0 + 6]), "+v"(W[t0 + 7]) : : "memory"); } while (0)
  int n;
#pragma unroll 1
  for (n = 11; n < 21; n += 2) {
    wo_t = n < 19 ? n - 11 : -1; K_STEP(A, n, n + 2);
    wo_t = n < 19 ? n - 10 : -1; K_STEP(B, n + 1, n + 3);
  }
  WO_COPY(0);
#pragma unroll 1
  for (; n < 31; n += 2) {
    wo_t = n < 29 ? n - 13 : -1; K_STEP(A, n, n + 2);
    wo_t = n < 29 ? n - 12 : -1; K_STEP(B, n + 1, n + 3);
  }
  WO_COPY(8);
  wo_t = -1;
  K_STEP(A, 31, 33);

  STAMP(6);
  mx = fmaxf(mx, dpp<XOR8>(mx)); mx = max16_32(mx);
  const float mw = mx * CS;
  LDS_FENCE();
  {
    const int lane = fresh_lane();
    LAS v4f* p = (LAS v4f*)(L + L_SC + w * 2048 + lane * 32);
    v4f a = p[0], bb = p[1];
    float l = 0.f;
#pragma unroll
    for (int e = 0; e < 4; ++e) { a[e] = ex2(fmaf(a[e], CS, -mw)); bb[e] = ex2(fmaf(bb[e], CS, -mw)); l += a[e] + bb[e]; }
    p[0] = a; p[1] = bb;
    l = wave_sum(l);
    if (lane == 0) __builtin_amdgcn_raw_buffer_store_b128(v4u{__float_as_uint(mw), tagA, __float_as_uint(l), tagB}, rws, WS_ML + (((b * NH + h) * 2 + s) * 8 + w) * 16, 0, AUX_SC1);
  }
  LDS_FENCE();
  STAMP(11);

  v2f acc2[12];
#pragma unroll
  for (int e = 0; e < 12; ++e) acc2[e] = v2f{0.f, 0.f};
#define PIN_ACC() asm volatile("" : "+v"(acc2[0]), "+v"(acc2[1]), "+v"(acc2[2]), "+v"(acc2[3]), "+v"(acc2[4]), "+v"(acc2[5]), "+v"(acc2[6]), "+v"(acc2[7]), \
                                    "+v"(acc2[8]), "+v"(acc2[9]), "+v"(acc2[10]), "+v"(acc2[11]) : : "memory")
  auto v_tile = [&](const v4u& r0, const v4u& r1, const v4u& r2, float p) {
    const v2f pp = v2f{p, p};
#pragma unroll
    for (int d = 0; d < 4; ++d) {
      acc2[d] = __builtin_elementwise_fma(pp, v2f{bflo(r0[d]), bfhi(r0[d])}, acc2[d]);
      acc2[4 + d] = __builtin_elementwise_fma(pp, v2f{bflo(r1[d]), bfhi(r1[d])}, acc2[4 + d]);
      acc2[8 + d] = __builtin_elementwise_fma(pp, v2f{bflo(r2[d]), bfhi(r2[d])}, acc2[8 + d]);
    }
    PIN_ACC();
  };
#define V_USE(G, n) do { const v2f p2_ = *(LAS v2f*)(L + scoff + (n) * 64); v_tile(G.r[0][0], G.r[0][1], G.r[0][2], p2_[0]); v_tile(G.r[1][0], G.r[1][1], G.r[1][2], p2_[1]); } while (0)
#pragma unroll 1
  for (n = 0; n < 10; n += 2) {
    V_USE(B, n); wo_t = n < 8 ? n + 16 : -1; ISSUE(B, NGW + n + 2);
    V_USE(A, n + 1); wo_t = n < 8 ? n + 17 : -1; ISSUE(A, NGW + n + 3);
  }
  WO_COPY(16);
#pragma unroll 1
  for (; n < 20; n += 2) {
    V_USE(B, n); wo_t = n < 18 ? n + 14 : -1; ISSUE(B, NGW + n + 2);
    V_USE(A, n + 1); wo_t = n < 18 ? n + 15 : -1; ISSUE(A, NGW + n + 3);
  }
  WO_COPY(24);
  wo_t = -1;
  const int mloff = WS_ML + ((b * NH + h) * 16 + (fresh_lane() & 15)) * 16;
  v4u ml = poll16(rws, mloff);
  V_USE(B, n); ISSUE(B, NGW + n + 2); V_USE(A, n + 1); ISSUE(A, NGW + n + 3);
  for (unsigned spins = 0; !__all((ml[1] == tagA) & (ml[3] == tagB)); ) {
    if (++spins > SPIN_CAP) break;
    __builtin_amdgcn_s_sleep(2);
    ml = poll16(rws, mloff);
  }
  float fw;
  {
    const float m_i = __uint_as_float(ml[0]), l_i = __uint_as_float(ml[2]);
    float M = m_i;
    M = fmaxf(M, dpp<XOR1>(M)); M = fmaxf(M, dpp<XOR2>(M)); M = fmaxf(M, dpp<XOR7>(M)); M = fmaxf(M, dpp<XOR8>(M));
    float Ls = l_i * ex2(m_i - M);
    Ls = oct_sum(Ls); Ls += dpp<XOR8>(Ls);
    fw = __uint_as_float(__builtin_amdgcn_readfirstlane(__float_as_uint(ex2(mw - M) / Ls * *(LAS float*)(L + L_MISC))));
  }
#pragma unroll 1
  for (n += 2; n < NGW - 2; n += 2) { V_USE(B, n); ISSUE(B, NGW + n + 2); V_USE(A, n + 1); ISSUE(A, NGW + n + 3); }
  V_USE(B, NGW - 2);
  V_USE(A, NGW - 1);

  STAMP(7);
  TR(40);
  {
    float b12[12], c6[6];
#pragma unroll
    for (int i = 0; i < 12; ++i) {
      auto r = __builtin_amdgcn_permlane32_swap(__float_as_uint(acc2[i >> 1][i & 1]), __float_as_uint(acc2[6 + (i >> 1)][i & 1]), false, false);
      b12[i] = __uint_as_float(r[0]) + __uint_as_float(r[1]);
    }
#pragma unroll
    for (int i = 0; i < 6; ++i) {
      auto r = __builtin_amdgcn_permlane16_swap(__float_as_uint(b12[i]), __float_as_uint(b12[i + 6]), false, false);
      c6[i] = __uint_as_float(r[0]) + __uint_as_float(r[1]);
    }
#pragma unroll
    for (int i = 0; i < 6; ++i) c6[i] = (c6[i] + dpp<XOR8>(c6[i])) * fw;
    const int ln = fresh_lane();
    if ((ln & 8) == 0) {
      const int v0 = (ln >> 4) * 6;
#pragma unroll
      for (int p = 0; p < 3; ++p) { const int v = v0 + 2 * p; *(LAS v2f*)(L + L_QP + (w * QPS + (v >> 3) * 64 + (ln & 7) * 8 + (v & 7)) * 4) = v2f{c6[2 * p], c6[2 * p + 1]}; }
    }
  }
  const int ln = fresh_lane();
  const int yr = ln < 60 ? ln / 10 : 0, yc = ln - (ln / 10) * 10;
  float ya[8];
  { const LAS v4f* p = (const LAS v4f*)(L + L_YF + (w * 480 + yr * 80 + yc * 8) * 4); const v4f a = p[0], bb = p[1];
#pragma unroll
    for (int e = 0; e < 4; ++e) { ya[e] = a[e]; ya[4 + e] = bb[e]; } }
  STAMP(8);
  TR(41);
  __syncthreads();
  TR(42);
  if (w < 6) {
    const int pi = w * 16 + (ln >> 2), r = pi >> 4, u = pi & 15, qq = ln & 3;
    const LAS float* p = (const LAS float*)(L + L_QP) + 2 * qq * QPS + u * 12 + r;
    float a0 = p[0] + p[QPS], a1 = p[6] + p[QPS + 6];
    a0 += dpp<XOR1>(a0); a1 += dpp<XOR1>(a1); a0 += dpp<XOR2>(a0); a1 += dpp<XOR2>(a1);
    if (qq == 0) *(LAS unsigned*)(L + L_OB + pi * 4) = cvtpk(a0, a1);
  }
  TR(43);
  __syncthreads();
  TR(44);
  {
    LAS float* yrp = (LAS float*)(L + L_FP) + w * 480;
    const LAS v4u* op = (const LAS v4u*)(L + L_OB + yr * 64);
#pragma unroll
    for (int u4 = 0; u4 < 4; ++u4) {
      const v4u o4 = op[u4];
#pragma unroll
      for (int e = 0; e < 4; ++e) {
        const int u = u4 * 4 + e;
#pragma unroll
        for (int d = 0; d < 4; ++d) { ya[2 * d] = dot2(W[2 * u][d], o4[e], ya[2 * d]); ya[2 * d + 1] = dot2(W[2 * u + 1][d], o4[e], ya[2 * d + 1]); }
      }
    }
    asm volatile("" : "+v"(ya[0]), "+v"(ya[1]), "+v"(ya[2]), "+v"(ya[3]), "+v"(ya[4]), "+v"(ya[5]), "+v"(ya[6]), "+v"(ya[7]));
    TR(45);
    if (ln < 60) { LAS v4f* p = (LAS v4f*)(yrp + yr * 80 + yc * 8); p[0] = v4f{ya[0], ya[1], ya[2], ya[3]}; p[1] = v4f{ya[4], ya[5], ya[6], ya[7]}; }
    LDS_FENCE();
    TR(46);
    if (ln < 40) {
      v2f y = v2f{0.f, 0.f};
#pragma unroll
      for (int r = 0; r < 6; ++r) y += *(LAS v2f*)(yrp + r * 80 + ln * 2);
      const int cons = w * 8 + ln / 5, g5 = ln % 5;
      __builtin_amdgcn_raw_buffer_store_b128(v4u{__float_as_uint(y[0]), tagA, __float_as_uint(y[1]), tagB}, rws, WS_INBOX + (((b * 64 + cons) * 64 + j) * 5 + g5) * 16, 0, AUX_SC1);
    }
  }
  STAMP(9);
  TR(47);
  if (w == 0) {
    const int io = WS_INBOX + ((b * 64 + j) * 64 + ln) * 80;
    v4u g5[5];
    for (unsigned spins = 0;;) {
      bool ok = true;
#pragma unroll
      for (int e = 0; e < 5; ++e) g5[e] = poll16(rws, io + e * 16);
#pragma unroll
      for (int e = 0; e < 5; ++e) ok = ok & (g5[e][1] == tagA) & (g5[e][3] == tagB);
      if (__all(ok) || ++spins > SPIN_CAP) break;
      __builtin_amdgcn_s_sleep(1);
    }
    TR(48);
    const unsigned yv[10] = {g5[0][0], g5[0][2], g5[1][0], g5[1][2], g5[4][0], g5[2][0], g5[2][2], g5[3][0], g5[3][2], g5[4][2]};
    float b5[5];
#pragma unroll
    for (int i = 0; i < 5; ++i) { auto r = __builtin_amdgcn_permlane32_swap(yv[i], yv[5 + i], false, false); b5[i] = __uint_as_float(r[0]) + __uint_as_float(r[1]); }
    float z[3];
    { auto r = __builtin_amdgcn_permlane16_swap(__float_as_uint(b5[0]), __float_as_uint(b5[2]), false, false); z[0] = __uint_as_float(r[0]) + __uint_as_float(r[1]); }
    { auto r = __builtin_amdgcn_permlane16_swap(__float_as_uint(b5[1]), __float_as_uint(b5[3]), false, false); z[1] = __uint_as_float(r[0]) + __uint_as_float(r[1]); }
    { auto r = __builtin_amdgcn_permlane16_swap(__float_as_uint(b5[4]), __float_as_uint(b5[4]), false, false); z[2] = __uint_as_float(r[0]) + __uint_as_float(r[1]); }
#pragma unroll
    for (int i = 0; i < 3; ++i) { z[i] += dpp<XOR1>(z[i]); z[i] += dpp<XOR2>(z[i]); z[i] += dpp<XOR7>(z[i]); z[i] += dpp<XOR8>(z[i]); }
    auto r89 = __builtin_amdgcn_permlane32_swap(__float_as_uint(z[2]), __float_as_uint(z[2]), false, false);
    const unsigned o03 = cvtpk(z[0], z[1]), o4 = cvtpk(__uint_as_float(r89[0]), __uint_as_float(r89[1]));
    if ((ln & 15) == 0 || ln == 1) *(GAS unsigned*)(out + b * DM + j * 10 + (ln == 1 ? 8 : (ln >> 4) * 2)) = ln == 1 ? o4 : o03;
    if (b == 0 && j == 0 && ln == 0) __hip_atomic_store((GAS u64*)(ws + WS_EPOCH), ((u64)tagB << 32) | tagA, __ATOMIC_RELAXED, __HIP_MEMORY_SCOPE_AGENT);
  }
  TR(49);
  if (FIXSEL) while ((int)(now32() - (T0 - T0OFF + FIXDUR)) < 0) __builtin_amdgcn_s_sleep(8);
}
}

extern "C" void kernel_launch(void* const* d_in, const int* in_sizes, int n_in, void* d_out, int out_size, void* d_ws, size_t ws_size,
                              hipStream_t stream) {
  (void)in_sizes; (void)n_in; (void)out_size; (void)ws_size;
  hipLaunchKernelGGL(fused_decode, dim3(NWG), dim3(NTHR), 0, stream, (const u16*)d_in[0], (const u16*)d_in[1], (const u16*)d_in[2], (const u16*)d_in[3],
                     (const u16*)d_in[4], (const u16*)d_in[6], (const u16*)d_in[7], (const u16*)d_in[8], (const u16*)d_in[9], (u16*)d_out, (unsigned char*)d_ws);
}
```

```cpp
#include <hip/hip_runtime.h>
#include <hip/hip_bf16.h>
#include <stdint.h>

#define GAS __attribute__((address_space(1)))
#define LAS __attribute__((address_space(3)))
typedef unsigned v4u __attribute__((ext_vector_type(4)));
typedef unsigned v2u __attribute__((ext_vector_type(2)));
typedef float v4f __attribute__((ext_vector_type(4)));
typedef float v2f __attribute__((ext_vector_type(2)));
typedef __bf16 bf16x2 __attribute__((ext_vector_type(2)));
typedef unsigned long long u64;
typedef unsigned short u16;

namespace {
constexpr int NB = 4, NH = 32, SK = 8192, DH = 192, DM = 640, FF = 1536;
constexpr int NWG = 256, NWAVE = 8, NTHR = NWAVE * 64;
constexpr int KEYS_WG = SK / 2;
constexpr int ROWB = DH * 2;
constexpr int TILEB = 8 * ROWB;
constexpr int GRPB = 2 * TILEB;
constexpr int KVB = KEYS_WG * ROWB;
constexpr int NGW = KVB / GRPB / NWAVE;
constexpr int RSRC3 = 0x00020000;
#define POLL_AUX 16
#define STORE_AUX 16
constexpr int AUX_NT = 2, AUX_SC1 = STORE_AUX;
constexpr int OOB = 1 << 28;
constexpr float CS = 0.07216878364870322f * 1.4426950408889634f;
#define TAU16 (176 * 16)
#define WO_POLICY ""
#define W2_AUX 0
#define XHEAD16 (TAU16 * 10 / 10)
#define XPOLL16 (TAU16 * 3 / 10)
#define XFFN16 (TAU16 * 3 / 10)
#define XDMA16 (TAU16 * 3 / 100)
#define PHW 0

constexpr int WS_EPOCH = 0;
constexpr int WS_ML = 4096;
constexpr int WS_INBOX = 65536;
constexpr int WS_QX = WS_INBOX + NB * 64 * 64 * 80;
constexpr int WS_FX = WS_QX + NH * NB * 8 * 128 * 16;
constexpr int WS_BYTES = WS_FX + 8 * NB * 8 * 384 * 16;

constexpr int L_XS = 0;
constexpr int L_QP = 5120;
constexpr int QPS = 196;
constexpr int L_QB = L_QP + 8 * QPS * 4;
constexpr int L_OB = L_QB + 384;
constexpr int L_MISC = L_OB + 384;
constexpr int L_G = L_MISC + 256;
constexpr int L_YF = L_G + 3072;
constexpr int L_SC = L_YF + 8 * 480 * 4;
constexpr int L_FP2 = L_YF;
constexpr int L_FP = L_YF + 22 * 4 * 192 * 4;
constexpr int L_TR = L_FP + 22 * 4 * 192 * 4;
constexpr int L_BYTES = L_TR;

constexpr unsigned SPIN_CAP = 1u << 17;

__device__ __forceinline__ float dot2(unsigned a, unsigned b, float c) {
  return __builtin_amdgcn_fdot2_f32_bf16(__builtin_bit_cast(bf16x2, a), __builtin_bit_cast(bf16x2, b), c, false);
}
template <int CTRL> __device__ __forceinline__ float dpp(float x) {
  return __builtin_bit_cast(float, __builtin_amdgcn_mov_dpp(__builtin_bit_cast(int, x), CTRL, 0xf, 0xf, true));
}
constexpr int XOR1 = 0xB1, XOR2 = 0x4E, XOR7 = 0x141, XOR8 = 0x128;
__device__ __forceinline__ float sum16_32(float x) {
  auto s = __builtin_amdgcn_permlane16_swap(__float_as_uint(x), __float_as_uint(x), false, false);
  x = __uint_as_float(s[0]) + __uint_as_float(s[1]);
  auto t = __builtin_amdgcn_permlane32_swap(__float_as_uint(x), __float_as_uint(x), false, false);
  return __uint_as_float(t[0]) + __uint_as_float(t[1]);
}
__device__ __forceinline__ float max16_32(float x) {
  auto s = __builtin_amdgcn_permlane16_swap(__float_as_uint(x), __float_as_uint(x), false, false);
  x = fmaxf(__uint_as_float(s[0]), __uint_as_float(s[1]));
  auto t = __builtin_amdgcn_permlane32_swap(__float_as_uint(x), __float_as_uint(x), false, false);
  return fmaxf(__uint_as_float(t[0]), __uint_as_float(t[1]));
}
__device__ __forceinline__ float oct_sum(float x) { x += dpp<XOR1>(x); x += dpp<XOR2>(x); x += dpp<XOR7>(x); return x; }
__device__ __forceinline__ float xoct_sum(float x) { x += dpp<XOR8>(x); return sum16_32(x); }
__device__ __forceinline__ float wave_sum(float x) { return xoct_sum(oct_sum(x)); }
__device__ __forceinline__ unsigned cvtpk(float lo, float hi) { unsigned r; asm("v_cvt_pk_bf16_f32 %0, %1, %2" : "=v"(r) : "v"(lo), "v"(hi)); return r; }
__device__ __forceinline__ float bflo(unsigned u) { return __uint_as_float(u << 16); }
__device__ __forceinline__ float bfhi(unsigned u) { return __uint_as_float(u & 0xffff0000u); }
__device__ __forceinline__ float ex2(float x) { return __builtin_amdgcn_exp2f(x); }
__device__ __forceinline__ __amdgpu_buffer_rsrc_t rsrc(const void* p, int bytes) { return __builtin_amdgcn_make_buffer_rsrc((void*)p, (short)0, bytes, RSRC3); }
template <int AUX> __device__ __forceinline__ v4u ld16(__amdgpu_buffer_rsrc_t r, int voff, int soff) { return __builtin_amdgcn_raw_buffer_load_b128(r, voff, soff, AUX); }
template <int AUX> __device__ __forceinline__ v2u ld8(__amdgpu_buffer_rsrc_t r, int voff, int soff) { return __builtin_amdgcn_raw_buffer_load_b64(r, voff, soff, AUX); }
__device__ __forceinline__ unsigned now32() { return (unsigned)__builtin_amdgcn_s_memrealtime(); }
#define STAMP(i) do { } while (0)
#define TR(id) do { } while (0)
#define SCHED_FENCE() __builtin_amdgcn_sched_barrier(0)
#define LDS_FENCE() asm volatile("s_waitcnt lgkmcnt(0)" ::: "memory")
#define MEM_FENCE() asm volatile("" ::: "memory")
__device__ __forceinline__ v4u poll16(__amdgpu_buffer_rsrc_t r, int voff) { asm volatile("" : "+v"(voff) : : "memory"); return __builtin_amdgcn_raw_buffer_load_b128(r, voff, 0, POLL_AUX); }
__device__ __forceinline__ int fresh_lane() { int l; asm volatile("v_mbcnt_lo_u32_b32 %0, -1, 0\n\tv_mbcnt_hi_u32_b32 %0, -1, %0" : "=v"(l)); return l; }

__device__ __forceinline__ void dma16(__amdgpu_buffer_rsrc_t r, int voff, int soff, unsigned lds) {
  unsigned keep;
  asm volatile("s_mov_b32 %0, m0\n\ts_mov_b32 m0, %3\n\ts_nop 0\n\tbuffer_load_dwordx4 %1, %2, %4 offen " WO_POLICY " lds\n\ts_mov_b32 m0, %0"
               : "=&s"(keep) : "v"(voff), "s"(r), "s"(lds), "s"(soff) : "memory");
}
#define WQ_AUX AUX_NT
struct Grp { v4u r[2][3]; };

__global__ void __launch_bounds__(NTHR, 2)
fused_decode(const u16* __restrict__ x, const u16* __restrict__ Kc, const u16* __restrict__ Vc, const u16* __restrict__ wq,
             const u16* __restrict__ wo, const u16* __restrict__ w1, const u16* __restrict__ w3, const u16* __restrict__ w2,
             const u16* __restrict__ gw, u16* __restrict__ out, unsigned char* ws) {
  __shared__ __attribute__((aligned(16))) unsigned char lds_raw[L_BYTES];
  LAS unsigned char* L = (LAS unsigned char*)lds_raw;
  const int tid = threadIdx.x, lane = tid & 63, w = __builtin_amdgcn_readfirstlane(tid >> 6);
  const int k = lane >> 3, c = lane & 7;
  const int blk = blockIdx.x, xg = blk & 7, idx = blk >> 3;
  const int h = xg * 4 + (idx & 3), sh = idx >> 2, b = sh >> 1, s = sh & 1;
  const int j = h * 2 + s;
  STAMP(0);
#define PHASE 0
#define T0OFF 80
  unsigned T0 = now32() + T0OFF + idx * PHASE * (TAU16 / 32) / (16 * 32);
  const __amdgpu_buffer_rsrc_t rws = rsrc(ws, WS_BYTES);
  const v4u tiny = __builtin_nontemporal_load((const GAS v4u*)(tid == 320 ? (const unsigned char*)ws + WS_EPOCH : (const unsigned char*)x + (tid < 320 ? tid : 0) * 16));
  const int qr = tid / 24, qc = tid - qr * 24;
  v4u wqv[4], fwa, fwb;
  {
    const __amdgpu_buffer_rsrc_t rq = rsrc(wq + h * DH, (DM * NH * DH - h * DH) * 2);
    const int oob = tid < 480 ? 0 : OOB;
#pragma unroll
    for (int e = 0; e < 4; ++e) wqv[e] = ld16<WQ_AUX>(rq, (sh * 80 + qr * 4) * (NH * DH * 2) + qc * 16 + oob, e * (NH * DH * 2));
    const int row0 = (idx >> 1) * 40 + qr * 2;
    const __amdgpu_buffer_rsrc_t r13 = rsrc(((idx & 1) ? w3 : w1) + xg * 192, (DM * FF - xg * 192) * 2);
    fwa = ld16<WQ_AUX>(r13, row0 * (FF * 2) + qc * 16 + oob, 0); fwb = ld16<WQ_AUX>(r13, row0 * (FF * 2) + qc * 16 + oob, FF * 2);
  }
  const size_t kvoff = ((size_t)(b * NH + h) * SK + (size_t)s * KEYS_WG) * DH;
  const u16* Kb = Kc + kvoff; const u16* Vb = Vc + kvoff;
  const int kvo = w * GRPB + k * ROWB + c * 16;
  unsigned tn = T0 << 4;
  auto pace = [&](unsigned step16) { while ((int)((now32() << 4) - tn) < 0) __builtin_amdgcn_s_sleep(1); tn += step16; };
  int wo_t = -1;
  __amdgpu_buffer_rsrc_t ro = rsrc(wo + (size_t)h * DH * DM, DH * DM * 2);
  int wo_vo = 0;
  auto issue = [&](v4u& r00, v4u& r01, v4u& r02, v4u& r10, v4u& r11, v4u& r12, int m) {
    TR(1);
    pace(TAU16 + (wo_t >= 0 ? XDMA16 : 0));
    TR(2);
    if (wo_t >= 0) dma16(ro, wo_vo, wo_t * 6 * (DM * 2), (unsigned)(L_FP + w * 8192 + (wo_t & 7) * 1024));
    const __amdgpu_buffer_rsrc_t r = rsrc(m < NGW ? Kb : Vb, KVB);
    const int so = (m & (NGW - 1)) * (NWAVE * GRPB);
    r00 = ld16<AUX_NT>(r, kvo, so); r01 = ld16<AUX_NT>(r, kvo + 128, so); r02 = ld16<AUX_NT>(r, kvo + 256, so);
    r10 = ld16<AUX_NT>(r, kvo, so + TILEB); r11 = ld16<AUX_NT>(r, kvo + 128, so + TILEB); r12 = ld16<AUX_NT>(r, kvo + 256, so + TILEB);
    SCHED_FENCE();
    TR(3);
  };
#define ISSUE(G, m) issue(G.r[0][0], G.r[0][1], G.r[0][2], G.r[1][0], G.r[1][1], G.r[1][2], m)
#define ISSUE_W(o, m) issue(W[o], W[o + 1], W[o + 2], W[o + 3], W[o + 4], W[o + 5], m)
  Grp A, B;
  v4u W[32];
  ISSUE(A, 0);
  ISSUE(B, 1);
  tn += XHEAD16;
  STAMP(1);
  if (tid <= 320) *(LAS v4u*)(L + (tid < 320 ? L_XS + tid * 16 : L_MISC + 16)) = tiny;
  __syncthreads();
  const unsigned tagA = __builtin_amdgcn_readfirstlane(*(LAS unsigned*)(L + L_MISC + 16)) + 1u, tagB = __builtin_amdgcn_readfirstlane(*(LAS unsigned*)(L + L_MISC + 20)) + 1u;
  v2u xq[4]; unsigned fx4[4];
  { const int qrc = qr < 20 ? qr : 0;
#pragma unroll
    for (int e = 0; e < 4; ++e) { xq[e] = *(LAS v2u*)(L + L_XS + e * (DM * 2) + (sh * 80 + qrc * 4) * 2); fx4[e] = *(LAS unsigned*)(L + L_XS + e * (DM * 2) + ((idx >> 1) * 40 + qrc * 2) * 2); } }
  {
    unsigned lo01[4], hi01[4], lo23[4], hi23[4];
#pragma unroll
    for (int d = 0; d < 4; ++d) {
      lo01[d] = __builtin_amdgcn_perm(wqv[1][d], wqv[0][d], 0x05040100u); hi01[d] = __builtin_amdgcn_perm(wqv[1][d], wqv[0][d], 0x07060302u);
      lo23[d] = __builtin_amdgcn_perm(wqv[3][d], wqv[2][d], 0x05040100u); hi23[d] = __builtin_amdgcn_perm(wqv[3][d], wqv[2][d], 0x07060302u);
    }
#pragma unroll
    for (int bb = 0; bb < 4; ++bb) {
      float a[8];
#pragma unroll
      for (int d = 0; d < 4; ++d) { a[2 * d] = dot2(lo23[d], xq[bb][1], dot2(lo01[d], xq[bb][0], 0.f)); a[2 * d + 1] = dot2(hi23[d], xq[bb][1], dot2(hi01[d], xq[bb][0], 0.f)); }
      LAS v4f* p = (LAS v4f*)(L + L_FP + ((qr * 4 + bb) * 192 + qc * 8) * 4);
      p[0] = v4f{a[0], a[1], a[2], a[3]}; p[1] = v4f{a[4], a[5], a[6], a[7]};
    }
  }
  {
    unsigned lo[4], hi[4];
#pragma unroll
    for (int d = 0; d < 4; ++d) { lo[d] = __builtin_amdgcn_perm(fwb[d], fwa[d], 0x05040100u); hi[d] = __builtin_amdgcn_perm(fwb[d], fwa[d], 0x07060302u); }
#pragma unroll
    for (int bb = 0; bb < 4; ++bb) {
      LAS v4f* p = (LAS v4f*)(L + L_FP2 + ((qr * 4 + bb) * 192 + qc * 8) * 4);
      p[0] = v4f{dot2(lo[0], fx4[bb], 0.f), dot2(hi[0], fx4[bb], 0.f), dot2(lo[1], fx4[bb], 0.f), dot2(hi[1], fx4[bb], 0.f)};
      p[1] = v4f{dot2(lo[2], fx4[bb], 0.f), dot2(hi[2], fx4[bb], 0.f), dot2(lo[3], fx4[bb], 0.f), dot2(hi[3], fx4[bb], 0.f)};
    }
  }
  TR(9);
  __syncthreads();
  TR(10);
  STAMP(2);
  if (tid < 384) {
    const int bb = tid / 96, p = tid - bb * 96;
    float a0 = 0.f, a1 = 0.f;
#pragma unroll
    for (int r = 0; r < 20; ++r) { const v2f v = *(LAS v2f*)(L + L_FP + ((r * 4 + bb) * 192 + p * 2) * 4); a0 += v[0]; a1 += v[1]; }
    __builtin_amdgcn_raw_buffer_store_b128(v4u{__float_as_uint(a0), tagA, __float_as_uint(a1), tagB}, rws, WS_QX + (((h * 4 + bb) * 8 + sh) * 128 + p) * 16, 0, AUX_SC1);
    a0 = 0.f; a1 = 0.f;
#pragma unroll
    for (int r = 0; r < 20; ++r) { const v2f v = *(LAS v2f*)(L + L_FP2 + ((r * 4 + bb) * 192 + p * 2) * 4); a0 += v[0]; a1 += v[1]; }
    __builtin_amdgcn_raw_buffer_store_b128(v4u{__float_as_uint(a0), tagA, __float_as_uint(a1), tagB}, rws,
                                           WS_FX + (((((xg * 4 + bb) * 8 + p / 12) * 2 + (idx & 1)) * 16 + (idx >> 1)) * 12 + p % 12) * 16, 0, AUX_SC1);
  }
  if (tid >= 384 && tid < 388)
    __builtin_amdgcn_raw_buffer_store_b128(v4u{T0, tagA, T0, tagB}, rws, WS_QX + (((h * 4 + (tid - 384)) * 8 + sh) * 128 + 96) * 16, 0, AUX_SC1);
  SCHED_FENCE();
  TR(11);
  ISSUE_W(0, 2);
  ISSUE_W(6, 3);
  ISSUE_W(12, 4);
  TR(12);
  if (w == 0) {
    const int qo = WS_QX + (h * 4 + b) * 8 * 2048 + lane * 16;
    v4u ga[8], gb[8];
    for (unsigned spins = 0;;) {
#pragma unroll
      for (int p = 0; p < 8; ++p) { ga[p] = poll16(rws, qo + p * 2048); gb[p] = poll16(rws, qo + p * 2048 + 1024 + (lane <= 32 ? 0 : OOB)); }
      bool ok = true;
#pragma unroll
      for (int p = 0; p < 8; ++p) ok = ok & (ga[p][1] == tagA) & (ga[p][3] == tagB) & ((lane > 32) | ((gb[p][1] == tagA) & (gb[p][3] == tagB)));
      TR(17);
      if (__all(ok) || ++spins > SPIN_CAP) break;
      __builtin_amdgcn_s_sleep(1);
    }
    TR(16);
    float a0 = 0.f, a1 = 0.f, b0 = 0.f, b1 = 0.f;
#pragma unroll
    for (int p = 0; p < 8; ++p) { a0 += __uint_as_float(ga[p][0]); a1 += __uint_as_float(ga[p][2]); b0 += __uint_as_float(gb[p][0]); b1 += __uint_as_float(gb[p][2]); }
    *(LAS unsigned*)(L + L_QB + lane * 4) = cvtpk(a0, a1);
    if (lane < 32) *(LAS unsigned*)(L + L_QB + 256 + lane * 4) = cvtpk(b0, b1);
    unsigned tl = gb[0][0];
#pragma unroll
    for (int p = 1; p < 8; ++p) tl = (int)(gb[p][0] - tl) > 0 ? gb[p][0] : tl;
    if (lane == 32) *(LAS unsigned*)(L + L_MISC + 8) = tl;
  }
  TR(13);
  tn += XPOLL16;
  ISSUE_W(18, 5);
  ISSUE_W(24, 6);
  __syncthreads();
  TR(14);
  STAMP(3);
  { const unsigned tl = __builtin_amdgcn_readfirstlane(*(LAS unsigned*)(L + L_MISC + 8)); if (tl - T0 < 1000u) tn += (tl - T0) << 4; }
#define SLACK 3
  { const unsigned t = (now32() << 4) - SLACK * TAU16; if ((int)(t - tn) > 0) tn = t; }
  v4u Q[3];
#pragma unroll
  for (int i = 0; i < 3; ++i) Q[i] = *(LAS v4u*)(L + L_QB + (c + 8 * i) * 16);

  float mx = -INFINITY;
  const bool oct0 = c == 0;
  const int scoff = L_SC + w * 2048 + k * 8;
  auto k_tile = [&](const v4u& r0, const v4u& r1, const v4u& r2) -> float {
    float a0 = dot2(r0[0], Q[0][0], 0.f), a1 = dot2(r1[0], Q[1][0], 0.f), a2 = dot2(r2[0], Q[2][0], 0.f);
#pragma unroll
    for (int d = 1; d < 4; ++d) { a0 = dot2(r0[d], Q[0][d], a0); a1 = dot2(r1[d], Q[1][d], a1); a2 = dot2(r2[d], Q[2][d], a2); }
    return (a0 + a1) + a2;
  };
  auto k_put = [&](float s0, float s1, int n) {
    s0 = oct_sum(s0); s1 = oct_sum(s1);
    mx = fmaxf(mx, fmaxf(s0, s1));
    if (oct0) *(LAS v2f*)(L + scoff + n * 64) = v2f{s0, s1};
  };
#define K_STEP(G, n, m) do { float s0_ = k_tile(G.r[0][0], G.r[0][1], G.r[0][2]), s1_ = k_tile(G.r[1][0], G.r[1][1], G.r[1][2]); \
    asm volatile("" : "+v"(s0_), "+v"(s1_) : : "memory"); ISSUE(G, m); k_put(s0_, s1_, n); } while (0)
  K_STEP(A, 0, 7);
#pragma unroll
  for (int g = 0; g < 5; ++g) k_put(k_tile(W[g * 6], W[g * 6 + 1], W[g * 6 + 2]), k_tile(W[g * 6 + 3], W[g * 6 + 4], W[g * 6 + 5]), 2 + g);
  STAMP(4);
  K_STEP(B, 1, 8);
  STAMP(5);
  {
    const int lane = fresh_lane(), tid2 = w * 64 + lane;
    const int yr = lane / 10, yc = lane - yr * 10;
    const int fxo = WS_FX + ((xg * 4 + b) * 8 + (j & 7)) * 6144 + tid2 * 16 + (tid2 < 384 ? 0 : OOB);
    v4u fx = poll16(rws, fxo);
    const __amdgpu_buffer_rsrc_t r2 = rsrc(w2, FF * DM * 2), rg = rsrc(gw, w == 0 ? DM * 2 : 0);
    const int vo2 = (j * 24 + yr) * (DM * 2) + w * 160 + yc * 16 + (lane < 60 ? 0 : OOB);
#pragma unroll
    for (int t = 0; t < 4; ++t) W[8 + t] = ld16<W2_AUX>(r2, vo2, t * 6 * (DM * 2));
    const v4u gg0 = ld16<AUX_NT>(rg, lane * 16, 0), gg1 = ld16<AUX_NT>(rg, 1024 + lane * 16, 0);
    SCHED_FENCE();
    TR(30);
    tn += XFFN16;
    K_STEP(A, 7, 9); K_STEP(B, 8, 10);
    TR(31);
    for (unsigned spins = 0; !__all((tid2 >= 384) | ((fx[1] == tagA) & (fx[3] == tagB))); ) {
      if (++spins > SPIN_CAP) break;
      __builtin_amdgcn_s_sleep(2);
      fx = poll16(rws, fxo);
    }
    TR(32);
    if (tid2 < 384) *(LAS v2f*)(L + L_G + ((tid2 / 12) * 24 + (tid2 % 12) * 2) * 4) = v2f{__uint_as_float(fx[0]), __uint_as_float(fx[2])};
    float gate = 0.f;
    if (w == 0) {
      const v4u gx0 = *(LAS v4u*)(L + L_XS + b * (DM * 2) + lane * 16), gx1 = *(LAS v4u*)(L + L_XS + b * (DM * 2) + 1024 + (lane & 15) * 16);
      float d = 0.f;
#pragma unroll
      for (int e = 0; e < 4; ++e) { d = dot2(gx0[e], gg0[e], d); d = dot2(gx1[e], gg1[e], d); }
      gate = 1.f / (1.f + __expf(-wave_sum(d)));
      if (lane == 0) *(LAS float*)(L + L_MISC) = gate;
    }
    __syncthreads();
    TR(33);
    if (tid2 < 24) {
      const LAS float* g = (const LAS float*)(L + L_G) + tid2;
      float ga = 0.f, up = 0.f;
#pragma unroll
      for (int r = 0; r < 16; ++r) { ga += g[r * 24]; up += g[(16 + r) * 24]; }
      *(LAS float*)(L + L_MISC + 64 + tid2 * 4) = ga / (1.f + __expf(-ga)) * up * (1.f - gate);
    }
    __syncthreads();
    TR(34);
    v2f ya[4];
#pragma unroll
    for (int e = 0; e < 4; ++e) ya[e] = v2f{0.f, 0.f};
#pragma unroll
    for (int t = 0; t < 4; ++t) {
      const float hv = *(LAS float*)(L + L_MISC + 64 + (t * 6 + (yr < 6 ? yr : 0)) * 4);
#pragma unroll
      for (int d = 0; d < 4; ++d) ya[d] = __builtin_elementwise_fma(v2f{hv, hv}, v2f{bflo(W[8 + t][d]), bfhi(W[8 + t][d])}, ya[d]);
    }
    if (lane < 60) {
      LAS v4f* p = (LAS v4f*)(L + L_YF + (w * 480 + yr * 80 + yc * 8) * 4);
      p[0] = v4f{ya[0][0], ya[0][1], ya[1][0], ya[1][1]}; p[1] = v4f{ya[2][0], ya[2][1], ya[3][0], ya[3][1]};
    }
    wo_vo = yr * (DM * 2) + w * 160 + yc * 16 + (lane < 60 ? 0 : OOB);
  }
  MEM_FENCE();
  TR(35);
  K_STEP(A, 9, 11); K_STEP(B, 10, 12);
  STAMP(13);
#define WO_COPY(t0) do { asm volatile("s_waitcnt vmcnt(12)" ::: "memory"); const int ln_ = fresh_lane(); \
    _Pragma("unroll") for (int t = 0; t < 8; t += 2) {     \
      const v4u a_ = *(LAS v4u*)(L + L_FP + w * 8192 + t * 1024 + ln_ * 16), b_ = *(LAS v4u*)(L + L_FP + w * 8192 + (t + 1) * 1024 + ln_ * 16); \
      _Pragma("unroll") for (int d = 0; d < 4; ++d) { W[t0 + t][d] = __builtin_amdgcn_perm(b_[d], a_[d], 0x05040100u); W[t0 + t + 1][d] = __builtin_amdgcn_perm(b_[d], a_[d], 0x07060302u); } } \
    asm volatile("" : "+v"(W[t0]), "+v"(W[t0 + 1]), "+v"(W[t0 + 2]), "+v"(W[t0 + 3]), "+v"(W[t0 + 4]), "+v"(W[t0 + 5]), "+v"(W[t0 + 6]), "+v"(W[t0 + 7]) : : "memory"); } while (0)
  int n;
#pragma unroll 1
  for (n = 11; n < 21; n += 2) {
    wo_t = n < 19 ? n - 11 : -1; K_STEP(A, n, n + 2);
    wo_t = n < 19 ? n - 10 : -1; K_STEP(B, n + 1, n + 3);
  }
  WO_COPY(0);
#pragma unroll 1
  for (; n < 31; n += 2) {
    wo_t = n < 29 ? n - 13 : -1; K_STEP(A, n, n + 2);
    wo_t = n < 29 ? n - 12 : -1; K_STEP(B, n + 1, n + 3);
  }
  WO_COPY(8);
  wo_t = -1;
  K_STEP(A, 31, 33);

  STAMP(6);
  mx = fmaxf(mx, dpp<XOR8>(mx)); mx = max16_32(mx);
  const float mw = mx * CS;
  LDS_FENCE();
  {
    const int lane = fresh_lane();
    LAS v4f* p = (LAS v4f*)(L + L_SC + w * 2048 + lane * 32);
    v4f a = p[0], bb = p[1];
    float l = 0.f;
#pragma unroll
    for (int e = 0; e < 4; ++e) { a[e] = ex2(fmaf(a[e], CS, -mw)); bb[e] = ex2(fmaf(bb[e], CS, -mw)); l += a[e] + bb[e]; }
    p[0] = a; p[1] = bb;
    l = wave_sum(l);
    if (lane == 0) __builtin_amdgcn_raw_buffer_store_b128(v4u{__float_as_uint(mw), tagA, __float_as_uint(l), tagB}, rws, WS_ML + (((b * NH + h) * 2 + s) * 8 + w) * 16, 0, AUX_SC1);
  }
  LDS_FENCE();
  STAMP(11);

  v2f acc2[12];
#pragma unroll
  for (int e = 0; e < 12; ++e) acc2[e] = v2f{0.f, 0.f};
#define PIN_ACC() asm volatile("" : "+v"(acc2[0]), "+v"(acc2[1]), "+v"(acc2[2]), "+v"(acc2[3]), "+v"(acc2[4]), "+v"(acc2[5]), "+v"(acc2[6]), "+v"(acc2[7]), \
                                    "+v"(acc2[8]), "+v"(acc2[9]), "+v"(acc2[10]), "+v"(acc2[11]) : : "memory")
  auto v_tile = [&](const v4u& r0, const v4u& r1, const v4u& r2, float p) {
    const v2f pp = v2f{p, p};
#pragma unroll
    for (int d = 0; d < 4; ++d) {
      acc2[d] = __builtin_elementwise_fma(pp, v2f{bflo(r0[d]), bfhi(r0[d])}, acc2[d]);
      acc2[4 + d] = __builtin_elementwise_fma(pp, v2f{bflo(r1[d]), bfhi(r1[d])}, acc2[4 + d]);
      acc2[8 + d] = __builtin_elementwise_fma(pp, v2f{bflo(r2[d]), bfhi(r2[d])}, acc2[8 + d]);
    }
    PIN_ACC();
  };
#define V_USE(G, n) do { const v2f p2_ = *(LAS v2f*)(L + scoff + (n) * 64); v_tile(G.r[0][0], G.r[0][1], G.r[0][2], p2_[0]); v_tile(G.r[1][0], G.r[1][1], G.r[1][2], p2_[1]); } while (0)
#pragma unroll 1
  for (n = 0; n < 10; n += 2) {
    V_USE(B, n); wo_t = n < 8 ? n + 16 : -1; ISSUE(B, NGW + n + 2);
    V_USE(A, n + 1); wo_t = n < 8 ? n + 17 : -1; ISSUE(A, NGW + n + 3);
  }
  WO_COPY(16);
#pragma unroll 1
  for (; n < 20; n += 2) {
    V_USE(B, n); wo_t = n < 18 ? n + 14 : -1; ISSUE(B, NGW + n + 2);
    V_USE(A, n + 1); wo_t = n < 18 ? n + 15 : -1; ISSUE(A, NGW + n + 3);
  }
  WO_COPY(24);
  wo_t = -1;
  const int mloff = WS_ML + ((b * NH + h) * 16 + (fresh_lane() & 15)) * 16;
  v4u ml = poll16(rws, mloff);
  V_USE(B, n); ISSUE(B, NGW + n + 2); V_USE(A, n + 1); ISSUE(A, NGW + n + 3);
  for (unsigned spins = 0; !__all((ml[1] == tagA) & (ml[3] == tagB)); ) {
    if (++spins > SPIN_CAP) break;
    __builtin_amdgcn_s_sleep(2);
    ml = poll16(rws, mloff);
  }
  float fw;
  {
    const float m_i = __uint_as_float(ml[0]), l_i = __uint_as_float(ml[2]);
    float M = m_i;
    M = fmaxf(M, dpp<XOR1>(M)); M = fmaxf(M, dpp<XOR2>(M)); M = fmaxf(M, dpp<XOR7>(M)); M = fmaxf(M, dpp<XOR8>(M));
    float Ls = l_i * ex2(m_i - M);
    Ls = oct_sum(Ls); Ls += dpp<XOR8>(Ls);
    fw = __uint_as_float(__builtin_amdgcn_readfirstlane(__float_as_uint(ex2(mw - M) / Ls * *(LAS float*)(L + L_MISC))));
  }
#pragma unroll 1
  for (n += 2; n < NGW - 2; n += 2) { V_USE(B, n); ISSUE(B, NGW + n + 2); V_USE(A, n + 1); ISSUE(A, NGW + n + 3); }
  V_USE(B, NGW - 2);
  V_USE(A, NGW - 1);

  STAMP(7);
  TR(40);
  {
    float b12[12], c6[6];
#pragma unroll
    for (int i = 0; i < 12; ++i) {
      auto r = __builtin_amdgcn_permlane32_swap(__float_as_uint(acc2[i >> 1][i & 1]), __float_as_uint(acc2[6 + (i >> 1)][i & 1]), false, false);
      b12[i] = __uint_as_float(r[0]) + __uint_as_float(r[1]);
    }
#pragma unroll
    for (int i = 0; i < 6; ++i) {
      auto r = __builtin_amdgcn_permlane16_swap(__float_as_uint(b12[i]), __float_as_uint(b12[i + 6]), false, false);
      c6[i] = __uint_as_float(r[0]) + __uint_as_float(r[1]);
    }
#pragma unroll
    for (int i = 0; i < 6; ++i) c6[i] = (c6[i] + dpp<XOR8>(c6[i])) * fw;
    const int ln = fresh_lane();
    if ((ln & 8) == 0) {
      const int v0 = (ln >> 4) * 6;
#pragma unroll
      for (int p = 0; p < 3; ++p) { const int v = v0 + 2 * p; *(LAS v2f*)(L + L_QP + (w * QPS + (v >> 3) * 64 + (ln & 7) * 8 + (v & 7)) * 4) = v2f{c6[2 * p], c6[2 * p + 1]}; }
    }
  }
  const int ln = fresh_lane();
  const int yr = ln < 60 ? ln / 10 : 0, yc = ln - (ln / 10) * 10;
  float ya[8];
  { const LAS v4f* p = (const LAS v4f*)(L + L_YF + (w * 480 + yr * 80 + yc * 8) * 4); const v4f a = p[0], bb = p[1];
#pragma unroll
    for (int e = 0; e < 4; ++e) { ya[e] = a[e]; ya[4 + e] = bb[e]; } }
  STAMP(8);
  TR(41);
  __syncthreads();
  TR(42);
  if (w < 6) {
    const int pi = w * 16 + (ln >> 2), r = pi >> 4, u = pi & 15, qq = ln & 3;
    const LAS float* p = (const LAS float*)(L + L_QP) + 2 * qq * QPS + u * 12 + r;
    float a0 = p[0] + p[QPS], a1 = p[6] + p[QPS + 6];
    a0 += dpp<XOR1>(a0); a1 += dpp<XOR1>(a1); a0 += dpp<XOR2>(a0); a1 += dpp<XOR2>(a1);
    if (qq == 0) *(LAS unsigned*)(L + L_OB + pi * 4) = cvtpk(a0, a1);
  }
  TR(43);
  __syncthreads();
  TR(44);
  {
    LAS float* yrp = (LAS float*)(L + L_FP) + w * 480;
    const LAS v4u* op = (const LAS v4u*)(L + L_OB + yr * 64);
#pragma unroll
    for (int u4 = 0; u4 < 4; ++u4) {
      const v4u o4 = op[u4];
#pragma unroll
      for (int e = 0; e < 4; ++e) {
        const int u = u4 * 4 + e;
#pragma unroll
        for (int d = 0; d < 4; ++d) { ya[2 * d] = dot2(W[2 * u][d], o4[e], ya[2 * d]); ya[2 * d + 1] = dot2(W[2 * u + 1][d], o4[e], ya[2 * d + 1]); }
      }
    }
    asm volatile("" : "+v"(ya[0]), "+v"(ya[1]), "+v"(ya[2]), "+v"(ya[3]), "+v"(ya[4]), "+v"(ya[5]), "+v"(ya[6]), "+v"(ya[7]));
    TR(45);
    if (ln < 60) { LAS v4f* p = (LAS v4f*)(yrp + yr * 80 + yc * 8); p[0] = v4f{ya[0], ya[1], ya[2], ya[3]}; p[1] = v4f{ya[4], ya[5], ya[6], ya[7]}; }
    LDS_FENCE();
    TR(46);
    if (ln < 40) {
      v2f y = v2f{0.f, 0.f};
#pragma unroll
      for (int r = 0; r < 6; ++r) y += *(LAS v2f*)(yrp + r * 80 + ln * 2);
      const int cons = w * 8 + ln / 5, g5 = ln % 5;
      __builtin_amdgcn_raw_buffer_store_b128(v4u{__float_as_uint(y[0]), tagA, __float_as_uint(y[1]), tagB}, rws, WS_INBOX + (((b * 64 + cons) * 64 + j) * 5 + g5) * 16, 0, AUX_SC1);
    }
  }
  STAMP(9);
  TR(47);
  if (w == 0) {
    const int io = WS_INBOX + ((b * 64 + j) * 64 + ln) * 80;
    v4u g5[5];
    for (unsigned spins = 0;;) {
      bool ok = true;
#pragma unroll
      for (int e = 0; e < 5; ++e) g5[e] = poll16(rws, io + e * 16);
#pragma unroll
      for (int e = 0; e < 5; ++e) ok = ok & (g5[e][1] == tagA) & (g5[e][3] == tagB);
      if (__all(ok) || ++spins > SPIN_CAP) break;
      __builtin_amdgcn_s_sleep(1);
    }
    TR(48);
    const unsigned yv[10] = {g5[0][0], g5[0][2], g5[1][0], g5[1][2], g5[4][0], g5[2][0], g5[2][2], g5[3][0], g5[3][2], g5[4][2]};
    float b5[5];
#pragma unroll
    for (int i = 0; i < 5; ++i) { auto r = __builtin_amdgcn_permlane32_swap(yv[i], yv[5 + i], false, false); b5[i] = __uint_as_float(r[0]) + __uint_as_float(r[1]); }
    float z[3];
    { auto r = __builtin_amdgcn_permlane16_swap(__float_as_uint(b5[0]), __float_as_uint(b5[2]), false, false); z[0] = __uint_as_float(r[0]) + __uint_as_float(r[1]); }
    { auto r = __builtin_amdgcn_permlane16_swap(__float_as_uint(b5[1]), __float_as_uint(b5[3]), false, false); z[1] = __uint_as_float(r[0]) + __uint_as_float(r[1]); }
    { auto r = __builtin_amdgcn_permlane16_swap(__float_as_uint(b5[4]), __float_as_uint(b5[4]), false, false); z[2] = __uint_as_float(r[0]) + __uint_as_float(r[1]); }
#pragma unroll
    for (int i = 0; i < 3; ++i) { z[i] += dpp<XOR1>(z[i]); z[i] += dpp<XOR2>(z[i]); z[i] += dpp<XOR7>(z[i]); z[i] += dpp<XOR8>(z[i]); }
    auto r89 = __builtin_amdgcn_permlane32_swap(__float_as_uint(z[2]), __float_as_uint(z[2]), false, false);
    const unsigned o03 = cvtpk(z[0], z[1]), o4 = cvtpk(__uint_as_float(r89[0]), __uint_as_float(r89[1]));
    if ((ln & 15) == 0 || ln == 1) *(GAS unsigned*)(out + b * DM + j * 10 + (ln == 1 ? 8 : (ln >> 4) * 2)) = ln == 1 ? o4 : o03;
    if (b == 0 && j == 0 && ln == 0) __hip_atomic_store((GAS u64*)(ws + WS_EPOCH), ((u64)tagB << 32) | tagA, __ATOMIC_RELAXED, __HIP_MEMORY_SCOPE_AGENT);
  }
  TR(49);
}
}

extern "C" void kernel_launch(void* const* d_in, const int* in_sizes, int n_in, void* d_out, int out_size, void* d_ws, size_t ws_size,
                              hipStream_t stream) {
  (void)in_sizes; (void)n_in; (void)out_size; (void)ws_size;
  hipLaunchKernelGGL(fused_decode, dim3(NWG), dim3(NTHR), 0, stream, (const u16*)d_in[0], (const u16*)d_in[1], (const u16*)d_in[2], (const u16*)d_in[3],
                     (const u16*)d_in[4], (const u16*)d_in[6], (const u16*)d_in[7], (const u16*)d_in[8], (const u16*)d_in[9], (u16*)d_out, (unsigned char*)d_ws);
}
```

```cpp
#include <hip/hip_runtime.h>
#include <hip/hip_bf16.h>
#include <stdint.h>

#define GAS __attribute__((address_space(1)))
#define LAS __attribute__((address_space(3)))
typedef unsigned v4u __attribute__((ext_vector_type(4)));
typedef unsigned v2u __attribute__((ext_vector_type(2)));
typedef float v4f __attribute__((ext_vector_type(4)));
typedef float v2f __attribute__((ext_vector_type(2)));
typedef __bf16 bf16x2 __attribute__((ext_vector_type(2)));
typedef unsigned long long u64;
typedef unsigned short u16;

namespace {
constexpr int NB = 4, NH = 32, SK = 8192, DH = 192, DM = 640, FF = 1536;
constexpr int NWG = 256, NWAVE = 8, NTHR = NWAVE * 64;
constexpr int KEYS_WG = SK / 2;
constexpr int ROWB = DH * 2;
constexpr int TILEB = 8 * ROWB;
constexpr int GRPB = 2 * TILEB;
constexpr int KVB = KEYS_WG * ROWB;
constexpr int NGW = KVB / GRPB / NWAVE;
constexpr int RSRC3 = 0x00020000;
#define POLL_AUX 16
#define STORE_AUX 16
constexpr int AUX_NT = 2, AUX_SC1 = STORE_AUX;
constexpr int OOB = 1 << 28;
constexpr float CS = 0.07216878364870322f * 1.4426950408889634f;
#define TAU16 (173 * 16)
#define XHEAD16 (TAU16 * 10 / 10)
#define XPOLL16 (TAU16 * 3 / 10)
#define XFFN16 (TAU16 * 3 / 10)
#define XDMA16 (TAU16 * 3 / 100)
#define PHW 0

constexpr int WS_EPOCH = 0;
constexpr int WS_ML = 4096;
constexpr int WS_INBOX = 65536;
constexpr int WS_QX = WS_INBOX + NB * 64 * 64 * 80;
constexpr int WS_FX = WS_QX + NH * NB * 8 * 128 * 16;
constexpr int WS_BYTES = WS_FX + 8 * NB * 8 * 384 * 16;

constexpr int L_XS = 0;
constexpr int L_QP = 5120;
constexpr int QPS = 196;
constexpr int L_QB = L_QP + 8 * QPS * 4;
constexpr int L_OB = L_QB + 384;
constexpr int L_MISC = L_OB + 384;
constexpr int L_G = L_MISC + 256;
constexpr int L_YF = L_G + 3072;
constexpr int L_SC = L_YF + 8 * 480 * 4;
constexpr int L_FP2 = L_YF;
constexpr int L_FP = L_YF + 22 * 4 * 192 * 4;
constexpr int L_TR = L_FP + 22 * 4 * 192 * 4;
constexpr int L_BYTES = L_TR;

constexpr unsigned SPIN_CAP = 1u << 17;

__device__ __forceinline__ float dot2(unsigned a, unsigned b, float c) {
  return __builtin_amdgcn_fdot2_f32_bf16(__builtin_bit_cast(bf16x2, a), __builtin_bit_cast(bf16x2, b), c, false);
}
template <int CTRL> __device__ __forceinline__ float dpp(float x) {
  return __builtin_bit_cast(float, __builtin_amdgcn_mov_dpp(__builtin_bit_cast(int, x), CTRL, 0xf, 0xf, true));
}
constexpr int XOR1 = 0xB1, XOR2 = 0x4E, XOR7 = 0x141, XOR8 = 0x128;
__device__ __forceinline__ float sum16_32(float x) {
  auto s = __builtin_amdgcn_permlane16_swap(__float_as_uint(x), __float_as_uint(x), false, false);
  x = __uint_as_float(s[0]) + __uint_as_float(s[1]);
  auto t = __builtin_amdgcn_permlane32_swap(__float_as_uint(x), __float_as_uint(x), false, false);
  return __uint_as_float(t[0]) + __uint_as_float(t[1]);
}
__device__ __forceinline__ float max16_32(float x) {
  auto s = __builtin_amdgcn_permlane16_swap(__float_as_uint(x), __float_as_uint(x), false, false);
  x = fmaxf(__uint_as_float(s[0]), __uint_as_float(s[1]));
  auto t = __builtin_amdgcn_permlane32_swap(__float_as_uint(x), __float_as_uint(x), false, false);
  return fmaxf(__uint_as_float(t[0]), __uint_as_float(t[1]));
}
__device__ __forceinline__ float oct_sum(float x) { x += dpp<XOR1>(x); x += dpp<XOR2>(x); x += dpp<XOR7>(x); return x; }
__device__ __forceinline__ float xoct_sum(float x) { x += dpp<XOR8>(x); return sum16_32(x); }
__device__ __forceinline__ float wave_sum(float x) { return xoct_sum(oct_sum(x)); }
__device__ __forceinline__ unsigned cvtpk(float lo, float hi) { unsigned r; asm("v_cvt_pk_bf16_f32 %0, %1, %2" : "=v"(r) : "v"(lo), "v"(hi)); return r; }
__device__ __forceinline__ float bflo(unsigned u) { return __uint_as_float(u << 16); }
__device__ __forceinline__ float bfhi(unsigned u) { return __uint_as_float(u & 0xffff0000u); }
__device__ __forceinline__ float ex2(float x) { return __builtin_amdgcn_exp2f(x); }
__device__ __forceinline__ __amdgpu_buffer_rsrc_t rsrc(const void* p, int bytes) { return __builtin_amdgcn_make_buffer_rsrc((void*)p, (short)0, bytes, RSRC3); }
template <int AUX> __device__ __forceinline__ v4u ld16(__amdgpu_buffer_rsrc_t r, int voff, int soff) { return __builtin_amdgcn_raw_buffer_load_b128(r, voff, soff, AUX); }
template <int AUX> __device__ __forceinline__ v2u ld8(__amdgpu_buffer_rsrc_t r, int voff, int soff) { return __builtin_amdgcn_raw_buffer_load_b64(r, voff, soff, AUX); }
__device__ __forceinline__ unsigned now32() { return (unsigned)__builtin_amdgcn_s_memrealtime(); }
#define STAMP(i) do { } while (0)
#define TR(id) do { } while (0)
#define SCHED_FENCE() __builtin_amdgcn_sched_barrier(0)
#define LDS_FENCE() asm volatile("s_waitcnt lgkmcnt(0)" ::: "memory")
#define MEM_FENCE() asm volatile("" ::: "memory")
__device__ __forceinline__ v4u poll16(__amdgpu_buffer_rsrc_t r, int voff) { asm volatile("" : "+v"(voff) : : "memory"); return __builtin_amdgcn_raw_buffer_load_b128(r, voff, 0, POLL_AUX); }
__device__ __forceinline__ int fresh_lane() { int l; asm volatile("v_mbcnt_lo_u32_b32 %0, -1, 0\n\tv_mbcnt_hi_u32_b32 %0, -1, %0" : "=v"(l)); return l; }

#define WO_POLICY ""
__device__ __forceinline__ void dma16(__amdgpu_buffer_rsrc_t r, int voff, int soff, unsigned lds) {
  unsigned keep;
  asm volatile("s_mov_b32 %0, m0\n\ts_mov_b32 m0, %3\n\ts_nop 0\n\tbuffer_load_dwordx4 %1, %2, %4 offen " WO_POLICY " lds\n\ts_mov_b32 m0, %0"
               : "=&s"(keep) : "v"(voff), "s"(r), "s"(lds), "s"(soff) : "memory");
}
#define KV_AUX AUX_NT
#define WQ_AUX AUX_NT
#define W2_AUX 0
struct Grp { v4u r[2][3]; };

__global__ void __launch_bounds__(NTHR, 2)
fused_decode(const u16* __restrict__ x, const u16* __restrict__ Kc, const u16* __restrict__ Vc, const u16* __restrict__ wq,
             const u16* __restrict__ wo, const u16* __restrict__ w1, const u16* __restrict__ w3, const u16* __restrict__ w2,
             const u16* __restrict__ gw, u16* __restrict__ out, unsigned char* ws) {
  __shared__ __attribute__((aligned(16))) unsigned char lds_raw[L_BYTES];
  LAS unsigned char* L = (LAS unsigned char*)lds_raw;
  const int tid = threadIdx.x, lane = tid & 63, w = __builtin_amdgcn_readfirstlane(tid >> 6);
  const int k = lane >> 3, c = lane & 7;
  const int blk = blockIdx.x, xg = blk & 7, idx = blk >> 3;
  const int h = xg * 4 + (idx & 3), sh = idx >> 2, b = sh >> 1, s = sh & 1;
  const int j = h * 2 + s;
  STAMP(0);
#define PHASE 0
#define T0OFF 80
  unsigned T0 = now32() + T0OFF + idx * PHASE * (TAU16 / 32) / (16 * 32);
  const __amdgpu_buffer_rsrc_t rws = rsrc(ws, WS_BYTES);
  const v4u tiny = __builtin_nontemporal_load((const GAS v4u*)(tid == 320 ? (const unsigned char*)ws + WS_EPOCH : (const unsigned char*)x + (tid < 320 ? tid : 0) * 16));
  const int qr = tid / 24, qc = tid - qr * 24;
  v4u wqv[4], fwa, fwb;
  {
    const __amdgpu_buffer_rsrc_t rq = rsrc(wq + h * DH, (DM * NH * DH - h * DH) * 2);
    const int oob = tid < 480 ? 0 : OOB;
#pragma unroll
    for (int e = 0; e < 4; ++e) wqv[e] = ld16<WQ_AUX>(rq, (sh * 80 + qr * 4) * (NH * DH * 2) + qc * 16 + oob, e * (NH * DH * 2));
    const int row0 = (idx >> 1) * 40 + qr * 2;
    const __amdgpu_buffer_rsrc_t r13 = rsrc(((idx & 1) ? w3 : w1) + xg * 192, (DM * FF - xg * 192) * 2);
    fwa = ld16<WQ_AUX>(r13, row0 * (FF * 2) + qc * 16 + oob, 0); fwb = ld16<WQ_AUX>(r13, row0 * (FF * 2) + qc * 16 + oob, FF * 2);
  }
  const size_t kvoff = ((size_t)(b * NH + h) * SK + (size_t)s * KEYS_WG) * DH;
  const u16* Kb = Kc + kvoff; const u16* Vb = Vc + kvoff;
  const int kvo = w * GRPB + k * ROWB + c * 16;
  unsigned tn = T0 << 4;
  auto pace = [&](unsigned step16) { while ((int)((now32() << 4) - tn) < 0) __builtin_amdgcn_s_sleep(1); tn += step16; };
  int wo_t = -1;
  __amdgpu_buffer_rsrc_t ro = rsrc(wo + (size_t)h * DH * DM, DH * DM * 2);
  int wo_vo = 0;
  auto issue = [&](v4u& r00, v4u& r01, v4u& r02, v4u& r10, v4u& r11, v4u& r12, int m) {
    TR(1);
    pace(TAU16 + (wo_t >= 0 ? XDMA16 : 0));
    TR(2);
    if (wo_t >= 0) dma16(ro, wo_vo, wo_t * 6 * (DM * 2), (unsigned)(L_FP + w * 8192 + (wo_t & 7) * 1024));
    const __amdgpu_buffer_rsrc_t r = rsrc(m < NGW ? Kb : Vb, KVB);
    const int so = (m & (NGW - 1)) * (NWAVE * GRPB);
    r00 = ld16<KV_AUX>(r, kvo, so); r01 = ld16<KV_AUX>(r, kvo + 128, so); r02 = ld16<KV_AUX>(r, kvo + 256, so);
    r10 = ld16<KV_AUX>(r, kvo, so + TILEB); r11 = ld16<KV_AUX>(r, kvo + 128, so + TILEB); r12 = ld16<KV_AUX>(r, kvo + 256, so + TILEB);
    SCHED_FENCE();
    TR(3);
  };
#define ISSUE(G, m) issue(G.r[0][0], G.r[0][1], G.r[0][2], G.r[1][0], G.r[1][1], G.r[1][2], m)
#define ISSUE_W(o, m) issue(W[o], W[o + 1], W[o + 2], W[o + 3], W[o + 4], W[o + 5], m)
  Grp A, B;
  v4u W[32];
  ISSUE(A, 0);
  ISSUE(B, 1);
  tn += XHEAD16;
  STAMP(1);
  if (tid <= 320) *(LAS v4u*)(L + (tid < 320 ? L_XS + tid * 16 : L_MISC + 16)) = tiny;
  __syncthreads();
  const unsigned tagA = __builtin_amdgcn_readfirstlane(*(LAS unsigned*)(L + L_MISC + 16)) + 1u, tagB = __builtin_amdgcn_readfirstlane(*(LAS unsigned*)(L + L_MISC + 20)) + 1u;
  v2u xq[4]; unsigned fx4[4];
  { const int qrc = qr < 20 ? qr : 0;
#pragma unroll
    for (int e = 0; e < 4; ++e) { xq[e] = *(LAS v2u*)(L + L_XS + e * (DM * 2) + (sh * 80 + qrc * 4) * 2); fx4[e] = *(LAS unsigned*)(L + L_XS + e * (DM * 2) + ((idx >> 1) * 40 + qrc * 2) * 2); } }
  {
    unsigned lo01[4], hi01[4], lo23[4], hi23[4];
#pragma unroll
    for (int d = 0; d < 4; ++d) {
      lo01[d] = __builtin_amdgcn_perm(wqv[1][d], wqv[0][d], 0x05040100u); hi01[d] = __builtin_amdgcn_perm(wqv[1][d], wqv[0][d], 0x07060302u);
      lo23[d] = __builtin_amdgcn_perm(wqv[3][d], wqv[2][d], 0x05040100u); hi23[d] = __builtin_amdgcn_perm(wqv[3][d], wqv[2][d], 0x07060302u);
    }
#pragma unroll
    for (int bb = 0; bb < 4; ++bb) {
      float a[8];
#pragma unroll
      for (int d = 0; d < 4; ++d) { a[2 * d] = dot2(lo23[d], xq[bb][1], dot2(lo01[d], xq[bb][0], 0.f)); a[2 * d + 1] = dot2(hi23[d], xq[bb][1], dot2(hi01[d], xq[bb][0], 0.f)); }
      LAS v4f* p = (LAS v4f*)(L + L_FP + ((qr * 4 + bb) * 192 + qc * 8) * 4);
      p[0] = v4f{a[0], a[1], a[2], a[3]}; p[1] = v4f{a[4], a[5], a[6], a[7]};
    }
  }
  {
    unsigned lo[4], hi[4];
#pragma unroll
    for (int d = 0; d < 4; ++d) { lo[d] = __builtin_amdgcn_perm(fwb[d], fwa[d], 0x05040100u); hi[d] = __builtin_amdgcn_perm(fwb[d], fwa[d], 0x07060302u); }
#pragma unroll
    for (int bb = 0; bb < 4; ++bb) {
      LAS v4f* p = (LAS v4f*)(L + L_FP2 + ((qr * 4 + bb) * 192 + qc * 8) * 4);
      p[0] = v4f{dot2(lo[0], fx4[bb], 0.f), dot2(hi[0], fx4[bb], 0.f), dot2(lo[1], fx4[bb], 0.f), dot2(hi[1], fx4[bb], 0.f)};
      p[1] = v4f{dot2(lo[2], fx4[bb], 0.f), dot2(hi[2], fx4[bb], 0.f), dot2(lo[3], fx4[bb], 0.f), dot2(hi[3], fx4[bb], 0.f)};
    }
  }
  TR(9);
  __syncthreads();
  TR(10);
  STAMP(2);
  if (tid < 384) {
    const int bb = tid / 96, p = tid - bb * 96;
    float a0 = 0.f, a1 = 0.f;
#pragma unroll
    for (int r = 0; r < 20; ++r) { const v2f v = *(LAS v2f*)(L + L_FP + ((r * 4 + bb) * 192 + p * 2) * 4); a0 += v[0]; a1 += v[1]; }
    __builtin_amdgcn_raw_buffer_store_b128(v4u{__float_as_uint(a0), tagA, __float_as_uint(a1), tagB}, rws, WS_QX + (((h * 4 + bb) * 8 + sh) * 128 + p) * 16, 0, AUX_SC1);
    a0 = 0.f; a1 = 0.f;
#pragma unroll
    for (int r = 0; r < 20; ++r) { const v2f v = *(LAS v2f*)(L + L_FP2 + ((r * 4 + bb) * 192 + p * 2) * 4); a0 += v[0]; a1 += v[1]; }
    __builtin_amdgcn_raw_buffer_store_b128(v4u{__float_as_uint(a0), tagA, __float_as_uint(a1), tagB}, rws,
                                           WS_FX + (((((xg * 4 + bb) * 8 + p / 12) * 2 + (idx & 1)) * 16 + (idx >> 1)) * 12 + p % 12) * 16, 0, AUX_SC1);
  }
  if (tid >= 384 && tid < 388)
    __builtin_amdgcn_raw_buffer_store_b128(v4u{T0, tagA, T0, tagB}, rws, WS_QX + (((h * 4 + (tid - 384)) * 8 + sh) * 128 + 96) * 16, 0, AUX_SC1);
  SCHED_FENCE();
  TR(11);
  ISSUE_W(0, 2);
  ISSUE_W(6, 3);
  ISSUE_W(12, 4);
  TR(12);
  if (w == 0) {
    const int qo = WS_QX + (h * 4 + b) * 8 * 2048 + lane * 16;
    v4u ga[8], gb[8];
    for (unsigned spins = 0;;) {
#pragma unroll
      for (int p = 0; p < 8; ++p) { ga[p] = poll16(rws, qo + p * 2048); gb[p] = poll16(rws, qo + p * 2048 + 1024 + (lane <= 32 ? 0 : OOB)); }
      bool ok = true;
#pragma unroll
      for (int p = 0; p < 8; ++p) ok = ok & (ga[p][1] == tagA) & (ga[p][3] == tagB) & ((lane > 32) | ((gb[p][1] == tagA) & (gb[p][3] == tagB)));
      TR(17);
      if (__all(ok) || ++spins > SPIN_CAP) break;
      __builtin_amdgcn_s_sleep(1);
    }
    TR(16);
    float a0 = 0.f, a1 = 0.f, b0 = 0.f, b1 = 0.f;
#pragma unroll
    for (int p = 0; p < 8; ++p) { a0 += __uint_as_float(ga[p][0]); a1 += __uint_as_float(ga[p][2]); b0 += __uint_as_float(gb[p][0]); b1 += __uint_as_float(gb[p][2]); }
    *(LAS unsigned*)(L + L_QB + lane * 4) = cvtpk(a0, a1);
    if (lane < 32) *(LAS unsigned*)(L + L_QB + 256 + lane * 4) = cvtpk(b0, b1);
    unsigned tl = gb[0][0];
#pragma unroll
    for (int p = 1; p < 8; ++p) tl = (int)(gb[p][0] - tl) > 0 ? gb[p][0] : tl;
    if (lane == 32) *(LAS unsigned*)(L + L_MISC + 8) = tl;
  }
  TR(13);
  tn += XPOLL16;
  ISSUE_W(18, 5);
  ISSUE_W(24, 6);
  __syncthreads();
  TR(14);
  STAMP(3);
  { const unsigned tl = __builtin_amdgcn_readfirstlane(*(LAS unsigned*)(L + L_MISC + 8)); if (tl - T0 < 1000u) tn += (tl - T0) << 4; }
#define SLACK 3
  { const unsigned t = (now32() << 4) - SLACK * TAU16; if ((int)(t - tn) > 0) tn = t; }
  v4u Q[3];
#pragma unroll
  for (int i = 0; i < 3; ++i) Q[i] = *(LAS v4u*)(L + L_QB + (c + 8 * i) * 16);

  float mx = -INFINITY;
  const bool oct0 = c == 0;
  const int scoff = L_SC + w * 2048 + k * 8;
  auto k_tile = [&](const v4u& r0, const v4u& r1, const v4u& r2) -> float {
    float a0 = dot2(r0[0], Q[0][0], 0.f), a1 = dot2(r1[0], Q[1][0], 0.f), a2 = dot2(r2[0], Q[2][0], 0.f);
#pragma unroll
    for (int d = 1; d < 4; ++d) { a0 = dot2(r0[d], Q[0][d], a0); a1 = dot2(r1[d], Q[1][d], a1); a2 = dot2(r2[d], Q[2][d], a2); }
    return (a0 + a1) + a2;
  };
  auto k_put = [&](float s0, float s1, int n) {
    s0 = oct_sum(s0); s1 = oct_sum(s1);
    mx = fmaxf(mx, fmaxf(s0, s1));
    if (oct0) *(LAS v2f*)(L + scoff + n * 64) = v2f{s0, s1};
  };
#define K_STEP(G, n, m) do { float s0_ = k_tile(G.r[0][0], G.r[0][1], G.r[0][2]), s1_ = k_tile(G.r[1][0], G.r[1][1], G.r[1][2]); \
    asm volatile("" : "+v"(s0_), "+v"(s1_) : : "memory"); ISSUE(G, m); k_put(s0_, s1_, n); } while (0)
  K_STEP(A, 0, 7);
#pragma unroll
  for (int g = 0; g < 5; ++g) k_put(k_tile(W[g * 6], W[g * 6 + 1], W[g * 6 + 2]), k_tile(W[g * 6 + 3], W[g * 6 + 4], W[g * 6 + 5]), 2 + g);
  STAMP(4);
  K_STEP(B, 1, 8);
  STAMP(5);
  {
    const int lane = fresh_lane(), tid2 = w * 64 + lane;
    const int yr = lane / 10, yc = lane - yr * 10;
    const int fxo = WS_FX + ((xg * 4 + b) * 8 + (j & 7)) * 6144 + tid2 * 16 + (tid2 < 384 ? 0 : OOB);
    v4u fx = poll16(rws, fxo);
    const __amdgpu_buffer_rsrc_t r2 = rsrc(w2, FF * DM * 2), rg = rsrc(gw, w == 0 ? DM * 2 : 0);
    const int vo2 = (j * 24 + yr) * (DM * 2) + w * 160 + yc * 16 + (lane < 60 ? 0 : OOB);
#pragma unroll
    for (int t = 0; t < 4; ++t) W[8 + t] = ld16<W2_AUX>(r2, vo2, t * 6 * (DM * 2));
    const v4u gg0 = ld16<AUX_NT>(rg, lane * 16, 0), gg1 = ld16<AUX_NT>(rg, 1024 + lane * 16, 0);
    SCHED_FENCE();
    TR(30);
    tn += XFFN16;
    K_STEP(A, 7, 9); K_STEP(B, 8, 10);
    TR(31);
    for (unsigned spins = 0; !__all((tid2 >= 384) | ((fx[1] == tagA) & (fx[3] == tagB))); ) {
      if (++spins > SPIN_CAP) break;
      __builtin_amdgcn_s_sleep(2);
      fx = poll16(rws, fxo);
    }
    TR(32);
    if (tid2 < 384) *(LAS v2f*)(L + L_G + ((tid2 / 12) * 24 + (tid2 % 12) * 2) * 4) = v2f{__uint_as_float(fx[0]), __uint_as_float(fx[2])};
    float gate = 0.f;
    if (w == 0) {
      const v4u gx0 = *(LAS v4u*)(L + L_XS + b * (DM * 2) + lane * 16), gx1 = *(LAS v4u*)(L + L_XS + b * (DM * 2) + 1024 + (lane & 15) * 16);
      float d = 0.f;
#pragma unroll
      for (int e = 0; e < 4; ++e) { d = dot2(gx0[e], gg0[e], d); d = dot2(gx1[e], gg1[e], d); }
      gate = 1.f / (1.f + __expf(-wave_sum(d)));
      if (lane == 0) *(LAS float*)(L + L_MISC) = gate;
    }
    __syncthreads();
    TR(33);
    if (tid2 < 24) {
      const LAS float* g = (const LAS float*)(L + L_G) + tid2;
      float ga = 0.f, up = 0.f;
#pragma unroll
      for (int r = 0; r < 16; ++r) { ga += g[r * 24]; up += g[(16 + r) * 24]; }
      *(LAS float*)(L + L_MISC + 64 + tid2 * 4) = ga / (1.f + __expf(-ga)) * up * (1.f - gate);
    }
    __syncthreads();
    TR(34);
    v2f ya[4];
#pragma unroll
    for (int e = 0; e < 4; ++e) ya[e] = v2f{0.f, 0.f};
#pragma unroll
    for (int t = 0; t < 4; ++t) {
      const float hv = *(LAS float*)(L + L_MISC + 64 + (t * 6 + (yr < 6 ? yr : 0)) * 4);
#pragma unroll
      for (int d = 0; d < 4; ++d) ya[d] = __builtin_elementwise_fma(v2f{hv, hv}, v2f{bflo(W[8 + t][d]), bfhi(W[8 + t][d])}, ya[d]);
    }
    if (lane < 60) {
      LAS v4f* p = (LAS v4f*)(L + L_YF + (w * 480 + yr * 80 + yc * 8) * 4);
      p[0] = v4f{ya[0][0], ya[0][1], ya[1][0], ya[1][1]}; p[1] = v4f{ya[2][0], ya[2][1], ya[3][0], ya[3][1]};
    }
    wo_vo = yr * (DM * 2) + w * 160 + yc * 16 + (lane < 60 ? 0 : OOB);
  }
  MEM_FENCE();
  TR(35);
  K_STEP(A, 9, 11); K_STEP(B, 10, 12);
  STAMP(13);
#define WO_COPY(t0) do { asm volatile("s_waitcnt vmcnt(12)" ::: "memory"); const int ln_ = fresh_lane(); \
    _Pragma("unroll") for (int t = 0; t < 8; t += 2) {     \
      const v4u a_ = *(LAS v4u*)(L + L_FP + w * 8192 + t * 1024 + ln_ * 16), b_ = *(LAS v4u*)(L + L_FP + w * 8192 + (t + 1) * 1024 + ln_ * 16); \
      _Pragma("unroll") for (int d = 0; d < 4; ++d) { W[t0 + t][d] = __builtin_amdgcn_perm(b_[d], a_[d], 0x05040100u); W[t0 + t + 1][d] = __builtin_amdgcn_perm(b_[d], a_[d], 0x07060302u); } } \
    asm volatile("" : "+v"(W[t0]), "+v"(W[t0 + 1]), "+v"(W[t0 + 2]), "+v"(W[t0 + 3]), "+v"(W[t0 + 4]), "+v"(W[t0 + 5]), "+v"(W[t0 + 6]), "+v"(W[t0 + 7]) : : "memory"); } while (0)
  int n;
#pragma unroll 1
  for (n = 11; n < 21; n += 2) {
    wo_t = n < 19 ? n - 11 : -1; K_STEP(A, n, n + 2);
    wo_t = n < 19 ? n - 10 : -1; K_STEP(B, n + 1, n + 3);
  }
  WO_COPY(0);
#pragma unroll 1
  for (; n < 31; n += 2) {
    wo_t = n < 29 ? n - 13 : -1; K_STEP(A, n, n + 2);
    wo_t = n < 29 ? n - 12 : -1; K_STEP(B, n + 1, n + 3);
  }
  WO_COPY(8);
  wo_t = -1;
  K_STEP(A, 31, 33);

  STAMP(6);
  mx = fmaxf(mx, dpp<XOR8>(mx)); mx = max16_32(mx);
  const float mw = mx * CS;
  LDS_FENCE();
  {
    const int lane = fresh_lane();
    LAS v4f* p = (LAS v4f*)(L + L_SC + w * 2048 + lane * 32);
    v4f a = p[0], bb = p[1];
    float l = 0.f;
#pragma unroll
    for (int e = 0; e < 4; ++e) { a[e] = ex2(fmaf(a[e], CS, -mw)); bb[e] = ex2(fmaf(bb[e], CS, -mw)); l += a[e] + bb[e]; }
    p[0] = a; p[1] = bb;
    l = wave_sum(l);
    if (lane == 0) __builtin_amdgcn_raw_buffer_store_b128(v4u{__float_as_uint(mw), tagA, __float_as_uint(l), tagB}, rws, WS_ML + (((b * NH + h) * 2 + s) * 8 + w) * 16, 0, AUX_SC1);
  }
  LDS_FENCE();
  STAMP(11);

  v2f acc2[12];
#pragma unroll
  for (int e = 0; e < 12; ++e) acc2[e] = v2f{0.f, 0.f};
#define PIN_ACC() asm volatile("" : "+v"(acc2[0]), "+v"(acc2[1]), "+v"(acc2[2]), "+v"(acc2[3]), "+v"(acc2[4]), "+v"(acc2[5]), "+v"(acc2[6]), "+v"(acc2[7]), \
                                    "+v"(acc2[8]), "+v"(acc2[9]), "+v"(acc2[10]), "+v"(acc2[11]) : : "memory")
  auto v_tile = [&](const v4u& r0, const v4u& r1, const v4u& r2, float p) {
    const v2f pp = v2f{p, p};
#pragma unroll
    for (int d = 0; d < 4; ++d) {
      acc2[d] = __builtin_elementwise_fma(pp, v2f{bflo(r0[d]), bfhi(r0[d])}, acc2[d]);
      acc2[4 + d] = __builtin_elementwise_fma(pp, v2f{bflo(r1[d]), bfhi(r1[d])}, acc2[4 + d]);
      acc2[8 + d] = __builtin_elementwise_fma(pp, v2f{bflo(r2[d]), bfhi(r2[d])}, acc2[8 + d]);
    }
    PIN_ACC();
  };
#define V_USE(G, n) do { const v2f p2_ = *(LAS v2f*)(L + scoff + (n) * 64); v_tile(G.r[0][0], G.r[0][1], G.r[0][2], p2_[0]); v_tile(G.r[1][0], G.r[1][1], G.r[1][2], p2_[1]); } while (0)
#pragma unroll 1
  for (n = 0; n < 10; n += 2) {
    V_USE(B, n); wo_t = n < 8 ? n + 16 : -1; ISSUE(B, NGW + n + 2);
    V_USE(A, n + 1); wo_t = n < 8 ? n + 17 : -1; ISSUE(A, NGW + n + 3);
  }
  WO_COPY(16);
#pragma unroll 1
  for (; n < 20; n += 2) {
    V_USE(B, n); wo_t = n < 18 ? n + 14 : -1; ISSUE(B, NGW + n + 2);
    V_USE(A, n + 1); wo_t = n < 18 ? n + 15 : -1; ISSUE(A, NGW + n + 3);
  }
  WO_COPY(24);
  wo_t = -1;
  const int mloff = WS_ML + ((b * NH + h) * 16 + (fresh_lane() & 15)) * 16;
  v4u ml = poll16(rws, mloff);
  V_USE(B, n); ISSUE(B, NGW + n + 2); V_USE(A, n + 1); ISSUE(A, NGW + n + 3);
  for (unsigned spins = 0; !__all((ml[1] == tagA) & (ml[3] == tagB)); ) {
    if (++spins > SPIN_CAP) break;
    __builtin_amdgcn_s_sleep(2);
    ml = poll16(rws, mloff);
  }
  float fw;
  {
    const float m_i = __uint_as_float(ml[0]), l_i = __uint_as_float(ml[2]);
    float M = m_i;
    M = fmaxf(M, dpp<XOR1>(M)); M = fmaxf(M, dpp<XOR2>(M)); M = fmaxf(M, dpp<XOR7>(M)); M = fmaxf(M, dpp<XOR8>(M));
    float Ls = l_i * ex2(m_i - M);
    Ls = oct_sum(Ls); Ls += dpp<XOR8>(Ls);
    fw = __uint_as_float(__builtin_amdgcn_readfirstlane(__float_as_uint(ex2(mw - M) / Ls * *(LAS float*)(L + L_MISC))));
  }
#pragma unroll 1
  for (n += 2; n < NGW - 2; n += 2) { V_USE(B, n); ISSUE(B, NGW + n + 2); V_USE(A, n + 1); ISSUE(A, NGW + n + 3); }
  V_USE(B, NGW - 2);
  V_USE(A, NGW - 1);

  STAMP(7);
  TR(40);
  {
    float b12[12], c6[6];
#pragma unroll
    for (int i = 0; i < 12; ++i) {
      auto r = __builtin_amdgcn_permlane32_swap(__float_as_uint(acc2[i >> 1][i & 1]), __float_as_uint(acc2[6 + (i >> 1)][i & 1]), false, false);
      b12[i] = __uint_as_float(r[0]) + __uint_as_float(r[1]);
    }
#pragma unroll
    for (int i = 0; i < 6; ++i) {
      auto r = __builtin_amdgcn_permlane16_swap(__float_as_uint(b12[i]), __float_as_uint(b12[i + 6]), false, false);
      c6[i] = __uint_as_float(r[0]) + __uint_as_float(r[1]);
    }
#pragma unroll
    for (int i = 0; i < 6; ++i) c6[i] = (c6[i] + dpp<XOR8>(c6[i])) * fw;
    const int ln = fresh_lane();
    if ((ln & 8) == 0) {
      const int v0 = (ln >> 4) * 6;
#pragma unroll
      for (int p = 0; p < 3; ++p) { const int v = v0 + 2 * p; *(LAS v2f*)(L + L_QP + (w * QPS + (v >> 3) * 64 + (ln & 7) * 8 + (v & 7)) * 4) = v2f{c6[2 * p], c6[2 * p + 1]}; }
    }
  }
  const int ln = fresh_lane();
  const int yr = ln < 60 ? ln / 10 : 0, yc = ln - (ln / 10) * 10;
  float ya[8];
  { const LAS v4f* p = (const LAS v4f*)(L + L_YF + (w * 480 + yr * 80 + yc * 8) * 4); const v4f a = p[0], bb = p[1];
#pragma unroll
    for (int e = 0; e < 4; ++e) { ya[e] = a[e]; ya[4 + e] = bb[e]; } }
  STAMP(8);
  TR(41);
  __syncthreads();
  TR(42);
  if (w < 6) {
    const int pi = w * 16 + (ln >> 2), r = pi >> 4, u = pi & 15, qq = ln & 3;
    const LAS float* p = (const LAS float*)(L + L_QP) + 2 * qq * QPS + u * 12 + r;
    float a0 = p[0] + p[QPS], a1 = p[6] + p[QPS + 6];
    a0 += dpp<XOR1>(a0); a1 += dpp<XOR1>(a1); a0 += dpp<XOR2>(a0); a1 += dpp<XOR2>(a1);
    if (qq == 0) *(LAS unsigned*)(L + L_OB + pi * 4) = cvtpk(a0, a1);
  }
  TR(43);
  __syncthreads();
  TR(44);
  {
    LAS float* yrp = (LAS float*)(L + L_FP) + w * 480;
    const LAS v4u* op = (const LAS v4u*)(L + L_OB + yr * 64);
#pragma unroll
    for (int u4 = 0; u4 < 4; ++u4) {
      const v4u o4 = op[u4];
#pragma unroll
      for (int e = 0; e < 4; ++e) {
        const int u = u4 * 4 + e;
#pragma unroll
        for (int d = 0; d < 4; ++d) { ya[2 * d] = dot2(W[2 * u][d], o4[e], ya[2 * d]); ya[2 * d + 1] = dot2(W[2 * u + 1][d], o4[e], ya[2 * d + 1]); }
      }
    }
    asm volatile("" : "+v"(ya[0]), "+v"(ya[1]), "+v"(ya[2]), "+v"(ya[3]), "+v"(ya[4]), "+v"(ya[5]), "+v"(ya[6]), "+v"(ya[7]));
    TR(45);
    if (ln < 60) { LAS v4f* p = (LAS v4f*)(yrp + yr * 80 + yc * 8); p[0] = v4f{ya[0], ya[1], ya[2], ya[3]}; p[1] = v4f{ya[4], ya[5], ya[6], ya[7]}; }
    LDS_FENCE();
    TR(46);
    if (ln < 40) {
      v2f y = v2f{0.f, 0.f};
#pragma unroll
      for (int r = 0; r < 6; ++r) y += *(LAS v2f*)(yrp + r * 80 + ln * 2);
      const int cons = w * 8 + ln / 5, g5 = ln % 5;
      __builtin_amdgcn_raw_buffer_store_b128(v4u{__float_as_uint(y[0]), tagA, __float_as_uint(y[1]), tagB}, rws, WS_INBOX + (((b * 64 + cons) * 64 + j) * 5 + g5) * 16, 0, AUX_SC1);
    }
  }
  STAMP(9);
  TR(47);
  if (w == 0) {
    const int io = WS_INBOX + ((b * 64 + j) * 64 + ln) * 80;
    v4u g5[5];
    for (unsigned spins = 0;;) {
      bool ok = true;
#pragma unroll
      for (int e = 0; e < 5; ++e) g5[e] = poll16(rws, io + e * 16);
#pragma unroll
      for (int e = 0; e < 5; ++e) ok = ok & (g5[e][1] == tagA) & (g5[e][3] == tagB);
      if (__all(ok) || ++spins > SPIN_CAP) break;
      __builtin_amdgcn_s_sleep(1);
    }
    TR(48);
    const unsigned yv[10] = {g5[0][0], g5[0][2], g5[1][0], g5[1][2], g5[4][0], g5[2][0], g5[2][2], g5[3][0], g5[3][2], g5[4][2]};
    float b5[5];
#pragma unroll
    for (int i = 0; i < 5; ++i) { auto r = __builtin_amdgcn_permlane32_swap(yv[i], yv[5 + i], false, false); b5[i] = __uint_as_float(r[0]) + __uint_as_float(r[1]); }
    float z[3];
    { auto r = __builtin_amdgcn_permlane16_swap(__float_as_uint(b5[0]), __float_as_uint(b5[2]), false, false); z[0] = __uint_as_float(r[0]) + __uint_as_float(r[1]); }
    { auto r = __builtin_amdgcn_permlane16_swap(__float_as_uint(b5[1]), __float_as_uint(b5[3]), false, false); z[1] = __uint_as_float(r[0]) + __uint_as_float(r[1]); }
    { auto r = __builtin_amdgcn_permlane16_swap(__float_as_uint(b5[4]), __float_as_uint(b5[4]), false, false); z[2] = __uint_as_float(r[0]) + __uint_as_float(r[1]); }
#pragma unroll
    for (int i = 0; i < 3; ++i) { z[i] += dpp<XOR1>(z[i]); z[i] += dpp<XOR2>(z[i]); z[i] += dpp<XOR7>(z[i]); z[i] += dpp<XOR8>(z[i]); }
    auto r89 = __builtin_amdgcn_permlane32_swap(__float_as_uint(z[2]), __float_as_uint(z[2]), false, false);
    const unsigned o03 = cvtpk(z[0], z[1]), o4 = cvtpk(__uint_as_float(r89[0]), __uint_as_float(r89[1]));
    if ((ln & 15) == 0 || ln == 1) *(GAS unsigned*)(out + b * DM + j * 10 + (ln == 1 ? 8 : (ln >> 4) * 2)) = ln == 1 ? o4 : o03;
    if (b == 0 && j == 0 && ln == 0) __hip_atomic_store((GAS u64*)(ws + WS_EPOCH), ((u64)tagB << 32) | tagA, __ATOMIC_RELAXED, __HIP_MEMORY_SCOPE_AGENT);
  }
  TR(49);
}
}

extern "C" void kernel_launch(void* const* d_in, const int* in_sizes, int n_in, void* d_out, int out_size, void* d_ws, size_t ws_size,
                              hipStream_t stream) {
  (void)in_sizes; (void)n_in; (void)out_size; (void)ws_size;
  hipLaunchKernelGGL(fused_decode, dim3(NWG), dim3(NTHR), 0, stream, (const u16*)d_in[0], (const u16*)d_in[1], (const u16*)d_in[2], (const u16*)d_in[3],
                     (const u16*)d_in[4], (const u16*)d_in[6], (const u16*)d_in[7], (const u16*)d_in[8], (const u16*)d_in[9], (u16*)d_out, (unsigned char*)d_ws);
}
```

```cpp
#define T0OFF 50
#define TAU16 (171 * 16)
#define XDMA16 (TAU16 * 6 / 100)
#include <hip/hip_runtime.h>
#include <hip/hip_bf16.h>
#include <stdint.h>

#define GAS __attribute__((address_space(1)))
#define LAS __attribute__((address_space(3)))
typedef unsigned v4u __attribute__((ext_vector_type(4)));
typedef unsigned v2u __attribute__((ext_vector_type(2)));
typedef float v4f __attribute__((ext_vector_type(4)));
typedef float v2f __attribute__((ext_vector_type(2)));
typedef __bf16 bf16x2 __attribute__((ext_vector_type(2)));
typedef unsigned long long u64;
typedef unsigned short u16;

namespace {
constexpr int NB = 4, NH = 32, SK = 8192, DH = 192, DM = 640, FF = 1536;
constexpr int NWG = 256, NWAVE = 8, NTHR = NWAVE * 64;
constexpr int KEYS_WG = SK / 2;
constexpr int ROWB = DH * 2;
constexpr int TILEB = 8 * ROWB;
constexpr int GRPB = 2 * TILEB;
constexpr int KVB = KEYS_WG * ROWB;
constexpr int NGW = KVB / GRPB / NWAVE;
constexpr int RSRC3 = 0x00020000;
#define POLL_AUX 16
#define STORE_AUX 16
constexpr int AUX_NT = 2, AUX_SC1 = STORE_AUX;
constexpr int OOB = 1 << 28;
constexpr float CS = 0.07216878364870322f * 1.4426950408889634f;
#define XHEAD16 (TAU16 * 10 / 10)
#define XPOLL16 (TAU16 * 3 / 10)
#define XFFN16 (TAU16 * 3 / 10)
#define ENDSYNC 0
#define ADAPT 0
#define TAUMIN16 (171 * 16)
#define TAUMAX16 (188 * 16)
#define TAUUP16 64
#define TAUDN16 32
#define MISS_HI 12
#define MISS_LO 4
#define PRIO 1
#define PHW 0

constexpr int WS_EPOCH = 0;
constexpr int WS_ML = 4096;
constexpr int WS_INBOX = 65536;
constexpr int WS_QX = WS_INBOX + NB * 64 * 64 * 80;
constexpr int WS_FX = WS_QX + NH * NB * 8 * 128 * 16;
constexpr int WS_FL = WS_FX + 8 * NB * 8 * 384 * 16;
constexpr int WS_BYTES = WS_FL + 8 * 1024;

constexpr int L_XS = 0;
constexpr int L_QP = 5120;
constexpr int QPS = 196;
constexpr int L_QB = L_QP + 8 * QPS * 4;
constexpr int L_OB = L_QB + 384;
constexpr int L_MISC = L_OB + 384;
constexpr int L_G = L_MISC + 256;
constexpr int L_YF = L_G + 3072;
constexpr int L_SC = L_YF + 8 * 480 * 4;
constexpr int L_FP2 = L_YF;
constexpr int L_FP = L_YF + 22 * 4 * 192 * 4;
constexpr int L_TR = L_FP + 22 * 4 * 192 * 4;
constexpr int L_BYTES = L_TR;

constexpr unsigned SPIN_CAP = 1u << 17;

__device__ __forceinline__ float dot2(unsigned a, unsigned b, float c) {
  return __builtin_amdgcn_fdot2_f32_bf16(__builtin_bit_cast(bf16x2, a), __builtin_bit_cast(bf16x2, b), c, false);
}
template <int CTRL> __device__ __forceinline__ float dpp(float x) {
  return __builtin_bit_cast(float, __builtin_amdgcn_mov_dpp(__builtin_bit_cast(int, x), CTRL, 0xf, 0xf, true));
}
constexpr int XOR1 = 0xB1, XOR2 = 0x4E, XOR7 = 0x141, XOR8 = 0x128;
__device__ __forceinline__ float sum16_32(float x) {
  auto s = __builtin_amdgcn_permlane16_swap(__float_as_uint(x), __float_as_uint(x), false, false);
  x = __uint_as_float(s[0]) + __uint_as_float(s[1]);
  auto t = __builtin_amdgcn_permlane32_swap(__float_as_uint(x), __float_as_uint(x), false, false);
  return __uint_as_float(t[0]) + __uint_as_float(t[1]);
}
__device__ __forceinline__ float max16_32(float x) {
  auto s = __builtin_amdgcn_permlane16_swap(__float_as_uint(x), __float_as_uint(x), false, false);
  x = fmaxf(__uint_as_float(s[0]), __uint_as_float(s[1]));
  auto t = __builtin_amdgcn_permlane32_swap(__float_as_uint(x), __float_as_uint(x), false, false);
  return fmaxf(__uint_as_float(t[0]), __uint_as_float(t[1]));
}
__device__ __forceinline__ float oct_sum(float x) { x += dpp<XOR1>(x); x += dpp<XOR2>(x); x += dpp<XOR7>(x); return x; }
__device__ __forceinline__ float xoct_sum(float x) { x += dpp<XOR8>(x); return sum16_32(x); }
__device__ __forceinline__ float wave_sum(float x) { return xoct_sum(oct_sum(x)); }
__device__ __forceinline__ unsigned cvtpk(float lo, float hi) { unsigned r; asm("v_cvt_pk_bf16_f32 %0, %1, %2" : "=v"(r) : "v"(lo), "v"(hi)); return r; }
__device__ __forceinline__ float bflo(unsigned u) { return __uint_as_float(u << 16); }
__device__ __forceinline__ float bfhi(unsigned u) { return __uint_as_float(u & 0xffff0000u); }
__device__ __forceinline__ float ex2(float x) { return __builtin_amdgcn_exp2f(x); }
__device__ __forceinline__ __amdgpu_buffer_rsrc_t rsrc(const void* p, int bytes) { return __builtin_amdgcn_make_buffer_rsrc((void*)p, (short)0, bytes, RSRC3); }
template <int AUX> __device__ __forceinline__ v4u ld16(__amdgpu_buffer_rsrc_t r, int voff, int soff) { return __builtin_amdgcn_raw_buffer_load_b128(r, voff, soff, AUX); }
template <int AUX> __device__ __forceinline__ v2u ld8(__amdgpu_buffer_rsrc_t r, int voff, int soff) { return __builtin_amdgcn_raw_buffer_load_b64(r, voff, soff, AUX); }
__device__ __forceinline__ unsigned now32() { return (unsigned)__builtin_amdgcn_s_memrealtime(); }
#define STAMP(i) do { } while (0)
#define TR(id) do { } while (0)
#define TRV(id, val) do { } while (0)
#define SCHED_FENCE() __builtin_amdgcn_sched_barrier(0)
#define LDS_FENCE() asm volatile("s_waitcnt lgkmcnt(0)" ::: "memory")
#define MEM_FENCE() asm volatile("" ::: "memory")
__device__ __forceinline__ v4u poll16(__amdgpu_buffer_rsrc_t r, int voff) { asm volatile("" : "+v"(voff) : : "memory"); return __builtin_amdgcn_raw_buffer_load_b128(r, voff, 0, POLL_AUX); }
__device__ __forceinline__ int fresh_lane() { int l; asm volatile("v_mbcnt_lo_u32_b32 %0, -1, 0\n\tv_mbcnt_hi_u32_b32 %0, -1, %0" : "=v"(l)); return l; }

#define WO_POLICY ""
__device__ __forceinline__ void dma16(__amdgpu_buffer_rsrc_t r, int voff, int soff, unsigned lds) {
  unsigned keep;
  asm volatile("s_mov_b32 %0, m0\n\ts_mov_b32 m0, %3\n\ts_nop 0\n\tbuffer_load_dwordx4 %1, %2, %4 offen " WO_POLICY " lds\n\ts_mov_b32 m0, %0"
               : "=&s"(keep) : "v"(voff), "s"(r), "s"(lds), "s"(soff) : "memory");
}
#define KV_AUX AUX_NT
#define WQ_AUX AUX_NT
#define W2_AUX 0
struct Grp { v4u r[2][3]; };

__global__ void __launch_bounds__(NTHR, 2)
fused_decode(const u16* __restrict__ x, const u16* __restrict__ Kc, const u16* __restrict__ Vc, const u16* __restrict__ wq,
             const u16* __restrict__ wo, const u16* __restrict__ w1, const u16* __restrict__ w3, const u16* __restrict__ w2,
             const u16* __restrict__ gw, u16* __restrict__ out, unsigned char* ws) {
  __shared__ __attribute__((aligned(16))) unsigned char lds_raw[L_BYTES];
  LAS unsigned char* L = (LAS unsigned char*)lds_raw;
  const int tid = threadIdx.x, lane = tid & 63, w = __builtin_amdgcn_readfirstlane(tid >> 6);
  const int k = lane >> 3, c = lane & 7;
  const int blk = blockIdx.x, xg = blk & 7, idx = blk >> 3;
  const int h = xg * 4 + (idx & 3), sh = idx >> 2, b = sh >> 1, s = sh & 1;
  const int j = h * 2 + s;
  STAMP(0);
#define PHASE 0
  unsigned T0 = now32() + T0OFF + idx * PHASE * (TAU16 / 32) / (16 * 32);
  const __amdgpu_buffer_rsrc_t rws = rsrc(ws, WS_BYTES);
  const v4u tiny = __builtin_nontemporal_load((const GAS v4u*)(tid == 320 ? (const unsigned char*)ws + WS_EPOCH : (const unsigned char*)x + (tid < 320 ? tid : 0) * 16));
  const int qr = tid / 24, qc = tid - qr * 24;
  v4u wqv[4], fwa, fwb;
  {
    const __amdgpu_buffer_rsrc_t rq = rsrc(wq + h * DH, (DM * NH * DH - h * DH) * 2);
    const int oob = tid < 480 ? 0 : OOB;
#pragma unroll
    for (int e = 0; e < 4; ++e) wqv[e] = ld16<WQ_AUX>(rq, (sh * 80 + qr * 4) * (NH * DH * 2) + qc * 16 + oob, e * (NH * DH * 2));
    const int row0 = (idx >> 1) * 40 + qr * 2;
    const __amdgpu_buffer_rsrc_t r13 = rsrc(((idx & 1) ? w3 : w1) + xg * 192, (DM * FF - xg * 192) * 2);
    fwa = ld16<WQ_AUX>(r13, row0 * (FF * 2) + qc * 16 + oob, 0); fwb = ld16<WQ_AUX>(r13, row0 * (FF * 2) + qc * 16 + oob, FF * 2);
  }
  const size_t kvoff = ((size_t)(b * NH + h) * SK + (size_t)s * KEYS_WG) * DH;
  const u16* Kb = Kc + kvoff; const u16* Vb = Vc + kvoff;
  const int kvo = w * GRPB + k * ROWB + c * 16;
#define WPH 1
#define GPH 0
  unsigned tn = (T0 << 4) + (WPH == 1 ? w * (TAU16 / 8) : WPH == 2 ? (w >> 2) * (TAU16 / 2) : 0) + (idx & 3) * (TAU16 * GPH / 32);
  auto pace = [&](unsigned step16) {
    const int late = (int)((now32() << 4) - tn);
    if (late < 0) {
      __builtin_amdgcn_s_setprio(0);
      while ((int)((now32() << 4) - tn) < 0) __builtin_amdgcn_s_sleep(1);
    }
    else if (late > 2 * TAU16) __builtin_amdgcn_s_setprio(3);
    else if (late > TAU16) __builtin_amdgcn_s_setprio(2);
    else if (late > TAU16 / 4) __builtin_amdgcn_s_setprio(1);
    else __builtin_amdgcn_s_setprio(0);
    tn += step16;
  };
#define ROT 2
  const int rho = ROT == 0 ? 0 : ROT == 1 ? ((blk * 11) & 31) : ROT == 2 ? (((blk >> 3) + (blk & 7) * 4) & 31) : ((int)((blk * 2654435761u) >> 27));
  unsigned tau = TAU16;
  int wo_t = -1;
  __amdgpu_buffer_rsrc_t ro = rsrc(wo + (size_t)h * DH * DM, DH * DM * 2);
  int wo_vo = 0;
  auto issue = [&](v4u& r00, v4u& r01, v4u& r02, v4u& r10, v4u& r11, v4u& r12, int m) {
    TR(1);
    pace(tau + (wo_t >= 0 ? XDMA16 : 0));
    TR(2);
    if (wo_t >= 0) dma16(ro, wo_vo, wo_t * 6 * (DM * 2), (unsigned)(L_FP + w * 8192 + (wo_t & 7) * 1024));
    const __amdgpu_buffer_rsrc_t r = rsrc(m < NGW ? Kb : Vb, KVB);
    const int so = ((m + rho) & (NGW - 1)) * (NWAVE * GRPB);
    r00 = ld16<KV_AUX>(r, kvo, so); r01 = ld16<KV_AUX>(r, kvo + 128, so); r02 = ld16<KV_AUX>(r, kvo + 256, so);
    r10 = ld16<KV_AUX>(r, kvo, so + TILEB); r11 = ld16<KV_AUX>(r, kvo + 128, so + TILEB); r12 = ld16<KV_AUX>(r, kvo + 256, so + TILEB);
    SCHED_FENCE();
    TR(3);
  };
#define ISSUE(G, m) issue(G.r[0][0], G.r[0][1], G.r[0][2], G.r[1][0], G.r[1][1], G.r[1][2], m)
#define ISSUE_W(o, m) issue(W[o], W[o + 1], W[o + 2], W[o + 3], W[o + 4], W[o + 5], m)
  Grp A, B;
  v4u W[32];
  ISSUE(A, 0);
  ISSUE(B, 1);
  tn += XHEAD16;
  STAMP(1);
  if (tid <= 320) *(LAS v4u*)(L + (tid < 320 ? L_XS + tid * 16 : L_MISC + 16)) = tiny;
  if (tid == 511) *(LAS unsigned*)(L + L_MISC + 12) = TAU16;
  __syncthreads();
  const unsigned tagA = __builtin_amdgcn_readfirstlane(*(LAS unsigned*)(L + L_MISC + 16)) + 1u, tagB = __builtin_amdgcn_readfirstlane(*(LAS unsigned*)(L + L_MISC + 20)) + 1u;
  v2u xq[4]; unsigned fx4[4];
  { const int qrc = qr < 20 ? qr : 0;
#pragma unroll
    for (int e = 0; e < 4; ++e) { xq[e] = *(LAS v2u*)(L + L_XS + e * (DM * 2) + (sh * 80 + qrc * 4) * 2); fx4[e] = *(LAS unsigned*)(L + L_XS + e * (DM * 2) + ((idx >> 1) * 40 + qrc * 2) * 2); } }
  {
    unsigned lo01[4], hi01[4], lo23[4], hi23[4];
#pragma unroll
    for (int d = 0; d < 4; ++d) {
      lo01[d] = __builtin_amdgcn_perm(wqv[1][d], wqv[0][d], 0x05040100u); hi01[d] = __builtin_amdgcn_perm(wqv[1][d], wqv[0][d], 0x07060302u);
      lo23[d] = __builtin_amdgcn_perm(wqv[3][d], wqv[2][d], 0x05040100u); hi23[d] = __builtin_amdgcn_perm(wqv[3][d], wqv[2][d], 0x07060302u);
    }
#pragma unroll
    for (int bb = 0; bb < 4; ++bb) {
      float a[8];
#pragma unroll
      for (int d = 0; d < 4; ++d) { a[2 * d] = dot2(lo23[d], xq[bb][1], dot2(lo01[d], xq[bb][0], 0.f)); a[2 * d + 1] = dot2(hi23[d], xq[bb][1], dot2(hi01[d], xq[bb][0], 0.f)); }
      LAS v4f* p = (LAS v4f*)(L + L_FP + ((qr * 4 + bb) * 192 + qc * 8) * 4);
      p[0] = v4f{a[0], a[1], a[2], a[3]}; p[1] = v4f{a[4], a[5], a[6], a[7]};
    }
  }
  {
    unsigned lo[4], hi[4];
#pragma unroll
    for (int d = 0; d < 4; ++d) { lo[d] = __builtin_amdgcn_perm(fwb[d], fwa[d], 0x05040100u); hi[d] = __builtin_amdgcn_perm(fwb[d], fwa[d], 0x07060302u); }
#pragma unroll
    for (int bb = 0; bb < 4; ++bb) {
      LAS v4f* p = (LAS v4f*)(L + L_FP2 + ((qr * 4 + bb) * 192 + qc * 8) * 4);
      p[0] = v4f{dot2(lo[0], fx4[bb], 0.f), dot2(hi[0], fx4[bb], 0.f), dot2(lo[1], fx4[bb], 0.f), dot2(hi[1], fx4[bb], 0.f)};
      p[1] = v4f{dot2(lo[2], fx4[bb], 0.f), dot2(hi[2], fx4[bb], 0.f), dot2(lo[3], fx4[bb], 0.f), dot2(hi[3], fx4[bb], 0.f)};
    }
  }
  TR(9);
  __syncthreads();
  TR(10);
  STAMP(2);
  if (tid < 384) {
    const int bb = tid / 96, p = tid - bb * 96;
    float a0 = 0.f, a1 = 0.f;
#pragma unroll
    for (int r = 0; r < 20; ++r) { const v2f v = *(LAS v2f*)(L + L_FP + ((r * 4 + bb) * 192 + p * 2) * 4); a0 += v[0]; a1 += v[1]; }
    __builtin_amdgcn_raw_buffer_store_b128(v4u{__float_as_uint(a0), tagA, __float_as_uint(a1), tagB}, rws, WS_QX + (((h * 4 + bb) * 8 + sh) * 128 + p) * 16, 0, AUX_SC1);
    a0 = 0.f; a1 = 0.f;
#pragma unroll
    for (int r = 0; r < 20; ++r) { const v2f v = *(LAS v2f*)(L + L_FP2 + ((r * 4 + bb) * 192 + p * 2) * 4); a0 += v[0]; a1 += v[1]; }
    __builtin_amdgcn_raw_buffer_store_b128(v4u{__float_as_uint(a0), tagA, __float_as_uint(a1), tagB}, rws,
                                           WS_FX + (((((xg * 4 + bb) * 8 + p / 12) * 2 + (idx & 1)) * 16 + (idx >> 1)) * 12 + p % 12) * 16, 0, AUX_SC1);
  }
  if (tid >= 384 && tid < 388)
    __builtin_amdgcn_raw_buffer_store_b128(v4u{T0, tagA, T0, tagB}, rws, WS_QX + (((h * 4 + (tid - 384)) * 8 + sh) * 128 + 96) * 16, 0, AUX_SC1);
  SCHED_FENCE();
  TR(11);
  ISSUE_W(0, 2);
  ISSUE_W(6, 3);
  ISSUE_W(12, 4);
  TR(12);
  if (w == 0) {
    __builtin_amdgcn_s_setprio(3);
    const int qo = WS_QX + (h * 4 + b) * 8 * 2048 + lane * 16;
    v4u ga[8], gb[8];
    for (unsigned spins = 0;;) {
#pragma unroll
      for (int p = 0; p < 8; ++p) { ga[p] = poll16(rws, qo + p * 2048); gb[p] = poll16(rws, qo + p * 2048 + 1024 + (lane <= 32 ? 0 : OOB)); }
      bool ok = true;
#pragma unroll
      for (int p = 0; p < 8; ++p) ok = ok & (ga[p][1] == tagA) & (ga[p][3] == tagB) & ((lane > 32) | ((gb[p][1] == tagA) & (gb[p][3] == tagB)));
      TR(17);
      if (__all(ok) || ++spins > SPIN_CAP) break;
      __builtin_amdgcn_s_sleep(1);
    }
    TR(16);
    float a0 = 0.f, a1 = 0.f, b0 = 0.f, b1 = 0.f;
#pragma unroll
    for (int p = 0; p < 8; ++p) { a0 += __uint_as_float(ga[p][0]); a1 += __uint_as_float(ga[p][2]); b0 += __uint_as_float(gb[p][0]); b1 += __uint_as_float(gb[p][2]); }
    *(LAS unsigned*)(L + L_QB + lane * 4) = cvtpk(a0, a1);
    if (lane < 32) *(LAS unsigned*)(L + L_QB + 256 + lane * 4) = cvtpk(b0, b1);
    unsigned tl = gb[0][0];
#pragma unroll
    for (int p = 1; p < 8; ++p) tl = (int)(gb[p][0] - tl) > 0 ? gb[p][0] : tl;
    if (lane == 32) *(LAS unsigned*)(L + L_MISC + 8) = tl;
  }
  TR(13);
  tn += XPOLL16;
  ISSUE_W(18, 5);
  ISSUE_W(24, 6);
  __syncthreads();
  TR(14);
  STAMP(3);
  { const unsigned tl = __builtin_amdgcn_readfirstlane(*(LAS unsigned*)(L + L_MISC + 8)); if (tl - T0 < 1000u) tn += (tl - T0) << 4; }
#define SLACK 3
  { const unsigned t = (now32() << 4) - SLACK * TAU16; if ((int)(t - tn) > 0) tn = t; }
  v4u Q[3];
#pragma unroll
  for (int i = 0; i < 3; ++i) Q[i] = *(LAS v4u*)(L + L_QB + (c + 8 * i) * 16);

  float mx = -INFINITY;
  const bool oct0 = c == 0;
  const int scoff = L_SC + w * 2048 + k * 8;
  auto k_tile = [&](const v4u& r0, const v4u& r1, const v4u& r2) -> float {
    float a0 = dot2(r0[0], Q[0][0], 0.f), a1 = dot2(r1[0], Q[1][0], 0.f), a2 = dot2(r2[0], Q[2][0], 0.f);
#pragma unroll
    for (int d = 1; d < 4; ++d) { a0 = dot2(r0[d], Q[0][d], a0); a1 = dot2(r1[d], Q[1][d], a1); a2 = dot2(r2[d], Q[2][d], a2); }
    return (a0 + a1) + a2;
  };
  auto k_put = [&](float s0, float s1, int n) {
    s0 = oct_sum(s0); s1 = oct_sum(s1);
    mx = fmaxf(mx, fmaxf(s0, s1));
    if (oct0) *(LAS v2f*)(L + scoff + n * 64) = v2f{s0, s1};
  };
#define K_STEP(G, n, m) do { float s0_ = k_tile(G.r[0][0], G.r[0][1], G.r[0][2]), s1_ = k_tile(G.r[1][0], G.r[1][1], G.r[1][2]); \
    asm volatile("" : "+v"(s0_), "+v"(s1_) : : "memory"); ISSUE(G, m); k_put(s0_, s1_, n); } while (0)
  K_STEP(A, 0, 7);
#pragma unroll
  for (int g = 0; g < 5; ++g) k_put(k_tile(W[g * 6], W[g * 6 + 1], W[g * 6 + 2]), k_tile(W[g * 6 + 3], W[g * 6 + 4], W[g * 6 + 5]), 2 + g);
  STAMP(4);
  K_STEP(B, 1, 8);
  STAMP(5);
  {
    const int lane = fresh_lane(), tid2 = w * 64 + lane;
    const int yr = lane / 10, yc = lane - yr * 10;
    const int fxo = WS_FX + ((xg * 4 + b) * 8 + (j & 7)) * 6144 + tid2 * 16 + (tid2 < 384 ? 0 : OOB);
    v4u fx = poll16(rws, fxo);
    const __amdgpu_buffer_rsrc_t r2 = rsrc(w2, FF * DM * 2), rg = rsrc(gw, w == 0 ? DM * 2 : 0);
    const int vo2 = (j * 24 + yr) * (DM * 2) + w * 160 + yc * 16 + (lane < 60 ? 0 : OOB);
#pragma unroll
    for (int t = 0; t < 4; ++t) W[8 + t] = ld16<W2_AUX>(r2, vo2, t * 6 * (DM * 2));
    const v4u gg0 = ld16<AUX_NT>(rg, lane * 16, 0), gg1 = ld16<AUX_NT>(rg, 1024 + lane * 16, 0);
    SCHED_FENCE();
    TR(30);
    tn += XFFN16;
    K_STEP(A, 7, 9); K_STEP(B, 8, 10);
    TR(31);
    for (unsigned spins = 0; !__all((tid2 >= 384) | ((fx[1] == tagA) & (fx[3] == tagB))); ) {
      if (++spins > SPIN_CAP) break;
      __builtin_amdgcn_s_sleep(2);
      fx = poll16(rws, fxo);
    }
    TR(32);
    if (tid2 < 384) *(LAS v2f*)(L + L_G + ((tid2 / 12) * 24 + (tid2 % 12) * 2) * 4) = v2f{__uint_as_float(fx[0]), __uint_as_float(fx[2])};
    float gate = 0.f;
    if (w == 0) {
      const v4u gx0 = *(LAS v4u*)(L + L_XS + b * (DM * 2) + lane * 16), gx1 = *(LAS v4u*)(L + L_XS + b * (DM * 2) + 1024 + (lane & 15) * 16);
      float d = 0.f;
#pragma unroll
      for (int e = 0; e < 4; ++e) { d = dot2(gx0[e], gg0[e], d); d = dot2(gx1[e], gg1[e], d); }
      gate = 1.f / (1.f + __expf(-wave_sum(d)));
      if (lane == 0) *(LAS float*)(L + L_MISC) = gate;
    }
    __syncthreads();
    TR(33);
    if (tid2 < 24) {
      const LAS float* g = (const LAS float*)(L + L_G) + tid2;
      float ga = 0.f, up = 0.f;
#pragma unroll
      for (int r = 0; r < 16; ++r) { ga += g[r * 24]; up += g[(16 + r) * 24]; }
      *(LAS float*)(L + L_MISC + 64 + tid2 * 4) = ga / (1.f + __expf(-ga)) * up * (1.f - gate);
    }
    __syncthreads();
    TR(34);
    v2f ya[4];
#pragma unroll
    for (int e = 0; e < 4; ++e) ya[e] = v2f{0.f, 0.f};
#pragma unroll
    for (int t = 0; t < 4; ++t) {
      const float hv = *(LAS float*)(L + L_MISC + 64 + (t * 6 + (yr < 6 ? yr : 0)) * 4);
#pragma unroll
      for (int d = 0; d < 4; ++d) ya[d] = __builtin_elementwise_fma(v2f{hv, hv}, v2f{bflo(W[8 + t][d]), bfhi(W[8 + t][d])}, ya[d]);
    }
    if (lane < 60) {
      LAS v4f* p = (LAS v4f*)(L + L_YF + (w * 480 + yr * 80 + yc * 8) * 4);
      p[0] = v4f{ya[0][0], ya[0][1], ya[1][0], ya[1][1]}; p[1] = v4f{ya[2][0], ya[2][1], ya[3][0], ya[3][1]};
    }
    wo_vo = yr * (DM * 2) + w * 160 + yc * 16 + (lane < 60 ? 0 : OOB);
  }
  MEM_FENCE();
  TR(35);
  K_STEP(A, 9, 11); K_STEP(B, 10, 12);
  STAMP(13);
#define WO_COPY(t0) do { asm volatile("s_waitcnt vmcnt(12)" ::: "memory"); const int ln_ = fresh_lane(); \
    _Pragma("unroll") for (int t = 0; t < 8; t += 2) {     \
      const v4u a_ = *(LAS v4u*)(L + L_FP + w * 8192 + t * 1024 + ln_ * 16), b_ = *(LAS v4u*)(L + L_FP + w * 8192 + (t + 1) * 1024 + ln_ * 16); \
      _Pragma("unroll") for (int d = 0; d < 4; ++d) { W[t0 + t][d] = __builtin_amdgcn_perm(b_[d], a_[d], 0x05040100u); W[t0 + t + 1][d] = __builtin_amdgcn_perm(b_[d], a_[d], 0x07060302u); } } \
    asm volatile("" : "+v"(W[t0]), "+v"(W[t0 + 1]), "+v"(W[t0 + 2]), "+v"(W[t0 + 3]), "+v"(W[t0 + 4]), "+v"(W[t0 + 5]), "+v"(W[t0 + 6]), "+v"(W[t0 + 7]) : : "memory"); } while (0)
#define CHK_BEGIN(pubc, rdc)
#define CHK_END()
  int n;
#pragma unroll 1
  for (n = 11; n < 21; n += 2) {
    CHK_BEGIN(n == 15 ? 0 : -1, n == 17 ? 0 : -1);
    wo_t = n < 19 ? n - 11 : -1; K_STEP(A, n, n + 2);
    wo_t = n < 19 ? n - 10 : -1; K_STEP(B, n + 1, n + 3);
    CHK_END();
  }
  WO_COPY(0);
#pragma unroll 1
  for (; n < 31; n += 2) {
    CHK_BEGIN(n == 23 ? 1 : -1, n == 25 ? 1 : -1);
    wo_t = n < 29 ? n - 13 : -1; K_STEP(A, n, n + 2);
    wo_t = n < 29 ? n - 12 : -1; K_STEP(B, n + 1, n + 3);
    CHK_END();
  }
  WO_COPY(8);
  wo_t = -1;
  K_STEP(A, 31, 33);

  STAMP(6);
  mx = fmaxf(mx, dpp<XOR8>(mx)); mx = max16_32(mx);
  const float mw = mx * CS;
  LDS_FENCE();
  {
    const int lane = fresh_lane();
    LAS v4f* p = (LAS v4f*)(L + L_SC + w * 2048 + lane * 32);
    v4f a = p[0], bb = p[1];
    float l = 0.f;
#pragma unroll
    for (int e = 0; e < 4; ++e) { a[e] = ex2(fmaf(a[e], CS, -mw)); bb[e] = ex2(fmaf(bb[e], CS, -mw)); l += a[e] + bb[e]; }
    p[0] = a; p[1] = bb;
    l = wave_sum(l);
    if (lane == 0) __builtin_amdgcn_raw_buffer_store_b128(v4u{__float_as_uint(mw), tagA, __float_as_uint(l), tagB}, rws, WS_ML + (((b * NH + h) * 2 + s) * 8 + w) * 16, 0, AUX_SC1);
  }
  LDS_FENCE();
  STAMP(11);

  v2f acc2[12];
#pragma unroll
  for (int e = 0; e < 12; ++e) acc2[e] = v2f{0.f, 0.f};
#define PIN_ACC() asm volatile("" : "+v"(acc2[0]), "+v"(acc2[1]), "+v"(acc2[2]), "+v"(acc2[3]), "+v"(acc2[4]), "+v"(acc2[5]), "+v"(acc2[6]), "+v"(acc2[7]), \
                                    "+v"(acc2[8]), "+v"(acc2[9]), "+v"(acc2[10]), "+v"(acc2[11]) : : "memory")
  auto v_tile = [&](const v4u& r0, const v4u& r1, const v4u& r2, float p) {
    const v2f pp = v2f{p, p};
#pragma unroll
    for (int d = 0; d < 4; ++d) {
      acc2[d] = __builtin_elementwise_fma(pp, v2f{bflo(r0[d]), bfhi(r0[d])}, acc2[d]);
      acc2[4 + d] = __builtin_elementwise_fma(pp, v2f{bflo(r1[d]), bfhi(r1[d])}, acc2[4 + d]);
      acc2[8 + d] = __builtin_elementwise_fma(pp, v2f{bflo(r2[d]), bfhi(r2[d])}, acc2[8 + d]);
    }
    PIN_ACC();
  };
#define V_USE(G, n) do { const v2f p2_ = *(LAS v2f*)(L + scoff + (n) * 64); v_tile(G.r[0][0], G.r[0][1], G.r[0][2], p2_[0]); v_tile(G.r[1][0], G.r[1][1], G.r[1][2], p2_[1]); } while (0)
#pragma unroll 1
  for (n = 0; n < 10; n += 2) {
    CHK_BEGIN((n & 7) == 0 ? 2 + (n >> 3) : -1, n == 2 ? 2 : -1);
    V_USE(B, n); wo_t = n < 8 ? n + 16 : -1; ISSUE(B, NGW + n + 2);
    V_USE(A, n + 1); wo_t = n < 8 ? n + 17 : -1; ISSUE(A, NGW + n + 3);
    CHK_END();
  }
  WO_COPY(16);
#pragma unroll 1
  for (; n < 20; n += 2) {
    CHK_BEGIN(n == 16 ? 4 : -1, n == 10 ? 3 : n == 18 ? 4 : -1);
    V_USE(B, n); wo_t = n < 18 ? n + 14 : -1; ISSUE(B, NGW + n + 2);
    V_USE(A, n + 1); wo_t = n < 18 ? n + 15 : -1; ISSUE(A, NGW + n + 3);
    CHK_END();
  }
  WO_COPY(24);
  wo_t = -1;
  const int mloff = WS_ML + ((b * NH + h) * 16 + (fresh_lane() & 15)) * 16;
  v4u ml = poll16(rws, mloff);
  V_USE(B, n); ISSUE(B, NGW + n + 2); V_USE(A, n + 1); ISSUE(A, NGW + n + 3);
  for (unsigned spins = 0; !__all((ml[1] == tagA) & (ml[3] == tagB)); ) {
    if (++spins > SPIN_CAP) break;
    __builtin_amdgcn_s_sleep(2);
    ml = poll16(rws, mloff);
  }
  float fw;
  {
    const float m_i = __uint_as_float(ml[0]), l_i = __uint_as_float(ml[2]);
    float M = m_i;
    M = fmaxf(M, dpp<XOR1>(M)); M = fmaxf(M, dpp<XOR2>(M)); M = fmaxf(M, dpp<XOR7>(M)); M = fmaxf(M, dpp<XOR8>(M));
    float Ls = l_i * ex2(m_i - M);
    Ls = oct_sum(Ls); Ls += dpp<XOR8>(Ls);
    fw = __uint_as_float(__builtin_amdgcn_readfirstlane(__float_as_uint(ex2(mw - M) / Ls * *(LAS float*)(L + L_MISC))));
  }
#pragma unroll 1
  for (n += 2; n < NGW - 2; n += 2) {
    V_USE(B, n); ISSUE(B, NGW + n + 2); V_USE(A, n + 1); ISSUE(A, NGW + n + 3);
  }
  V_USE(B, NGW - 2);
  V_USE(A, NGW - 1);

  STAMP(7);
  TR(40);
  {
    float b12[12], c6[6];
#pragma unroll
    for (int i = 0; i < 12; ++i) {
      auto r = __builtin_amdgcn_permlane32_swap(__float_as_uint(acc2[i >> 1][i & 1]), __float_as_uint(acc2[6 + (i >> 1)][i & 1]), false, false);
      b12[i] = __uint_as_float(r[0]) + __uint_as_float(r[1]);
    }
#pragma unroll
    for (int i = 0; i < 6; ++i) {
      auto r = __builtin_amdgcn_permlane16_swap(__float_as_uint(b12[i]), __float_as_uint(b12[i + 6]), false, false);
      c6[i] = __uint_as_float(r[0]) + __uint_as_float(r[1]);
    }
#pragma unroll
    for (int i = 0; i < 6; ++i) c6[i] = (c6[i] + dpp<XOR8>(c6[i])) * fw;
    const int ln = fresh_lane();
    if ((ln & 8) == 0) {
      const int v0 = (ln >> 4) * 6;
#pragma unroll
      for (int p = 0; p < 3; ++p) { const int v = v0 + 2 * p; *(LAS v2f*)(L + L_QP + (w * QPS + (v >> 3) * 64 + (ln & 7) * 8 + (v & 7)) * 4) = v2f{c6[2 * p], c6[2 * p + 1]}; }
    }
  }
  const int ln = fresh_lane();
  const int yr = ln < 60 ? ln / 10 : 0, yc = ln - (ln / 10) * 10;
  float ya[8];
  { const LAS v4f* p = (const LAS v4f*)(L + L_YF + (w * 480 + yr * 80 + yc * 8) * 4); const v4f a = p[0], bb = p[1];
#pragma unroll
    for (int e = 0; e < 4; ++e) { ya[e] = a[e]; ya[4 + e] = bb[e]; } }
  STAMP(8);
  TR(41);
  __syncthreads();
  TR(42);
  if (w < 6) {
    const int pi = w * 16 + (ln >> 2), r = pi >> 4, u = pi & 15, qq = ln & 3;
    const LAS float* p = (const LAS float*)(L + L_QP) + 2 * qq * QPS + u * 12 + r;
    float a0 = p[0] + p[QPS], a1 = p[6] + p[QPS + 6];
    a0 += dpp<XOR1>(a0); a1 += dpp<XOR1>(a1); a0 += dpp<XOR2>(a0); a1 += dpp<XOR2>(a1);
    if (qq == 0) *(LAS unsigned*)(L + L_OB + pi * 4) = cvtpk(a0, a1);
  }
  TR(43);
  __syncthreads();
  TR(44);
  {
    LAS float* yrp = (LAS float*)(L + L_FP) + w * 480;
    const LAS v4u* op = (const LAS v4u*)(L + L_OB + yr * 64);
#pragma unroll
    for (int u4 = 0; u4 < 4; ++u4) {
      const v4u o4 = op[u4];
#pragma unroll
      for (int e = 0; e < 4; ++e) {
        const int u = u4 * 4 + e;
#pragma unroll
        for (int d = 0; d < 4; ++d) { ya[2 * d] = dot2(W[2 * u][d], o4[e], ya[2 * d]); ya[2 * d + 1] = dot2(W[2 * u + 1][d], o4[e], ya[2 * d + 1]); }
      }
    }
    asm volatile("" : "+v"(ya[0]), "+v"(ya[1]), "+v"(ya[2]), "+v"(ya[3]), "+v"(ya[4]), "+v"(ya[5]), "+v"(ya[6]), "+v"(ya[7]));
    TR(45);
    if (ln < 60) { LAS v4f* p = (LAS v4f*)(yrp + yr * 80 + yc * 8); p[0] = v4f{ya[0], ya[1], ya[2], ya[3]}; p[1] = v4f{ya[4], ya[5], ya[6], ya[7]}; }
    LDS_FENCE();
    TR(46);
    if (ln < 40) {
      v2f y = v2f{0.f, 0.f};
#pragma unroll
      for (int r = 0; r < 6; ++r) y += *(LAS v2f*)(yrp + r * 80 + ln * 2);
      const int cons = w * 8 + ln / 5, g5 = ln % 5;
      __builtin_amdgcn_raw_buffer_store_b128(v4u{__float_as_uint(y[0]), tagA, __float_as_uint(y[1]), tagB}, rws, WS_INBOX + (((b * 64 + cons) * 64 + j) * 5 + g5) * 16, 0, AUX_SC1);
    }
  }
  STAMP(9);
  TR(47);
  if (w == 0) {
    const int io = WS_INBOX + ((b * 64 + j) * 64 + ln) * 80;
    v4u g5[5];
    for (unsigned spins = 0;;) {
      bool ok = true;
#pragma unroll
      for (int e = 0; e < 5; ++e) g5[e] = poll16(rws, io + e * 16);
#pragma unroll
      for (int e = 0; e < 5; ++e) ok = ok & (g5[e][1] == tagA) & (g5[e][3] == tagB);
      if (__all(ok) || ++spins > SPIN_CAP) break;
      __builtin_amdgcn_s_sleep(1);
    }
    TR(48);
    const unsigned yv[10] = {g5[0][0], g5[0][2], g5[1][0], g5[1][2], g5[4][0], g5[2][0], g5[2][2], g5[3][0], g5[3][2], g5[4][2]};
    float b5[5];
#pragma unroll
    for (int i = 0; i < 5; ++i) { auto r = __builtin_amdgcn_permlane32_swap(yv[i], yv[5 + i], false, false); b5[i] = __uint_as_float(r[0]) + __uint_as_float(r[1]); }
    float z[3];
    { auto r = __builtin_amdgcn_permlane16_swap(__float_as_uint(b5[0]), __float_as_uint(b5[2]), false, false); z[0] = __uint_as_float(r[0]) + __uint_as_float(r[1]); }
    { auto r = __builtin_amdgcn_permlane16_swap(__float_as_uint(b5[1]), __float_as_uint(b5[3]), false, false); z[1] = __uint_as_float(r[0]) + __uint_as_float(r[1]); }
    { auto r = __builtin_amdgcn_permlane16_swap(__float_as_uint(b5[4]), __float_as_uint(b5[4]), false, false); z[2] = __uint_as_float(r[0]) + __uint_as_float(r[1]); }
#pragma unroll
    for (int i = 0; i < 3; ++i) { z[i] += dpp<XOR1>(z[i]); z[i] += dpp<XOR2>(z[i]); z[i] += dpp<XOR7>(z[i]); z[i] += dpp<XOR8>(z[i]); }
    auto r89 = __builtin_amdgcn_permlane32_swap(__float_as_uint(z[2]), __float_as_uint(z[2]), false, false);
    const unsigned o03 = cvtpk(z[0], z[1]), o4 = cvtpk(__uint_as_float(r89[0]), __uint_as_float(r89[1]));
    if ((ln & 15) == 0 || ln == 1) *(GAS unsigned*)(out + b * DM + j * 10 + (ln == 1 ? 8 : (ln >> 4) * 2)) = ln == 1 ? o4 : o03;
    if (b == 0 && j == 0 && ln == 0) __hip_atomic_store((GAS u64*)(ws + WS_EPOCH), ((u64)tagB << 32) | tagA, __ATOMIC_RELAXED, __HIP_MEMORY_SCOPE_AGENT);
  }
  TR(49);
}
}

extern "C" void kernel_launch(void* const* d_in, const int* in_sizes, int n_in, void* d_out, int out_size, void* d_ws, size_t ws_size,
                              hipStream_t stream) {
  (void)in_sizes; (void)n_in; (void)out_size; (void)ws_size;
  hipLaunchKernelGGL(fused_decode, dim3(NWG), dim3(NTHR), 0, stream, (const u16*)d_in[0], (const u16*)d_in[1], (const u16*)d_in[2], (const u16*)d_in[3],
                     (const u16*)d_in[4], (const u16*)d_in[6], (const u16*)d_in[7], (const u16*)d_in[8], (const u16*)d_in[9], (u16*)d_out, (unsigned char*)d_ws);
}
```

```cpp
#include <hip/hip_runtime.h>
#include <hip/hip_bf16.h>
#include <stdint.h>

#define GAS __attribute__((address_space(1)))
#define LAS __attribute__((address_space(3)))
typedef unsigned v4u __attribute__((ext_vector_type(4)));
typedef unsigned v2u __attribute__((ext_vector_type(2)));
typedef float v4f __attribute__((ext_vector_type(4)));
typedef float v2f __attribute__((ext_vector_type(2)));
typedef __bf16 bf16x2 __attribute__((ext_vector_type(2)));
typedef unsigned long long u64;
typedef unsigned short u16;

namespace {
constexpr int NB = 4, NH = 32, SK = 8192, DH = 192, DM = 640, FF = 1536;
constexpr int NWG = 256, NWAVE = 8, NTHR = NWAVE * 64;
constexpr int KEYS_WG = SK / 2;
constexpr int ROWB = DH * 2;
constexpr int TILEB = 8 * ROWB;
constexpr int GRPB = 2 * TILEB;
constexpr int KVB = KEYS_WG * ROWB;
constexpr int NGW = KVB / GRPB / NWAVE;
constexpr int RSRC3 = 0x00020000;
#define POLL_AUX 16
#define STORE_AUX 16
constexpr int AUX_NT = 2, AUX_SC1 = STORE_AUX;
constexpr int OOB = 1 << 28;
constexpr float CS = 0.07216878364870322f * 1.4426950408889634f;
#define TAU16 (174 * 16)
#define XHEAD16 (TAU16 * 10 / 10)
#define XPOLL16 (TAU16 * 3 / 10)
#define XFFN16 (TAU16 * 3 / 10)
#define XDMA16 (TAU16 * 3 / 100)
#define ENDSYNC 0
#define ADAPT 0
#define TAUMIN16 (171 * 16)
#define TAUMAX16 (188 * 16)
#define TAUUP16 64
#define TAUDN16 32
#define MISS_HI 12
#define MISS_LO 4
#define ALPHA 0
#define PRIO 1
#define PHW 0

constexpr int WS_EPOCH = 0;
constexpr int WS_ML = 4096;
constexpr int WS_INBOX = 65536;
constexpr int WS_QX = WS_INBOX + NB * 64 * 64 * 80;
constexpr int WS_FX = WS_QX + NH * NB * 8 * 128 * 16;
constexpr int WS_FL = WS_FX + 8 * NB * 8 * 384 * 16;
constexpr int WS_BYTES = WS_FL + 8 * 1024;

constexpr int L_XS = 0;
constexpr int L_QP = 5120;
constexpr int QPS = 196;
constexpr int L_QB = L_QP + 8 * QPS * 4;
constexpr int L_OB = L_QB + 384;
constexpr int L_MISC = L_OB + 384;
constexpr int L_G = L_MISC + 256;
constexpr int L_YF = L_G + 3072;
constexpr int L_SC = L_YF + 8 * 480 * 4;
constexpr int L_FP2 = L_YF;
constexpr int L_FP = L_YF + 22 * 4 * 192 * 4;
constexpr int L_TR = L_FP + 22 * 4 * 192 * 4;
constexpr int L_BYTES = L_TR;

constexpr unsigned SPIN_CAP = 1u << 17;

__device__ __forceinline__ float dot2(unsigned a, unsigned b, float c) {
  return __builtin_amdgcn_fdot2_f32_bf16(__builtin_bit_cast(bf16x2, a), __builtin_bit_cast(bf16x2, b), c, false);
}
template <int CTRL> __device__ __forceinline__ float dpp(float x) {
  return __builtin_bit_cast(float, __builtin_amdgcn_mov_dpp(__builtin_bit_cast(int, x), CTRL, 0xf, 0xf, true));
}
constexpr int XOR1 = 0xB1, XOR2 = 0x4E, XOR7 = 0x141, XOR8 = 0x128;
__device__ __forceinline__ float sum16_32(float x) {
  auto s = __builtin_amdgcn_permlane16_swap(__float_as_uint(x), __float_as_uint(x), false, false);
  x = __uint_as_float(s[0]) + __uint_as_float(s[1]);
  auto t = __builtin_amdgcn_permlane32_swap(__float_as_uint(x), __float_as_uint(x), false, false);
  return __uint_as_float(t[0]) + __uint_as_float(t[1]);
}
__device__ __forceinline__ float max16_32(float x) {
  auto s = __builtin_amdgcn_permlane16_swap(__float_as_uint(x), __float_as_uint(x), false, false);
  x = fmaxf(__uint_as_float(s[0]), __uint_as_float(s[1]));
  auto t = __builtin_amdgcn_permlane32_swap(__float_as_uint(x), __float_as_uint(x), false, false);
  return fmaxf(__uint_as_float(t[0]), __uint_as_float(t[1]));
}
__device__ __forceinline__ float oct_sum(float x) { x += dpp<XOR1>(x); x += dpp<XOR2>(x); x += dpp<XOR7>(x); return x; }
__device__ __forceinline__ float xoct_sum(float x) { x += dpp<XOR8>(x); return sum16_32(x); }
__device__ __forceinline__ float wave_sum(float x) { return xoct_sum(oct_sum(x)); }
__device__ __forceinline__ unsigned cvtpk(float lo, float hi) { unsigned r; asm("v_cvt_pk_bf16_f32 %0, %1, %2" : "=v"(r) : "v"(lo), "v"(hi)); return r; }
__device__ __forceinline__ float bflo(unsigned u) { return __uint_as_float(u << 16); }
__device__ __forceinline__ float bfhi(unsigned u) { return __uint_as_float(u & 0xffff0000u); }
__device__ __forceinline__ float ex2(float x) { return __builtin_amdgcn_exp2f(x); }
__device__ __forceinline__ __amdgpu_buffer_rsrc_t rsrc(const void* p, int bytes) { return __builtin_amdgcn_make_buffer_rsrc((void*)p, (short)0, bytes, RSRC3); }
template <int AUX> __device__ __forceinline__ v4u ld16(__amdgpu_buffer_rsrc_t r, int voff, int soff) { return __builtin_amdgcn_raw_buffer_load_b128(r, voff, soff, AUX); }
template <int AUX> __device__ __forceinline__ v2u ld8(__amdgpu_buffer_rsrc_t r, int voff, int soff) { return __builtin_amdgcn_raw_buffer_load_b64(r, voff, soff, AUX); }
__device__ __forceinline__ unsigned now32() { return (unsigned)__builtin_amdgcn_s_memrealtime(); }
#define STAMP(i) do { } while (0)
#define TR(id) do { } while (0)
#define TRV(id, val) do { } while (0)
#define SCHED_FENCE() __builtin_amdgcn_sched_barrier(0)
#define LDS_FENCE() asm volatile("s_waitcnt lgkmcnt(0)" ::: "memory")
#define MEM_FENCE() asm volatile("" ::: "memory")
__device__ __forceinline__ v4u poll16(__amdgpu_buffer_rsrc_t r, int voff) { asm volatile("" : "+v"(voff) : : "memory"); return __builtin_amdgcn_raw_buffer_load_b128(r, voff, 0, POLL_AUX); }
__device__ __forceinline__ int fresh_lane() { int l; asm volatile("v_mbcnt_lo_u32_b32 %0, -1, 0\n\tv_mbcnt_hi_u32_b32 %0, -1, %0" : "=v"(l)); return l; }

#define WO_POLICY ""
__device__ __forceinline__ void dma16(__amdgpu_buffer_rsrc_t r, int voff, int soff, unsigned lds) {
  unsigned keep;
  asm volatile("s_mov_b32 %0, m0\n\ts_mov_b32 m0, %3\n\ts_nop 0\n\tbuffer_load_dwordx4 %1, %2, %4 offen " WO_POLICY " lds\n\ts_mov_b32 m0, %0"
               : "=&s"(keep) : "v"(voff), "s"(r), "s"(lds), "s"(soff) : "memory");
}
#define KV_AUX AUX_NT
#define WQ_AUX AUX_NT
#define W2_AUX 0
struct Grp { v4u r[2][3]; };

__global__ void __launch_bounds__(NTHR, 2)
fused_decode(const u16* __restrict__ x, const u16* __restrict__ Kc, const u16* __restrict__ Vc, const u16* __restrict__ wq,
             const u16* __restrict__ wo, const u16* __restrict__ w1, const u16* __restrict__ w3, const u16* __restrict__ w2,
             const u16* __restrict__ gw, u16* __restrict__ out, unsigned char* ws) {
  __shared__ __attribute__((aligned(16))) unsigned char lds_raw[L_BYTES];
  LAS unsigned char* L = (LAS unsigned char*)lds_raw;
  const int tid = threadIdx.x, lane = tid & 63, w = __builtin_amdgcn_readfirstlane(tid >> 6);
  const int k = lane >> 3, c = lane & 7;
  const int blk = blockIdx.x, xg = blk & 7, idx = blk >> 3;
  const int h = xg * 4 + (idx & 3), sh = idx >> 2, b = sh >> 1, s = sh & 1;
  const int j = h * 2 + s;
  STAMP(0);
#define PHASE 0
#define T0OFF 50
  unsigned T0 = now32() + T0OFF + idx * PHASE * (TAU16 / 32) / (16 * 32);
  const __amdgpu_buffer_rsrc_t rws = rsrc(ws, WS_BYTES);
  const v4u tiny = __builtin_nontemporal_load((const GAS v4u*)(tid == 320 ? (const unsigned char*)ws + WS_EPOCH : (const unsigned char*)x + (tid < 320 ? tid : 0) * 16));
  const int qr = tid / 24, qc = tid - qr * 24;
  v4u wqv[4], fwa, fwb;
  {
    const __amdgpu_buffer_rsrc_t rq = rsrc(wq + h * DH, (DM * NH * DH - h * DH) * 2);
    const int oob = tid < 480 ? 0 : OOB;
#pragma unroll
    for (int e = 0; e < 4; ++e) wqv[e] = ld16<WQ_AUX>(rq, (sh * 80 + qr * 4) * (NH * DH * 2) + qc * 16 + oob, e * (NH * DH * 2));
    const int row0 = (idx >> 1) * 40 + qr * 2;
    const __amdgpu_buffer_rsrc_t r13 = rsrc(((idx & 1) ? w3 : w1) + xg * 192, (DM * FF - xg * 192) * 2);
    fwa = ld16<WQ_AUX>(r13, row0 * (FF * 2) + qc * 16 + oob, 0); fwb = ld16<WQ_AUX>(r13, row0 * (FF * 2) + qc * 16 + oob, FF * 2);
  }
  const size_t kvoff = ((size_t)(b * NH + h) * SK + (size_t)s * KEYS_WG) * DH;
  const u16* Kb = Kc + kvoff; const u16* Vb = Vc + kvoff;
#define SIGMA 1
  const int sigma = SIGMA == 0 ? 0 : SIGMA == 1 ? (int)((blockIdx.x * 2246822519u) >> 29) : (int)((blockIdx.x >> 3) & 7);
  const int kvo = ((w + sigma) & 7) * GRPB + k * ROWB + c * 16;
#define WPH 1
#define GPH 0
  bool alpha_on = false;
  unsigned tn = (T0 << 4) + (WPH == 1 ? w * (TAU16 / 8) : WPH == 2 ? (w >> 2) * (TAU16 / 2) : 0) + (idx & 3) * (TAU16 * GPH / 32);
  unsigned tmin = tn;
  auto pace = [&](unsigned step16) {
    const unsigned tt = (ALPHA && (int)(tmin - tn) > 0) ? tmin : tn;
    unsigned now = now32() << 4;
    const int late = (int)(now - tn);
    if (late > 2 * TAU16) __builtin_amdgcn_s_setprio(3);
    else if (late > TAU16) __builtin_amdgcn_s_setprio(2);
    else if (late > TAU16 / 4) __builtin_amdgcn_s_setprio(1);
    else __builtin_amdgcn_s_setprio(0);
    while ((int)(now - tt) < 0) { __builtin_amdgcn_s_sleep(1); now = now32() << 4; }
    if (ALPHA) tmin = now + (alpha_on ? TAU16 * ALPHA / 100 : 0);
    tn += step16;
  };
#define ROT 2
  const int rho = ROT == 0 ? 0 : ROT == 1 ? ((blk * 11) & 31) : ROT == 2 ? (((blk >> 3) + (blk & 7) * 4) & 31) : ROT == 3 ? ((int)((blk * 2654435761u) >> 27)) : (((blk >> 3) * 5 + (blk & 7) * 3) & 31);
#define AMUL 0
  const int amul = AMUL == 0 ? 1 : AMUL == 1 ? (int)(((blk * 0x9E3779B1u) >> 28) * 2 + 1) : (int)((((blk >> 3) + 3 * (blk & 7)) & 15) * 2 + 1);
  unsigned tau = TAU16;
  int wo_t = -1;
  __amdgpu_buffer_rsrc_t ro = rsrc(wo + (size_t)h * DH * DM, DH * DM * 2);
  int wo_vo = 0;
  auto issue = [&](v4u& r00, v4u& r01, v4u& r02, v4u& r10, v4u& r11, v4u& r12, int m) {
    TR(1);
    pace(tau + (wo_t >= 0 ? XDMA16 : 0));
    TR(2);
    if (wo_t >= 0) dma16(ro, wo_vo, wo_t * 6 * (DM * 2), (unsigned)(L_FP + w * 8192 + (wo_t & 7) * 1024));
    const __amdgpu_buffer_rsrc_t r = rsrc(m < NGW ? Kb : Vb, KVB);
    const int so = ((m * amul + rho) & (NGW - 1)) * (NWAVE * GRPB);
    r00 = ld16<KV_AUX>(r, kvo, so); r01 = ld16<KV_AUX>(r, kvo + 128, so); r02 = ld16<KV_AUX>(r, kvo + 256, so);
    r10 = ld16<KV_AUX>(r, kvo, so + TILEB); r11 = ld16<KV_AUX>(r, kvo + 128, so + TILEB); r12 = ld16<KV_AUX>(r, kvo + 256, so + TILEB);
    SCHED_FENCE();
    TR(3);
  };
#define ISSUE(G, m) issue(G.r[0][0], G.r[0][1], G.r[0][2], G.r[1][0], G.r[1][1], G.r[1][2], m)
#define ISSUE_W(o, m) issue(W[o], W[o + 1], W[o + 2], W[o + 3], W[o + 4], W[o + 5], m)
  Grp A, B;
  v4u W[32];
  ISSUE(A, 0);
  ISSUE(B, 1);
  tn += XHEAD16;
  STAMP(1);
  if (tid <= 320) *(LAS v4u*)(L + (tid < 320 ? L_XS + tid * 16 : L_MISC + 16)) = tiny;
  if (tid == 511) *(LAS unsigned*)(L + L_MISC + 12) = TAU16;
  __syncthreads();
  const unsigned tagA = __builtin_amdgcn_readfirstlane(*(LAS unsigned*)(L + L_MISC + 16)) + 1u, tagB = __builtin_amdgcn_readfirstlane(*(LAS unsigned*)(L + L_MISC + 20)) + 1u;
  v2u xq[4]; unsigned fx4[4];
  { const int qrc = qr < 20 ? qr : 0;
#pragma unroll
    for (int e = 0; e < 4; ++e) { xq[e] = *(LAS v2u*)(L + L_XS + e * (DM * 2) + (sh * 80 + qrc * 4) * 2); fx4[e] = *(LAS unsigned*)(L + L_XS + e * (DM * 2) + ((idx >> 1) * 40 + qrc * 2) * 2); } }
  {
    unsigned lo01[4], hi01[4], lo23[4], hi23[4];
#pragma unroll
    for (int d = 0; d < 4; ++d) {
      lo01[d] = __builtin_amdgcn_perm(wqv[1][d], wqv[0][d], 0x05040100u); hi01[d] = __builtin_amdgcn_perm(wqv[1][d], wqv[0][d], 0x07060302u);
      lo23[d] = __builtin_amdgcn_perm(wqv[3][d], wqv[2][d], 0x05040100u); hi23[d] = __builtin_amdgcn_perm(wqv[3][d], wqv[2][d], 0x07060302u);
    }
#pragma unroll
    for (int bb = 0; bb < 4; ++bb) {
      float a[8];
#pragma unroll
      for (int d = 0; d < 4; ++d) { a[2 * d] = dot2(lo23[d], xq[bb][1], dot2(lo01[d], xq[bb][0], 0.f)); a[2 * d + 1] = dot2(hi23[d], xq[bb][1], dot2(hi01[d], xq[bb][0], 0.f)); }
      LAS v4f* p = (LAS v4f*)(L + L_FP + ((qr * 4 + bb) * 192 + qc * 8) * 4);
      p[0] = v4f{a[0], a[1], a[2], a[3]}; p[1] = v4f{a[4], a[5], a[6], a[7]};
    }
  }
  {
    unsigned lo[4], hi[4];
#pragma unroll
    for (int d = 0; d < 4; ++d) { lo[d] = __builtin_amdgcn_perm(fwb[d], fwa[d], 0x05040100u); hi[d] = __builtin_amdgcn_perm(fwb[d], fwa[d], 0x07060302u); }
#pragma unroll
    for (int bb = 0; bb < 4; ++bb) {
      LAS v4f* p = (LAS v4f*)(L + L_FP2 + ((qr * 4 + bb) * 192 + qc * 8) * 4);
      p[0] = v4f{dot2(lo[0], fx4[bb], 0.f), dot2(hi[0], fx4[bb], 0.f), dot2(lo[1], fx4[bb], 0.f), dot2(hi[1], fx4[bb], 0.f)};
      p[1] = v4f{dot2(lo[2], fx4[bb], 0.f), dot2(hi[2], fx4[bb], 0.f), dot2(lo[3], fx4[bb], 0.f), dot2(hi[3], fx4[bb], 0.f)};
    }
  }
  TR(9);
  __syncthreads();
  TR(10);
  STAMP(2);
  if (tid < 384) {
    const int bb = tid / 96, p = tid - bb * 96;
    float a0 = 0.f, a1 = 0.f;
#pragma unroll
    for (int r = 0; r < 20; ++r) { const v2f v = *(LAS v2f*)(L + L_FP + ((r * 4 + bb) * 192 + p * 2) * 4); a0 += v[0]; a1 += v[1]; }
    __builtin_amdgcn_raw_buffer_store_b128(v4u{__float_as_uint(a0), tagA, __float_as_uint(a1), tagB}, rws, WS_QX + (((h * 4 + bb) * 8 + sh) * 128 + p) * 16, 0, AUX_SC1);
    a0 = 0.f; a1 = 0.f;
#pragma unroll
    for (int r = 0; r < 20; ++r) { const v2f v = *(LAS v2f*)(L + L_FP2 + ((r * 4 + bb) * 192 + p * 2) * 4); a0 += v[0]; a1 += v[1]; }
    __builtin_amdgcn_raw_buffer_store_b128(v4u{__float_as_uint(a0), tagA, __float_as_uint(a1), tagB}, rws,
                                           WS_FX + (((((xg * 4 + bb) * 8 + p / 12) * 2 + (idx & 1)) * 16 + (idx >> 1)) * 12 + p % 12) * 16, 0, AUX_SC1);
  }
  if (tid >= 384 && tid < 388)
    __builtin_amdgcn_raw_buffer_store_b128(v4u{T0, tagA, T0, tagB}, rws, WS_QX + (((h * 4 + (tid - 384)) * 8 + sh) * 128 + 96) * 16, 0, AUX_SC1);
  SCHED_FENCE();
  TR(11);
  ISSUE_W(0, 2);
  ISSUE_W(6, 3);
  ISSUE_W(12, 4);
  TR(12);
  if (w == 0) {
    __builtin_amdgcn_s_setprio(3);
    const int qo = WS_QX + (h * 4 + b) * 8 * 2048 + lane * 16;
    v4u ga[8], gb[8];
    for (unsigned spins = 0;;) {
#pragma unroll
      for (int p = 0; p < 8; ++p) { ga[p] = poll16(rws, qo + p * 2048); gb[p] = poll16(rws, qo + p * 2048 + 1024 + (lane <= 32 ? 0 : OOB)); }
      bool ok = true;
#pragma unroll
      for (int p = 0; p < 8; ++p) ok = ok & (ga[p][1] == tagA) & (ga[p][3] == tagB) & ((lane > 32) | ((gb[p][1] == tagA) & (gb[p][3] == tagB)));
      TR(17);
      if (__all(ok) || ++spins > SPIN_CAP) break;
      __builtin_amdgcn_s_sleep(1);
    }
    TR(16);
    float a0 = 0.f, a1 = 0.f, b0 = 0.f, b1 = 0.f;
#pragma unroll
    for (int p = 0; p < 8; ++p) { a0 += __uint_as_float(ga[p][0]); a1 += __uint_as_float(ga[p][2]); b0 += __uint_as_float(gb[p][0]); b1 += __uint_as_float(gb[p][2]); }
    *(LAS unsigned*)(L + L_QB + lane * 4) = cvtpk(a0, a1);
    if (lane < 32) *(LAS unsigned*)(L + L_QB + 256 + lane * 4) = cvtpk(b0, b1);
    unsigned tl = gb[0][0];
#pragma unroll
    for (int p = 1; p < 8; ++p) tl = (int)(gb[p][0] - tl) > 0 ? gb[p][0] : tl;
    if (lane == 32) *(LAS unsigned*)(L + L_MISC + 8) = tl;
  }
  TR(13);
  tn += XPOLL16;
  ISSUE_W(18, 5);
  ISSUE_W(24, 6);
  __syncthreads();
  TR(14);
  STAMP(3);
  { const unsigned tl = __builtin_amdgcn_readfirstlane(*(LAS unsigned*)(L + L_MISC + 8)); if (tl - T0 < 1000u) tn += (tl - T0) << 4; }
#define SLACK 3
  { const unsigned t = (now32() << 4) - SLACK * TAU16; if ((int)(t - tn) > 0) tn = t; }
  alpha_on = true;
  v4u Q[3];
#pragma unroll
  for (int i = 0; i < 3; ++i) Q[i] = *(LAS v4u*)(L + L_QB + (c + 8 * i) * 16);

  float mx = -INFINITY;
  const bool oct0 = c == 0;
  const int scoff = L_SC + w * 2048 + k * 8;
  auto k_tile = [&](const v4u& r0, const v4u& r1, const v4u& r2) -> float {
    float a0 = dot2(r0[0], Q[0][0], 0.f), a1 = dot2(r1[0], Q[1][0], 0.f), a2 = dot2(r2[0], Q[2][0], 0.f);
#pragma unroll
    for (int d = 1; d < 4; ++d) { a0 = dot2(r0[d], Q[0][d], a0); a1 = dot2(r1[d], Q[1][d], a1); a2 = dot2(r2[d], Q[2][d], a2); }
    return (a0 + a1) + a2;
  };
  auto k_put = [&](float s0, float s1, int n) {
    s0 = oct_sum(s0); s1 = oct_sum(s1);
    mx = fmaxf(mx, fmaxf(s0, s1));
    if (oct0) *(LAS v2f*)(L + scoff + n * 64) = v2f{s0, s1};
  };
#define K_STEP(G, n, m) do { float s0_ = k_tile(G.r[0][0], G.r[0][1], G.r[0][2]), s1_ = k_tile(G.r[1][0], G.r[1][1], G.r[1][2]); \
    asm volatile("" : "+v"(s0_), "+v"(s1_) : : "memory"); ISSUE(G, m); k_put(s0_, s1_, n); } while (0)
  K_STEP(A, 0, 7);
#pragma unroll
  for (int g = 0; g < 5; ++g) k_put(k_tile(W[g * 6], W[g * 6 + 1], W[g * 6 + 2]), k_tile(W[g * 6 + 3], W[g * 6 + 4], W[g * 6 + 5]), 2 + g);
  STAMP(4);
  K_STEP(B, 1, 8);
  STAMP(5);
  {
    const int lane = fresh_lane(), tid2 = w * 64 + lane;
    const int yr = lane / 10, yc = lane - yr * 10;
    const int fxo = WS_FX + ((xg * 4 + b) * 8 + (j & 7)) * 6144 + tid2 * 16 + (tid2 < 384 ? 0 : OOB);
    v4u fx = poll16(rws, fxo);
    const __amdgpu_buffer_rsrc_t r2 = rsrc(w2, FF * DM * 2), rg = rsrc(gw, w == 0 ? DM * 2 : 0);
    const int vo2 = (j * 24 + yr) * (DM * 2) + w * 160 + yc * 16 + (lane < 60 ? 0 : OOB);
#pragma unroll
    for (int t = 0; t < 4; ++t) W[8 + t] = ld16<W2_AUX>(r2, vo2, t * 6 * (DM * 2));
    const v4u gg0 = ld16<AUX_NT>(rg, lane * 16, 0), gg1 = ld16<AUX_NT>(rg, 1024 + lane * 16, 0);
    SCHED_FENCE();
    TR(30);
    tn += XFFN16;
    K_STEP(A, 7, 9); K_STEP(B, 8, 10);
    TR(31);
    for (unsigned spins = 0; !__all((tid2 >= 384) | ((fx[1] == tagA) & (fx[3] == tagB))); ) {
      if (++spins > SPIN_CAP) break;
      __builtin_amdgcn_s_sleep(2);
      fx = poll16(rws, fxo);
    }
    TR(32);
    volatile LAS unsigned* flag = (volatile LAS unsigned*)(L + L_MISC + 192);
    if (tid2 < 384) *(LAS v2f*)(L + L_G + ((tid2 / 12) * 24 + (tid2 % 12) * 2) * 4) = v2f{__uint_as_float(fx[0]), __uint_as_float(fx[2])};
    float gate = 0.f;
    if (w == 0) {
      const v4u gx0 = *(LAS v4u*)(L + L_XS + b * (DM * 2) + lane * 16), gx1 = *(LAS v4u*)(L + L_XS + b * (DM * 2) + 1024 + (lane & 15) * 16);
      float d = 0.f;
#pragma unroll
      for (int e = 0; e < 4; ++e) { d = dot2(gx0[e], gg0[e], d); d = dot2(gx1[e], gg1[e], d); }
      gate = 1.f / (1.f + __expf(-wave_sum(d)));
      if (lane == 0) *(LAS float*)(L + L_MISC) = gate;
    }
    LDS_FENCE();
    if (lane == 0) flag[w] = tagA;
    TR(33);
    K_STEP(A, 9, 11);
    if (w == 0) {
      for (unsigned spins = 0; !__all(lane >= 6 || flag[lane < 6 ? lane : 0] == tagA) && ++spins < (SPIN_CAP << 4); ) __builtin_amdgcn_s_sleep(1);
      MEM_FENCE();
      const LAS float* g = (const LAS float*)(L + L_G) + (lane < 24 ? lane : 0);
      float ga = 0.f, up = 0.f;
#pragma unroll
      for (int r = 0; r < 16; ++r) { ga += g[r * 24]; up += g[(16 + r) * 24]; }
      if (lane < 24) *(LAS float*)(L + L_MISC + 64 + lane * 4) = ga / (1.f + __expf(-ga)) * up * (1.f - gate);
      LDS_FENCE();
      if (lane == 0) flag[8] = tagA;
    }
    TR(34);
    K_STEP(B, 10, 12);
    for (unsigned spins = 0; flag[8] != tagA && ++spins < (SPIN_CAP << 4); ) __builtin_amdgcn_s_sleep(1);
    MEM_FENCE();
    v2f ya[4];
#pragma unroll
    for (int e = 0; e < 4; ++e) ya[e] = v2f{0.f, 0.f};
#pragma unroll
    for (int t = 0; t < 4; ++t) {
      const float hv = *(LAS float*)(L + L_MISC + 64 + (t * 6 + (yr < 6 ? yr : 0)) * 4);
#pragma unroll
      for (int d = 0; d < 4; ++d) ya[d] = __builtin_elementwise_fma(v2f{hv, hv}, v2f{bflo(W[8 + t][d]), bfhi(W[8 + t][d])}, ya[d]);
    }
    if (lane < 60) {
      LAS v4f* p = (LAS v4f*)(L + L_YF + (w * 480 + yr * 80 + yc * 8) * 4);
      p[0] = v4f{ya[0][0], ya[0][1], ya[1][0], ya[1][1]}; p[1] = v4f{ya[2][0], ya[2][1], ya[3][0], ya[3][1]};
    }
    wo_vo = yr * (DM * 2) + w * 160 + yc * 16 + (lane < 60 ? 0 : OOB);
  }
  MEM_FENCE();
  TR(35);
  STAMP(13);
#define WO_COPY(t0) do { asm volatile("s_waitcnt vmcnt(12)" ::: "memory"); const int ln_ = fresh_lane(); \
    _Pragma("unroll") for (int t = 0; t < 8; t += 2) {     \
      const v4u a_ = *(LAS v4u*)(L + L_FP + w * 8192 + t * 1024 + ln_ * 16), b_ = *(LAS v4u*)(L + L_FP + w * 8192 + (t + 1) * 1024 + ln_ * 16); \
      _Pragma("unroll") for (int d = 0; d < 4; ++d) { W[t0 + t][d] = __builtin_amdgcn_perm(b_[d], a_[d], 0x05040100u); W[t0 + t + 1][d] = __builtin_amdgcn_perm(b_[d], a_[d], 0x07060302u); } } \
    asm volatile("" : "+v"(W[t0]), "+v"(W[t0 + 1]), "+v"(W[t0 + 2]), "+v"(W[t0 + 3]), "+v"(W[t0 + 4]), "+v"(W[t0 + 5]), "+v"(W[t0 + 6]), "+v"(W[t0 + 7]) : : "memory"); } while (0)
#define CHK_BEGIN(pubc, rdc)
#define CHK_END()
  int n;
#pragma unroll 1
  for (n = 11; n < 21; n += 2) {
    CHK_BEGIN(n == 15 ? 0 : -1, n == 17 ? 0 : -1);
    wo_t = n < 19 ? n - 11 : -1; K_STEP(A, n, n + 2);
    wo_t = n < 19 ? n - 10 : -1; K_STEP(B, n + 1, n + 3);
    CHK_END();
  }
  WO_COPY(0);
#pragma unroll 1
  for (; n < 31; n += 2) {
    CHK_BEGIN(n == 23 ? 1 : -1, n == 25 ? 1 : -1);
    wo_t = n < 29 ? n - 13 : -1; K_STEP(A, n, n + 2);
    wo_t = n < 29 ? n - 12 : -1; K_STEP(B, n + 1, n + 3);
    CHK_END();
  }
  WO_COPY(8);
  wo_t = -1;
  K_STEP(A, 31, 33);

  STAMP(6);
  mx = fmaxf(mx, dpp<XOR8>(mx)); mx = max16_32(mx);
  const float mw = mx * CS;
  LDS_FENCE();
  {
    const int lane = fresh_lane();
    LAS v4f* p = (LAS v4f*)(L + L_SC + w * 2048 + lane * 32);
    v4f a = p[0], bb = p[1];
    float l = 0.f;
#pragma unroll
    for (int e = 0; e < 4; ++e) { a[e] = ex2(fmaf(a[e], CS, -mw)); bb[e] = ex2(fmaf(bb[e], CS, -mw)); l += a[e] + bb[e]; }
    p[0] = a; p[1] = bb;
    l = wave_sum(l);
    if (lane == 0) __builtin_amdgcn_raw_buffer_store_b128(v4u{__float_as_uint(mw), tagA, __float_as_uint(l), tagB}, rws, WS_ML + (((b * NH + h) * 2 + s) * 8 + w) * 16, 0, AUX_SC1);
  }
  LDS_FENCE();
  STAMP(11);

  v2f acc2[12];
#pragma unroll
  for (int e = 0; e < 12; ++e) acc2[e] = v2f{0.f, 0.f};
#define PIN_ACC() asm volatile("" : "+v"(acc2[0]), "+v"(acc2[1]), "+v"(acc2[2]), "+v"(acc2[3]), "+v"(acc2[4]), "+v"(acc2[5]), "+v"(acc2[6]), "+v"(acc2[7]), \
                                    "+v"(acc2[8]), "+v"(acc2[9]), "+v"(acc2[10]), "+v"(acc2[11]) : : "memory")
  auto v_tile = [&](const v4u& r0, const v4u& r1, const v4u& r2, float p) {
    const v2f pp = v2f{p, p};
#pragma unroll
    for (int d = 0; d < 4; ++d) {
      acc2[d] = __builtin_elementwise_fma(pp, v2f{bflo(r0[d]), bfhi(r0[d])}, acc2[d]);
      acc2[4 + d] = __builtin_elementwise_fma(pp, v2f{bflo(r1[d]), bfhi(r1[d])}, acc2[4 + d]);
      acc2[8 + d] = __builtin_elementwise_fma(pp, v2f{bflo(r2[d]), bfhi(r2[d])}, acc2[8 + d]);
    }
    PIN_ACC();
  };
#define V_USE(G, n) do { const v2f p2_ = *(LAS v2f*)(L + scoff + (n) * 64); v_tile(G.r[0][0], G.r[0][1], G.r[0][2], p2_[0]); v_tile(G.r[1][0], G.r[1][1], G.r[1][2], p2_[1]); } while (0)
#pragma unroll 1
  for (n = 0; n < 10; n += 2) {
    CHK_BEGIN((n & 7) == 0 ? 2 + (n >> 3) : -1, n == 2 ? 2 : -1);
    V_USE(B, n); wo_t = n < 8 ? n + 16 : -1; ISSUE(B, NGW + n + 2);
    V_USE(A, n + 1); wo_t = n < 8 ? n + 17 : -1; ISSUE(A, NGW + n + 3);
    CHK_END();
  }
  WO_COPY(16);
#pragma unroll 1
  for (; n < 20; n += 2) {
    CHK_BEGIN(n == 16 ? 4 : -1, n == 10 ? 3 : n == 18 ? 4 : -1);
    V_USE(B, n); wo_t = n < 18 ? n + 14 : -1; ISSUE(B, NGW + n + 2);
    V_USE(A, n + 1); wo_t = n < 18 ? n + 15 : -1; ISSUE(A, NGW + n + 3);
    CHK_END();
  }
  WO_COPY(24);
  wo_t = -1;
  const int mloff = WS_ML + ((b * NH + h) * 16 + (fresh_lane() & 15)) * 16;
  v4u ml = poll16(rws, mloff);
  V_USE(B, n); ISSUE(B, NGW + n + 2); V_USE(A, n + 1); ISSUE(A, NGW + n + 3);
  for (unsigned spins = 0; !__all((ml[1] == tagA) & (ml[3] == tagB)); ) {
    if (++spins > SPIN_CAP) break;
    __builtin_amdgcn_s_sleep(2);
    ml = poll16(rws, mloff);
  }
  float fw;
  {
    const float m_i = __uint_as_float(ml[0]), l_i = __uint_as_float(ml[2]);
    float M = m_i;
    M = fmaxf(M, dpp<XOR1>(M)); M = fmaxf(M, dpp<XOR2>(M)); M = fmaxf(M, dpp<XOR7>(M)); M = fmaxf(M, dpp<XOR8>(M));
    float Ls = l_i * ex2(m_i - M);
    Ls = oct_sum(Ls); Ls += dpp<XOR8>(Ls);
    fw = __uint_as_float(__builtin_amdgcn_readfirstlane(__float_as_uint(ex2(mw - M) / Ls * *(LAS float*)(L + L_MISC))));
  }
#pragma unroll 1
  for (n += 2; n < NGW - 2; n += 2) {
    V_USE(B, n); ISSUE(B, NGW + n + 2); V_USE(A, n + 1); ISSUE(A, NGW + n + 3);
  }
  V_USE(B, NGW - 2);
  V_USE(A, NGW - 1);

  STAMP(7);
  TR(40);
  {
    float b12[12], c6[6];
#pragma unroll
    for (int i = 0; i < 12; ++i) {
      auto r = __builtin_amdgcn_permlane32_swap(__float_as_uint(acc2[i >> 1][i & 1]), __float_as_uint(acc2[6 + (i >> 1)][i & 1]), false, false);
      b12[i] = __uint_as_float(r[0]) + __uint_as_float(r[1]);
    }
#pragma unroll
    for (int i = 0; i < 6; ++i) {
      auto r = __builtin_amdgcn_permlane16_swap(__float_as_uint(b12[i]), __float_as_uint(b12[i + 6]), false, false);
      c6[i] = __uint_as_float(r[0]) + __uint_as_float(r[1]);
    }
#pragma unroll
    for (int i = 0; i < 6; ++i) c6[i] = (c6[i] + dpp<XOR8>(c6[i])) * fw;
    const int ln = fresh_lane();
    if ((ln & 8) == 0) {
      const int v0 = (ln >> 4) * 6;
#pragma unroll
      for (int p = 0; p < 3; ++p) { const int v = v0 + 2 * p; *(LAS v2f*)(L + L_QP + (w * QPS + (v >> 3) * 64 + (ln & 7) * 8 + (v & 7)) * 4) = v2f{c6[2 * p], c6[2 * p + 1]}; }
    }
  }
  const int ln = fresh_lane();
  const int yr = ln < 60 ? ln / 10 : 0, yc = ln - (ln / 10) * 10;
  float ya[8];
  { const LAS v4f* p = (const LAS v4f*)(L + L_YF + (w * 480 + yr * 80 + yc * 8) * 4); const v4f a = p[0], bb = p[1];
#pragma unroll
    for (int e = 0; e < 4; ++e) { ya[e] = a[e]; ya[4 + e] = bb[e]; } }
  STAMP(8);
  TR(41);
  __syncthreads();
  TR(42);
  if (w < 6) {
    const int pi = w * 16 + (ln >> 2), r = pi >> 4, u = pi & 15, qq = ln & 3;
    const LAS float* p = (const LAS float*)(L + L_QP) + 2 * qq * QPS + u * 12 + r;
    float a0 = p[0] + p[QPS], a1 = p[6] + p[QPS + 6];
    a0 += dpp<XOR1>(a0); a1 += dpp<XOR1>(a1); a0 += dpp<XOR2>(a0); a1 += dpp<XOR2>(a1);
    if (qq == 0) *(LAS unsigned*)(L + L_OB + pi * 4) = cvtpk(a0, a1);
  }
  TR(43);
  __syncthreads();
  TR(44);
  {
    LAS float* yrp = (LAS float*)(L + L_FP) + w * 480;
    const LAS v4u* op = (const LAS v4u*)(L + L_OB + yr * 64);
#pragma unroll
    for (int u4 = 0; u4 < 4; ++u4) {
      const v4u o4 = op[u4];
#pragma unroll
      for (int e = 0; e < 4; ++e) {
        const int u = u4 * 4 + e;
#pragma unroll
        for (int d = 0; d < 4; ++d) { ya[2 * d] = dot2(W[2 * u][d], o4[e], ya[2 * d]); ya[2 * d + 1] = dot2(W[2 * u + 1][d], o4[e], ya[2 * d + 1]); }
      }
    }
    asm volatile("" : "+v"(ya[0]), "+v"(ya[1]), "+v"(ya[2]), "+v"(ya[3]), "+v"(ya[4]), "+v"(ya[5]), "+v"(ya[6]), "+v"(ya[7]));
    TR(45);
    if (ln < 60) { LAS v4f* p = (LAS v4f*)(yrp + yr * 80 + yc * 8); p[0] = v4f{ya[0], ya[1], ya[2], ya[3]}; p[1] = v4f{ya[4], ya[5], ya[6], ya[7]}; }
    LDS_FENCE();
    TR(46);
    if (ln < 40) {
      v2f y = v2f{0.f, 0.f};
#pragma unroll
      for (int r = 0; r < 6; ++r) y += *(LAS v2f*)(yrp + r * 80 + ln * 2);
      const int cons = w * 8 + ln / 5, g5 = ln % 5;
      __builtin_amdgcn_raw_buffer_store_b128(v4u{__float_as_uint(y[0]), tagA, __float_as_uint(y[1]), tagB}, rws, WS_INBOX + (((b * 64 + cons) * 64 + j) * 5 + g5) * 16, 0, AUX_SC1);
    }
  }
  STAMP(9);
  TR(47);
  if (w == 0) {
    const int io = WS_INBOX + ((b * 64 + j) * 64 + ln) * 80;
    v4u g5[5];
    for (unsigned spins = 0;;) {
      bool ok = true;
#pragma unroll
      for (int e = 0; e < 5; ++e) g5[e] = poll16(rws, io + e * 16);
#pragma unroll
      for (int e = 0; e < 5; ++e) ok = ok & (g5[e][1] == tagA) & (g5[e][3] == tagB);
      if (__all(ok) || ++spins > SPIN_CAP) break;
      __builtin_amdgcn_s_sleep(1);
    }
    TR(48);
    const unsigned yv[10] = {g5[0][0], g5[0][2], g5[1][0], g5[1][2], g5[4][0], g5[2][0], g5[2][2], g5[3][0], g5[3][2], g5[4][2]};
    float b5[5];
#pragma unroll
    for (int i = 0; i < 5; ++i) { auto r = __builtin_amdgcn_permlane32_swap(yv[i], yv[5 + i], false, false); b5[i] = __uint_as_float(r[0]) + __uint_as_float(r[1]); }
    float z[3];
    { auto r = __builtin_amdgcn_permlane16_swap(__float_as_uint(b5[0]), __float_as_uint(b5[2]), false, false); z[0] = __uint_as_float(r[0]) + __uint_as_float(r[1]); }
    { auto r = __builtin_amdgcn_permlane16_swap(__float_as_uint(b5[1]), __float_as_uint(b5[3]), false, false); z[1] = __uint_as_float(r[0]) + __uint_as_float(r[1]); }
    { auto r = __builtin_amdgcn_permlane16_swap(__float_as_uint(b5[4]), __float_as_uint(b5[4]), false, false); z[2] = __uint_as_float(r[0]) + __uint_as_float(r[1]); }
#pragma unroll
    for (int i = 0; i < 3; ++i) { z[i] += dpp<XOR1>(z[i]); z[i] += dpp<XOR2>(z[i]); z[i] += dpp<XOR7>(z[i]); z[i] += dpp<XOR8>(z[i]); }
    auto r89 = __builtin_amdgcn_permlane32_swap(__float_as_uint(z[2]), __float_as_uint(z[2]), false, false);
    const unsigned o03 = cvtpk(z[0], z[1]), o4 = cvtpk(__uint_as_float(r89[0]), __uint_as_float(r89[1]));
    if ((ln & 15) == 0 || ln == 1) *(GAS unsigned*)(out + b * DM + j * 10 + (ln == 1 ? 8 : (ln >> 4) * 2)) = ln == 1 ? o4 : o03;
    if (b == 0 && j == 0 && ln == 0) __hip_atomic_store((GAS u64*)(ws + WS_EPOCH), ((u64)tagB << 32) | tagA, __ATOMIC_RELAXED, __HIP_MEMORY_SCOPE_AGENT);
  }
  TR(49);
}
}

extern "C" void kernel_launch(void* const* d_in, const int* in_sizes, int n_in, void* d_out, int out_size, void* d_ws, size_t ws_size,
                              hipStream_t stream) {
  (void)in_sizes; (void)n_in; (void)out_size; (void)ws_size;
  hipLaunchKernelGGL(fused_decode, dim3(NWG), dim3(NTHR), 0, stream, (const u16*)d_in[0], (const u16*)d_in[1], (const u16*)d_in[2], (const u16*)d_in[3],
                     (const u16*)d_in[4], (const u16*)d_in[6], (const u16*)d_in[7], (const u16*)d_in[8], (const u16*)d_in[9], (u16*)d_out, (unsigned char*)d_ws);
}
```

```cpp
#define TAU16 (171 * 16)
#define XHEAD16 (TAU16 * 12 / 10)
#define XPOLL16 (TAU16 * 6 / 10)
#define XFFN16 (TAU16 * 6 / 10)
#include <hip/hip_runtime.h>
#include <hip/hip_bf16.h>
#include <stdint.h>

#define GAS __attribute__((address_space(1)))
#define LAS __attribute__((address_space(3)))
typedef unsigned v4u __attribute__((ext_vector_type(4)));
typedef unsigned v2u __attribute__((ext_vector_type(2)));
typedef float v4f __attribute__((ext_vector_type(4)));
typedef float v2f __attribute__((ext_vector_type(2)));
typedef __bf16 bf16x2 __attribute__((ext_vector_type(2)));
typedef unsigned long long u64;
typedef unsigned short u16;

namespace {
constexpr int NB = 4, NH = 32, SK = 8192, DH = 192, DM = 640, FF = 1536;
constexpr int NWG = 256, NWAVE = 8, NTHR = NWAVE * 64;
constexpr int KEYS_WG = SK / 2;
constexpr int ROWB = DH * 2;
constexpr int TILEB = 8 * ROWB;
constexpr int GRPB = 2 * TILEB;
constexpr int KVB = KEYS_WG * ROWB;
constexpr int NGW = KVB / GRPB / NWAVE;
constexpr int RSRC3 = 0x00020000;
#define POLL_AUX 16
#define STORE_AUX 16
constexpr int AUX_NT = 2, AUX_SC1 = STORE_AUX;
constexpr int OOB = 1 << 28;
constexpr float CS = 0.07216878364870322f * 1.4426950408889634f;
#define XDMA16 (TAU16 * 3 / 100)
#define ENDSYNC 0
#define ADAPT 0
#define TAUMIN16 (171 * 16)
#define TAUMAX16 (188 * 16)
#define TAUUP16 64
#define TAUDN16 32
#define MISS_HI 12
#define MISS_LO 4
#define ALPHA 0
#define PRIO 1
#define PHW 0

constexpr int WS_EPOCH = 0;
constexpr int WS_ML = 4096;
constexpr int WS_INBOX = 65536;
constexpr int WS_QX = WS_INBOX + NB * 64 * 64 * 80;
constexpr int WS_FX = WS_QX + NH * NB * 8 * 128 * 16;
constexpr int WS_FL = WS_FX + 8 * NB * 8 * 384 * 16;
constexpr int WS_BYTES = WS_FL + 8 * 1024;

constexpr int L_XS = 0;
constexpr int L_QP = 5120;
constexpr int QPS = 196;
constexpr int L_QB = L_QP + 8 * QPS * 4;
constexpr int L_OB = L_QB + 384;
constexpr int L_MISC = L_OB + 384;
constexpr int L_G = L_MISC + 256;
constexpr int L_YF = L_G + 3072;
constexpr int L_SC = L_YF + 8 * 480 * 4;
constexpr int L_FP2 = L_YF;
constexpr int L_FP = L_YF + 22 * 4 * 192 * 4;
constexpr int L_TR = L_FP + 22 * 4 * 192 * 4;
constexpr int L_BYTES = L_TR;

constexpr unsigned SPIN_CAP = 1u << 17;

__device__ __forceinline__ float dot2(unsigned a, unsigned b, float c) {
  return __builtin_amdgcn_fdot2_f32_bf16(__builtin_bit_cast(bf16x2, a), __builtin_bit_cast(bf16x2, b), c, false);
}
template <int CTRL> __device__ __forceinline__ float dpp(float x) {
  return __builtin_bit_cast(float, __builtin_amdgcn_mov_dpp(__builtin_bit_cast(int, x), CTRL, 0xf, 0xf, true));
}
constexpr int XOR1 = 0xB1, XOR2 = 0x4E, XOR7 = 0x141, XOR8 = 0x128;
__device__ __forceinline__ float sum16_32(float x) {
  auto s = __builtin_amdgcn_permlane16_swap(__float_as_uint(x), __float_as_uint(x), false, false);
  x = __uint_as_float(s[0]) + __uint_as_float(s[1]);
  auto t = __builtin_amdgcn_permlane32_swap(__float_as_uint(x), __float_as_uint(x), false, false);
  return __uint_as_float(t[0]) + __uint_as_float(t[1]);
}
__device__ __forceinline__ float max16_32(float x) {
  auto s = __builtin_amdgcn_permlane16_swap(__float_as_uint(x), __float_as_uint(x), false, false);
  x = fmaxf(__uint_as_float(s[0]), __uint_as_float(s[1]));
  auto t = __builtin_amdgcn_permlane32_swap(__float_as_uint(x), __float_as_uint(x), false, false);
  return fmaxf(__uint_as_float(t[0]), __uint_as_float(t[1]));
}
__device__ __forceinline__ float oct_sum(float x) { x += dpp<XOR1>(x); x += dpp<XOR2>(x); x += dpp<XOR7>(x); return x; }
__device__ __forceinline__ float xoct_sum(float x) { x += dpp<XOR8>(x); return sum16_32(x); }
__device__ __forceinline__ float wave_sum(float x) { return xoct_sum(oct_sum(x)); }
__device__ __forceinline__ unsigned cvtpk(float lo, float hi) { unsigned r; asm("v_cvt_pk_bf16_f32 %0, %1, %2" : "=v"(r) : "v"(lo), "v"(hi)); return r; }
__device__ __forceinline__ float bflo(unsigned u) { return __uint_as_float(u << 16); }
__device__ __forceinline__ float bfhi(unsigned u) { return __uint_as_float(u & 0xffff0000u); }
__device__ __forceinline__ float ex2(float x) { return __builtin_amdgcn_exp2f(x); }
__device__ __forceinline__ __amdgpu_buffer_rsrc_t rsrc(const void* p, int bytes) { return __builtin_amdgcn_make_buffer_rsrc((void*)p, (short)0, bytes, RSRC3); }
template <int AUX> __device__ __forceinline__ v4u ld16(__amdgpu_buffer_rsrc_t r, int voff, int soff) { return __builtin_amdgcn_raw_buffer_load_b128(r, voff, soff, AUX); }
template <int AUX> __device__ __forceinline__ v2u ld8(__amdgpu_buffer_rsrc_t r, int voff, int soff) { return __builtin_amdgcn_raw_buffer_load_b64(r, voff, soff, AUX); }
__device__ __forceinline__ unsigned now32() { return (unsigned)__builtin_amdgcn_s_memrealtime(); }
#define STAMP(i) do { } while (0)
#define TR(id) do { } while (0)
#define TRV(id, val) do { } while (0)
#define SCHED_FENCE() __builtin_amdgcn_sched_barrier(0)
#define LDS_FENCE() asm volatile("s_waitcnt lgkmcnt(0)" ::: "memory")
#define MEM_FENCE() asm volatile("" ::: "memory")
__device__ __forceinline__ v4u poll16(__amdgpu_buffer_rsrc_t r, int voff) { asm volatile("" : "+v"(voff) : : "memory"); return __builtin_amdgcn_raw_buffer_load_b128(r, voff, 0, POLL_AUX); }
__device__ __forceinline__ int fresh_lane() { int l; asm volatile("v_mbcnt_lo_u32_b32 %0, -1, 0\n\tv_mbcnt_hi_u32_b32 %0, -1, %0" : "=v"(l)); return l; }

#define WO_POLICY ""
__device__ __forceinline__ void dma16(__amdgpu_buffer_rsrc_t r, int voff, int soff, unsigned lds) {
  unsigned keep;
  asm volatile("s_mov_b32 %0, m0\n\ts_mov_b32 m0, %3\n\ts_nop 0\n\tbuffer_load_dwordx4 %1, %2, %4 offen " WO_POLICY " lds\n\ts_mov_b32 m0, %0"
               : "=&s"(keep) : "v"(voff), "s"(r), "s"(lds), "s"(soff) : "memory");
}
#define KV_AUX AUX_NT
#define WQ_AUX AUX_NT
#define W2_AUX 0
struct Grp { v4u r[2][3]; };

__global__ void __launch_bounds__(NTHR, 2)
fused_decode(const u16* __restrict__ x, const u16* __restrict__ Kc, const u16* __restrict__ Vc, const u16* __restrict__ wq,
             const u16* __restrict__ wo, const u16* __restrict__ w1, const u16* __restrict__ w3, const u16* __restrict__ w2,
             const u16* __restrict__ gw, u16* __restrict__ out, unsigned char* ws) {
  __shared__ __attribute__((aligned(16))) unsigned char lds_raw[L_BYTES];
  LAS unsigned char* L = (LAS unsigned char*)lds_raw;
  const int tid = threadIdx.x, lane = tid & 63, w = __builtin_amdgcn_readfirstlane(tid >> 6);
  const int k = lane >> 3, c = lane & 7;
  const int blk = blockIdx.x, xg = blk & 7, idx = blk >> 3;
  const int h = xg * 4 + (idx & 3), sh = idx >> 2, b = sh >> 1, s = sh & 1;
  const int j = h * 2 + s;
  STAMP(0);
#define PHASE 0
#define T0OFF 50
  unsigned T0 = now32() + T0OFF + idx * PHASE * (TAU16 / 32) / (16 * 32);
  const __amdgpu_buffer_rsrc_t rws = rsrc(ws, WS_BYTES);
  const v4u tiny = __builtin_nontemporal_load((const GAS v4u*)(tid == 320 ? (const unsigned char*)ws + WS_EPOCH : (const unsigned char*)x + (tid < 320 ? tid : 0) * 16));
  const int qr = tid / 24, qc = tid - qr * 24;
  v4u wqv[4], fwa, fwb;
  {
    const __amdgpu_buffer_rsrc_t rq = rsrc(wq + h * DH, (DM * NH * DH - h * DH) * 2);
    const int oob = tid < 480 ? 0 : OOB;
#pragma unroll
    for (int e = 0; e < 4; ++e) wqv[e] = ld16<WQ_AUX>(rq, (sh * 80 + qr * 4) * (NH * DH * 2) + qc * 16 + oob, e * (NH * DH * 2));
    const int row0 = (idx >> 1) * 40 + qr * 2;
    const __amdgpu_buffer_rsrc_t r13 = rsrc(((idx & 1) ? w3 : w1) + xg * 192, (DM * FF - xg * 192) * 2);
    fwa = ld16<WQ_AUX>(r13, row0 * (FF * 2) + qc * 16 + oob, 0); fwb = ld16<WQ_AUX>(r13, row0 * (FF * 2) + qc * 16 + oob, FF * 2);
  }
  const size_t kvoff = ((size_t)(b * NH + h) * SK + (size_t)s * KEYS_WG) * DH;
  const u16* Kb = Kc + kvoff; const u16* Vb = Vc + kvoff;
#define SIGMA 1
  const int sigma = SIGMA == 0 ? 0 : SIGMA == 1 ? (int)((blockIdx.x * 2246822519u) >> 29) : (int)((blockIdx.x >> 3) & 7);
  const int kvo = ((w + sigma) & 7) * GRPB + k * ROWB + c * 16;
#define WPH 1
#define GPH 0
  bool alpha_on = false;
  unsigned tn = (T0 << 4) + (WPH == 1 ? w * (TAU16 / 8) : WPH == 2 ? (w >> 2) * (TAU16 / 2) : 0) + (idx & 3) * (TAU16 * GPH / 32);
  unsigned tmin = tn;
  auto pace = [&](unsigned step16) {
    const unsigned tt = (ALPHA && (int)(tmin - tn) > 0) ? tmin : tn;
    unsigned now = now32() << 4;
    const int late = (int)(now - tn);
    if (late > 2 * TAU16) __builtin_amdgcn_s_setprio(3);
    else if (late > TAU16) __builtin_amdgcn_s_setprio(2);
    else if (late > TAU16 / 4) __builtin_amdgcn_s_setprio(1);
    else __builtin_amdgcn_s_setprio(0);
    while ((int)(now - tt) < 0) { __builtin_amdgcn_s_sleep(1); now = now32() << 4; }
    if (ALPHA) tmin = now + (alpha_on ? TAU16 * ALPHA / 100 : 0);
    tn += step16;
  };
#define ROT 2
  const int rho = ROT == 0 ? 0 : ROT == 1 ? ((blk * 11) & 31) : ROT == 2 ? (((blk >> 3) + (blk & 7) * 4) & 31) : ROT == 3 ? ((int)((blk * 2654435761u) >> 27)) : (((blk >> 3) * 5 + (blk & 7) * 3) & 31);
#define AMUL 0
  const int amul = AMUL == 0 ? 1 : AMUL == 1 ? (int)(((blk * 0x9E3779B1u) >> 28) * 2 + 1) : (int)((((blk >> 3) + 3 * (blk & 7)) & 15) * 2 + 1);
  unsigned tau = TAU16;
  int wo_t = -1;
  __amdgpu_buffer_rsrc_t ro = rsrc(wo + (size_t)h * DH * DM, DH * DM * 2);
  int wo_vo = 0;
  auto issue = [&](v4u& r00, v4u& r01, v4u& r02, v4u& r10, v4u& r11, v4u& r12, int m) {
    TR(1);
    pace(tau + (wo_t >= 0 ? XDMA16 : 0));
    TR(2);
    if (wo_t >= 0) dma16(ro, wo_vo, wo_t * 6 * (DM * 2), (unsigned)(L_FP + w * 8192 + (wo_t & 7) * 1024));
    const __amdgpu_buffer_rsrc_t r = rsrc(m < NGW ? Kb : Vb, KVB);
    const int so = ((m * amul + rho) & (NGW - 1)) * (NWAVE * GRPB);
    r00 = ld16<KV_AUX>(r, kvo, so); r01 = ld16<KV_AUX>(r, kvo + 128, so); r02 = ld16<KV_AUX>(r, kvo + 256, so);
    r10 = ld16<KV_AUX>(r, kvo, so + TILEB); r11 = ld16<KV_AUX>(r, kvo + 128, so + TILEB); r12 = ld16<KV_AUX>(r, kvo + 256, so + TILEB);
    SCHED_FENCE();
    TR(3);
  };
#define ISSUE(G, m) issue(G.r[0][0], G.r[0][1], G.r[0][2], G.r[1][0], G.r[1][1], G.r[1][2], m)
#define ISSUE_W(o, m) issue(W[o], W[o + 1], W[o + 2], W[o + 3], W[o + 4], W[o + 5], m)
  Grp A, B;
  v4u W[32];
  ISSUE(A, 0);
  ISSUE(B, 1);
  tn += XHEAD16;
  STAMP(1);
  if (tid <= 320) *(LAS v4u*)(L + (tid < 320 ? L_XS + tid * 16 : L_MISC + 16)) = tiny;
  if (tid == 511) *(LAS unsigned*)(L + L_MISC + 12) = TAU16;
  __syncthreads();
  const unsigned tagA = __builtin_amdgcn_readfirstlane(*(LAS unsigned*)(L + L_MISC + 16)) + 1u, tagB = __builtin_amdgcn_readfirstlane(*(LAS unsigned*)(L + L_MISC + 20)) + 1u;
  v2u xq[4]; unsigned fx4[4];
  { const int qrc = qr < 20 ? qr : 0;
#pragma unroll
    for (int e = 0; e < 4; ++e) { xq[e] = *(LAS v2u*)(L + L_XS + e * (DM * 2) + (sh * 80 + qrc * 4) * 2); fx4[e] = *(LAS unsigned*)(L + L_XS + e * (DM * 2) + ((idx >> 1) * 40 + qrc * 2) * 2); } }
  {
    unsigned lo01[4], hi01[4], lo23[4], hi23[4];
#pragma unroll
    for (int d = 0; d < 4; ++d) {
      lo01[d] = __builtin_amdgcn_perm(wqv[1][d], wqv[0][d], 0x05040100u); hi01[d] = __builtin_amdgcn_perm(wqv[1][d], wqv[0][d], 0x07060302u);
      lo23[d] = __builtin_amdgcn_perm(wqv[3][d], wqv[2][d], 0x05040100u); hi23[d] = __builtin_amdgcn_perm(wqv[3][d], wqv[2][d], 0x07060302u);
    }
#pragma unroll
    for (int bb = 0; bb < 4; ++bb) {
      float a[8];
#pragma unroll
      for (int d = 0; d < 4; ++d) { a[2 * d] = dot2(lo23[d], xq[bb][1], dot2(lo01[d], xq[bb][0], 0.f)); a[2 * d + 1] = dot2(hi23[d], xq[bb][1], dot2(hi01[d], xq[bb][0], 0.f)); }
      LAS v4f* p = (LAS v4f*)(L + L_FP + ((qr * 4 + bb) * 192 + qc * 8) * 4);
      p[0] = v4f{a[0], a[1], a[2], a[3]}; p[1] = v4f{a[4], a[5], a[6], a[7]};
    }
  }
  {
    unsigned lo[4], hi[4];
#pragma unroll
    for (int d = 0; d < 4; ++d) { lo[d] = __builtin_amdgcn_perm(fwb[d], fwa[d], 0x05040100u); hi[d] = __builtin_amdgcn_perm(fwb[d], fwa[d], 0x07060302u); }
#pragma unroll
    for (int bb = 0; bb < 4; ++bb) {
      LAS v4f* p = (LAS v4f*)(L + L_FP2 + ((qr * 4 + bb) * 192 + qc * 8) * 4);
      p[0] = v4f{dot2(lo[0], fx4[bb], 0.f), dot2(hi[0], fx4[bb], 0.f), dot2(lo[1], fx4[bb], 0.f), dot2(hi[1], fx4[bb], 0.f)};
      p[1] = v4f{dot2(lo[2], fx4[bb], 0.f), dot2(hi[2], fx4[bb], 0.f), dot2(lo[3], fx4[bb], 0.f), dot2(hi[3], fx4[bb], 0.f)};
    }
  }
  TR(9);
  __syncthreads();
  TR(10);
  STAMP(2);
  if (tid < 384) {
    const int bb = tid / 96, p = tid - bb * 96;
    float a0 = 0.f, a1 = 0.f;
#pragma unroll
    for (int r = 0; r < 20; ++r) { const v2f v = *(LAS v2f*)(L + L_FP + ((r * 4 + bb) * 192 + p * 2) * 4); a0 += v[0]; a1 += v[1]; }
    __builtin_amdgcn_raw_buffer_store_b128(v4u{__float_as_uint(a0), tagA, __float_as_uint(a1), tagB}, rws, WS_QX + (((h * 4 + bb) * 8 + sh) * 128 + p) * 16, 0, AUX_SC1);
    a0 = 0.f; a1 = 0.f;
#pragma unroll
    for (int r = 0; r < 20; ++r) { const v2f v = *(LAS v2f*)(L + L_FP2 + ((r * 4 + bb) * 192 + p * 2) * 4); a0 += v[0]; a1 += v[1]; }
    __builtin_amdgcn_raw_buffer_store_b128(v4u{__float_as_uint(a0), tagA, __float_as_uint(a1), tagB}, rws,
                                           WS_FX + (((((xg * 4 + bb) * 8 + p / 12) * 2 + (idx & 1)) * 16 + (idx >> 1)) * 12 + p % 12) * 16, 0, AUX_SC1);
  }
  if (tid >= 384 && tid < 388)
    __builtin_amdgcn_raw_buffer_store_b128(v4u{T0, tagA, T0, tagB}, rws, WS_QX + (((h * 4 + (tid - 384)) * 8 + sh) * 128 + 96) * 16, 0, AUX_SC1);
  SCHED_FENCE();
  TR(11);
  ISSUE_W(0, 2);
  ISSUE_W(6, 3);
  ISSUE_W(12, 4);
  TR(12);
  if (w == 0) {
    __builtin_amdgcn_s_setprio(3);
    const int qo = WS_QX + (h * 4 + b) * 8 * 2048 + lane * 16;
    v4u ga[8], gb[8];
    for (unsigned spins = 0;;) {
#pragma unroll
      for (int p = 0; p < 8; ++p) { ga[p] = poll16(rws, qo + p * 2048); gb[p] = poll16(rws, qo + p * 2048 + 1024 + (lane <= 32 ? 0 : OOB)); }
      bool ok = true;
#pragma unroll
      for (int p = 0; p < 8; ++p) ok = ok & (ga[p][1] == tagA) & (ga[p][3] == tagB) & ((lane > 32) | ((gb[p][1] == tagA) & (gb[p][3] == tagB)));
      TR(17);
      if (__all(ok) || ++spins > SPIN_CAP) break;
      __builtin_amdgcn_s_sleep(1);
    }
    TR(16);
    float a0 = 0.f, a1 = 0.f, b0 = 0.f, b1 = 0.f;
#pragma unroll
    for (int p = 0; p < 8; ++p) { a0 += __uint_as_float(ga[p][0]); a1 += __uint_as_float(ga[p][2]); b0 += __uint_as_float(gb[p][0]); b1 += __uint_as_float(gb[p][2]); }
    *(LAS unsigned*)(L + L_QB + lane * 4) = cvtpk(a0, a1);
    if (lane < 32) *(LAS unsigned*)(L + L_QB + 256 + lane * 4) = cvtpk(b0, b1);
    unsigned tl = gb[0][0];
#pragma unroll
    for (int p = 1; p < 8; ++p) tl = (int)(gb[p][0] - tl) > 0 ? gb[p][0] : tl;
    if (lane == 32) *(LAS unsigned*)(L + L_MISC + 8) = tl;
  }
  TR(13);
  tn += XPOLL16;
  ISSUE_W(18, 5);
  ISSUE_W(24, 6);
  __syncthreads();
  TR(14);
  STAMP(3);
  { const unsigned tl = __builtin_amdgcn_readfirstlane(*(LAS unsigned*)(L + L_MISC + 8)); if (tl - T0 < 1000u) tn += (tl - T0) << 4; }
#define SLACK 3
  { const unsigned t = (now32() << 4) - SLACK * TAU16; if ((int)(t - tn) > 0) tn = t; }
  alpha_on = true;
  v4u Q[3];
#pragma unroll
  for (int i = 0; i < 3; ++i) Q[i] = *(LAS v4u*)(L + L_QB + (c + 8 * i) * 16);

  float mx = -INFINITY;
  const bool oct0 = c == 0;
  const int scoff = L_SC + w * 2048 + k * 8;
  auto k_tile = [&](const v4u& r0, const v4u& r1, const v4u& r2) -> float {
    float a0 = dot2(r0[0], Q[0][0], 0.f), a1 = dot2(r1[0], Q[1][0], 0.f), a2 = dot2(r2[0], Q[2][0], 0.f);
#pragma unroll
    for (int d = 1; d < 4; ++d) { a0 = dot2(r0[d], Q[0][d], a0); a1 = dot2(r1[d], Q[1][d], a1); a2 = dot2(r2[d], Q[2][d], a2); }
    return (a0 + a1) + a2;
  };
  auto k_put = [&](float s0, float s1, int n) {
    s0 = oct_sum(s0); s1 = oct_sum(s1);
    mx = fmaxf(mx, fmaxf(s0, s1));
    if (oct0) *(LAS v2f*)(L + scoff + n * 64) = v2f{s0, s1};
  };
#define K_STEP(G, n, m) do { float s0_ = k_tile(G.r[0][0], G.r[0][1], G.r[0][2]), s1_ = k_tile(G.r[1][0], G.r[1][1], G.r[1][2]); \
    asm volatile("" : "+v"(s0_), "+v"(s1_) : : "memory"); ISSUE(G, m); k_put(s0_, s1_, n); } while (0)
  K_STEP(A, 0, 7);
#pragma unroll
  for (int g = 0; g < 5; ++g) k_put(k_tile(W[g * 6], W[g * 6 + 1], W[g * 6 + 2]), k_tile(W[g * 6 + 3], W[g * 6 + 4], W[g * 6 + 5]), 2 + g);
  STAMP(4);
  K_STEP(B, 1, 8);
  STAMP(5);
  {
    const int lane = fresh_lane(), tid2 = w * 64 + lane;
    const int yr = lane / 10, yc = lane - yr * 10;
    const int fxo = WS_FX + ((xg * 4 + b) * 8 + (j & 7)) * 6144 + tid2 * 16 + (tid2 < 384 ? 0 : OOB);
    v4u fx = poll16(rws, fxo);
    const __amdgpu_buffer_rsrc_t r2 = rsrc(w2, FF * DM * 2), rg = rsrc(gw, w == 0 ? DM * 2 : 0);
    const int vo2 = (j * 24 + yr) * (DM * 2) + w * 160 + yc * 16 + (lane < 60 ? 0 : OOB);
#pragma unroll
    for (int t = 0; t < 4; ++t) W[8 + t] = ld16<W2_AUX>(r2, vo2, t * 6 * (DM * 2));
    const v4u gg0 = ld16<AUX_NT>(rg, lane * 16, 0), gg1 = ld16<AUX_NT>(rg, 1024 + lane * 16, 0);
    SCHED_FENCE();
    TR(30);
    tn += XFFN16;
    K_STEP(A, 7, 9); K_STEP(B, 8, 10);
    TR(31);
    for (unsigned spins = 0; !__all((tid2 >= 384) | ((fx[1] == tagA) & (fx[3] == tagB))); ) {
      if (++spins > SPIN_CAP) break;
      __builtin_amdgcn_s_sleep(2);
      fx = poll16(rws, fxo);
    }
    TR(32);
    volatile LAS unsigned* flag = (volatile LAS unsigned*)(L + L_MISC + 192);
    if (tid2 < 384) *(LAS v2f*)(L + L_G + ((tid2 / 12) * 24 + (tid2 % 12) * 2) * 4) = v2f{__uint_as_float(fx[0]), __uint_as_float(fx[2])};
    float gate = 0.f;
    if (w == 0) {
      const v4u gx0 = *(LAS v4u*)(L + L_XS + b * (DM * 2) + lane * 16), gx1 = *(LAS v4u*)(L + L_XS + b * (DM * 2) + 1024 + (lane & 15) * 16);
      float d = 0.f;
#pragma unroll
      for (int e = 0; e < 4; ++e) { d = dot2(gx0[e], gg0[e], d); d = dot2(gx1[e], gg1[e], d); }
      gate = 1.f / (1.f + __expf(-wave_sum(d)));
      if (lane == 0) *(LAS float*)(L + L_MISC) = gate;
    }
    LDS_FENCE();
    if (lane == 0) flag[w] = tagA;
    TR(33);
    K_STEP(A, 9, 11);
    if (w == 0) {
      for (unsigned spins = 0; !__all(lane >= 6 || flag[lane < 6 ? lane : 0] == tagA) && ++spins < (SPIN_CAP << 4); ) __builtin_amdgcn_s_sleep(1);
      MEM_FENCE();
      const LAS float* g = (const LAS float*)(L + L_G) + (lane < 24 ? lane : 0);
      float ga = 0.f, up = 0.f;
#pragma unroll
      for (int r = 0; r < 16; ++r) { ga += g[r * 24]; up += g[(16 + r) * 24]; }
      if (lane < 24) *(LAS float*)(L + L_MISC + 64 + lane * 4) = ga / (1.f + __expf(-ga)) * up * (1.f - gate);
      LDS_FENCE();
      if (lane == 0) flag[8] = tagA;
    }
    TR(34);
    K_STEP(B, 10, 12);
    for (unsigned spins = 0; flag[8] != tagA && ++spins < (SPIN_CAP << 4); ) __builtin_amdgcn_s_sleep(1);
    MEM_FENCE();
    v2f ya[4];
#pragma unroll
    for (int e = 0; e < 4; ++e) ya[e] = v2f{0.f, 0.f};
#pragma unroll
    for (int t = 0; t < 4; ++t) {
      const float hv = *(LAS float*)(L + L_MISC + 64 + (t * 6 + (yr < 6 ? yr : 0)) * 4);
#pragma unroll
      for (int d = 0; d < 4; ++d) ya[d] = __builtin_elementwise_fma(v2f{hv, hv}, v2f{bflo(W[8 + t][d]), bfhi(W[8 + t][d])}, ya[d]);
    }
    if (lane < 60) {
      LAS v4f* p = (LAS v4f*)(L + L_YF + (w * 480 + yr * 80 + yc * 8) * 4);
      p[0] = v4f{ya[0][0], ya[0][1], ya[1][0], ya[1][1]}; p[1] = v4f{ya[2][0], ya[2][1], ya[3][0], ya[3][1]};
    }
    wo_vo = yr * (DM * 2) + w * 160 + yc * 16 + (lane < 60 ? 0 : OOB);
  }
  MEM_FENCE();
  TR(35);
  STAMP(13);
#define WO_COPY(t0) do { asm volatile("s_waitcnt vmcnt(12)" ::: "memory"); const int ln_ = fresh_lane(); \
    _Pragma("unroll") for (int t = 0; t < 8; t += 2) {     \
      const v4u a_ = *(LAS v4u*)(L + L_FP + w * 8192 + t * 1024 + ln_ * 16), b_ = *(LAS v4u*)(L + L_FP + w * 8192 + (t + 1) * 1024 + ln_ * 16); \
      _Pragma("unroll") for (int d = 0; d < 4; ++d) { W[t0 + t][d] = __builtin_amdgcn_perm(b_[d], a_[d], 0x05040100u); W[t0 + t + 1][d] = __builtin_amdgcn_perm(b_[d], a_[d], 0x07060302u); } } \
    asm volatile("" : "+v"(W[t0]), "+v"(W[t0 + 1]), "+v"(W[t0 + 2]), "+v"(W[t0 + 3]), "+v"(W[t0 + 4]), "+v"(W[t0 + 5]), "+v"(W[t0 + 6]), "+v"(W[t0 + 7]) : : "memory"); } while (0)
#define CHK_BEGIN(pubc, rdc)
#define CHK_END()
  int n;
#pragma unroll 1
  for (n = 11; n < 21; n += 2) {
    CHK_BEGIN(n == 15 ? 0 : -1, n == 17 ? 0 : -1);
    wo_t = n < 19 ? n - 11 : -1; K_STEP(A, n, n + 2);
    wo_t = n < 19 ? n - 10 : -1; K_STEP(B, n + 1, n + 3);
    CHK_END();
  }
  WO_COPY(0);
#pragma unroll 1
  for (; n < 31; n += 2) {
    CHK_BEGIN(n == 23 ? 1 : -1, n == 25 ? 1 : -1);
    wo_t = n < 29 ? n - 13 : -1; K_STEP(A, n, n + 2);
    wo_t = n < 29 ? n - 12 : -1; K_STEP(B, n + 1, n + 3);
    CHK_END();
  }
  WO_COPY(8);
  wo_t = -1;
  K_STEP(A, 31, 33);

  STAMP(6);
  mx = fmaxf(mx, dpp<XOR8>(mx)); mx = max16_32(mx);
  const float mw = mx * CS;
  LDS_FENCE();
  {
    const int lane = fresh_lane();
    LAS v4f* p = (LAS v4f*)(L + L_SC + w * 2048 + lane * 32);
    v4f a = p[0], bb = p[1];
    float l = 0.f;
#pragma unroll
    for (int e = 0; e < 4; ++e) { a[e] = ex2(fmaf(a[e], CS, -mw)); bb[e] = ex2(fmaf(bb[e], CS, -mw)); l += a[e] + bb[e]; }
    p[0] = a; p[1] = bb;
    l = wave_sum(l);
    if (lane == 0) __builtin_amdgcn_raw_buffer_store_b128(v4u{__float_as_uint(mw), tagA, __float_as_uint(l), tagB}, rws, WS_ML + (((b * NH + h) * 2 + s) * 8 + w) * 16, 0, AUX_SC1);
  }
  LDS_FENCE();
  STAMP(11);

  v2f acc2[12];
#pragma unroll
  for (int e = 0; e < 12; ++e) acc2[e] = v2f{0.f, 0.f};
#define PIN_ACC() asm volatile("" : "+v"(acc2[0]), "+v"(acc2[1]), "+v"(acc2[2]), "+v"(acc2[3]), "+v"(acc2[4]), "+v"(acc2[5]), "+v"(acc2[6]), "+v"(acc2[7]), \
                                    "+v"(acc2[8]), "+v"(acc2[9]), "+v"(acc2[10]), "+v"(acc2[11]) : : "memory")
  auto v_tile = [&](const v4u& r0, const v4u& r1, const v4u& r2, float p) {
    const v2f pp = v2f{p, p};
#pragma unroll
    for (int d = 0; d < 4; ++d) {
      acc2[d] = __builtin_elementwise_fma(pp, v2f{bflo(r0[d]), bfhi(r0[d])}, acc2[d]);
      acc2[4 + d] = __builtin_elementwise_fma(pp, v2f{bflo(r1[d]), bfhi(r1[d])}, acc2[4 + d]);
      acc2[8 + d] = __builtin_elementwise_fma(pp, v2f{bflo(r2[d]), bfhi(r2[d])}, acc2[8 + d]);
    }
    PIN_ACC();
  };
#define V_USE(G, n) do { const v2f p2_ = *(LAS v2f*)(L + scoff + (n) * 64); v_tile(G.r[0][0], G.r[0][1], G.r[0][2], p2_[0]); v_tile(G.r[1][0], G.r[1][1], G.r[1][2], p2_[1]); } while (0)
#pragma unroll 1
  for (n = 0; n < 10; n += 2) {
    CHK_BEGIN((n & 7) == 0 ? 2 + (n >> 3) : -1, n == 2 ? 2 : -1);
    V_USE(B, n); wo_t = n < 8 ? n + 16 : -1; ISSUE(B, NGW + n + 2);
    V_USE(A, n + 1); wo_t = n < 8 ? n + 17 : -1; ISSUE(A, NGW + n + 3);
    CHK_END();
  }
  WO_COPY(16);
#pragma unroll 1
  for (; n < 20; n += 2) {
    CHK_BEGIN(n == 16 ? 4 : -1, n == 10 ? 3 : n == 18 ? 4 : -1);
    V_USE(B, n); wo_t = n < 18 ? n + 14 : -1; ISSUE(B, NGW + n + 2);
    V_USE(A, n + 1); wo_t = n < 18 ? n + 15 : -1; ISSUE(A, NGW + n + 3);
    CHK_END();
  }
  WO_COPY(24);
  wo_t = -1;
  const int mloff = WS_ML + ((b * NH + h) * 16 + (fresh_lane() & 15)) * 16;
  v4u ml = poll16(rws, mloff);
  V_USE(B, n); ISSUE(B, NGW + n + 2); V_USE(A, n + 1); ISSUE(A, NGW + n + 3);
  for (unsigned spins = 0; !__all((ml[1] == tagA) & (ml[3] == tagB)); ) {
    if (++spins > SPIN_CAP) break;
    __builtin_amdgcn_s_sleep(2);
    ml = poll16(rws, mloff);
  }
  float fw;
  {
    const float m_i = __uint_as_float(ml[0]), l_i = __uint_as_float(ml[2]);
    float M = m_i;
    M = fmaxf(M, dpp<XOR1>(M)); M = fmaxf(M, dpp<XOR2>(M)); M = fmaxf(M, dpp<XOR7>(M)); M = fmaxf(M, dpp<XOR8>(M));
    float Ls = l_i * ex2(m_i - M);
    Ls = oct_sum(Ls); Ls += dpp<XOR8>(Ls);
    fw = __uint_as_float(__builtin_amdgcn_readfirstlane(__float_as_uint(ex2(mw - M) / Ls * *(LAS float*)(L + L_MISC))));
  }
#pragma unroll 1
  for (n += 2; n < NGW - 2; n += 2) {
    V_USE(B, n); ISSUE(B, NGW + n + 2); V_USE(A, n + 1); ISSUE(A, NGW + n + 3);
  }
  V_USE(B, NGW - 2);
  V_USE(A, NGW - 1);

  STAMP(7);
  TR(40);
  {
    float b12[12], c6[6];
#pragma unroll
    for (int i = 0; i < 12; ++i) {
      auto r = __builtin_amdgcn_permlane32_swap(__float_as_uint(acc2[i >> 1][i & 1]), __float_as_uint(acc2[6 + (i >> 1)][i & 1]), false, false);
      b12[i] = __uint_as_float(r[0]) + __uint_as_float(r[1]);
    }
#pragma unroll
    for (int i = 0; i < 6; ++i) {
      auto r = __builtin_amdgcn_permlane16_swap(__float_as_uint(b12[i]), __float_as_uint(b12[i + 6]), false, false);
      c6[i] = __uint_as_float(r[0]) + __uint_as_float(r[1]);
    }
#pragma unroll
    for (int i = 0; i < 6; ++i) c6[i] = (c6[i] + dpp<XOR8>(c6[i])) * fw;
    const int ln = fresh_lane();
    if ((ln & 8) == 0) {
      const int v0 = (ln >> 4) * 6;
#pragma unroll
      for (int p = 0; p < 3; ++p) { const int v = v0 + 2 * p; *(LAS v2f*)(L + L_QP + (w * QPS + (v >> 3) * 64 + (ln & 7) * 8 + (v & 7)) * 4) = v2f{c6[2 * p], c6[2 * p + 1]}; }
    }
  }
  const int ln = fresh_lane();
  const int yr = ln < 60 ? ln / 10 : 0, yc = ln - (ln / 10) * 10;
  float ya[8];
  { const LAS v4f* p = (const LAS v4f*)(L + L_YF + (w * 480 + yr * 80 + yc * 8) * 4); const v4f a = p[0], bb = p[1];
#pragma unroll
    for (int e = 0; e < 4; ++e) { ya[e] = a[e]; ya[4 + e] = bb[e]; } }
  STAMP(8);
  TR(41);
  __syncthreads();
  TR(42);
  if (w < 6) {
    const int pi = w * 16 + (ln >> 2), r = pi >> 4, u = pi & 15, qq = ln & 3;
    const LAS float* p = (const LAS float*)(L + L_QP) + 2 * qq * QPS + u * 12 + r;
    float a0 = p[0] + p[QPS], a1 = p[6] + p[QPS + 6];
    a0 += dpp<XOR1>(a0); a1 += dpp<XOR1>(a1); a0 += dpp<XOR2>(a0); a1 += dpp<XOR2>(a1);
    if (qq == 0) *(LAS unsigned*)(L + L_OB + pi * 4) = cvtpk(a0, a1);
  }
  TR(43);
  __syncthreads();
  TR(44);
  {
    LAS float* yrp = (LAS float*)(L + L_FP) + w * 480;
    const LAS v4u* op = (const LAS v4u*)(L + L_OB + yr * 64);
#pragma unroll
    for (int u4 = 0; u4 < 4; ++u4) {
      const v4u o4 = op[u4];
#pragma unroll
      for (int e = 0; e < 4; ++e) {
        const int u = u4 * 4 + e;
#pragma unroll
        for (int d = 0; d < 4; ++d) { ya[2 * d] = dot2(W[2 * u][d], o4[e], ya[2 * d]); ya[2 * d + 1] = dot2(W[2 * u + 1][d], o4[e], ya[2 * d + 1]); }
      }
    }
    asm volatile("" : "+v"(ya[0]), "+v"(ya[1]), "+v"(ya[2]), "+v"(ya[3]), "+v"(ya[4]), "+v"(ya[5]), "+v"(ya[6]), "+v"(ya[7]));
    TR(45);
    if (ln < 60) { LAS v4f* p = (LAS v4f*)(yrp + yr * 80 + yc * 8); p[0] = v4f{ya[0], ya[1], ya[2], ya[3]}; p[1] = v4f{ya[4], ya[5], ya[6], ya[7]}; }
    LDS_FENCE();
    TR(46);
    if (ln < 40) {
      v2f y = v2f{0.f, 0.f};
#pragma unroll
      for (int r = 0; r < 6; ++r) y += *(LAS v2f*)(yrp + r * 80 + ln * 2);
      const int cons = w * 8 + ln / 5, g5 = ln % 5;
      __builtin_amdgcn_raw_buffer_store_b128(v4u{__float_as_uint(y[0]), tagA, __float_as_uint(y[1]), tagB}, rws, WS_INBOX + (((b * 64 + cons) * 64 + j) * 5 + g5) * 16, 0, AUX_SC1);
    }
  }
  STAMP(9);
  TR(47);
  if (w == 0) {
    const int io = WS_INBOX + ((b * 64 + j) * 64 + ln) * 80;
    v4u g5[5];
    for (unsigned spins = 0;;) {
      bool ok = true;
#pragma unroll
      for (int e = 0; e < 5; ++e) g5[e] = poll16(rws, io + e * 16);
#pragma unroll
      for (int e = 0; e < 5; ++e) ok = ok & (g5[e][1] == tagA) & (g5[e][3] == tagB);
      if (__all(ok) || ++spins > SPIN_CAP) break;
      __builtin_amdgcn_s_sleep(1);
    }
    TR(48);
    const unsigned yv[10] = {g5[0][0], g5[0][2], g5[1][0], g5[1][2], g5[4][0], g5[2][0], g5[2][2], g5[3][0], g5[3][2], g5[4][2]};
    float b5[5];
#pragma unroll
    for (int i = 0; i < 5; ++i) { auto r = __builtin_amdgcn_permlane32_swap(yv[i], yv[5 + i], false, false); b5[i] = __uint_as_float(r[0]) + __uint_as_float(r[1]); }
    float z[3];
    { auto r = __builtin_amdgcn_permlane16_swap(__float_as_uint(b5[0]), __float_as_uint(b5[2]), false, false); z[0] = __uint_as_float(r[0]) + __uint_as_float(r[1]); }
    { auto r = __builtin_amdgcn_permlane16_swap(__float_as_uint(b5[1]), __float_as_uint(b5[3]), false, false); z[1] = __uint_as_float(r[0]) + __uint_as_float(r[1]); }
    { auto r = __builtin_amdgcn_permlane16_swap(__float_as_uint(b5[4]), __float_as_uint(b5[4]), false, false); z[2] = __uint_as_float(r[0]) + __uint_as_float(r[1]); }
#pragma unroll
    for (int i = 0; i < 3; ++i) { z[i] += dpp<XOR1>(z[i]); z[i] += dpp<XOR2>(z[i]); z[i] += dpp<XOR7>(z[i]); z[i] += dpp<XOR8>(z[i]); }
    auto r89 = __builtin_amdgcn_permlane32_swap(__float_as_uint(z[2]), __float_as_uint(z[2]), false, false);
    const unsigned o03 = cvtpk(z[0], z[1]), o4 = cvtpk(__uint_as_float(r89[0]), __uint_as_float(r89[1]));
    if ((ln & 15) == 0 || ln == 1) *(GAS unsigned*)(out + b * DM + j * 10 + (ln == 1 ? 8 : (ln >> 4) * 2)) = ln == 1 ? o4 : o03;
    if (b == 0 && j == 0 && ln == 0) __hip_atomic_store((GAS u64*)(ws + WS_EPOCH), ((u64)tagB << 32) | tagA, __ATOMIC_RELAXED, __HIP_MEMORY_SCOPE_AGENT);
  }
  TR(49);
}
}

extern "C" void kernel_launch(void* const* d_in, const int* in_sizes, int n_in, void* d_out, int out_size, void* d_ws, size_t ws_size,
                              hipStream_t stream) {
  (void)in_sizes; (void)n_in; (void)out_size; (void)ws_size;
  hipLaunchKernelGGL(fused_decode, dim3(NWG), dim3(NTHR), 0, stream, (const u16*)d_in[0], (const u16*)d_in[1], (const u16*)d_in[2], (const u16*)d_in[3],
                     (const u16*)d_in[4], (const u16*)d_in[6], (const u16*)d_in[7], (const u16*)d_in[8], (const u16*)d_in[9], (u16*)d_out, (unsigned char*)d_ws);
}
```

```cpp
#include <hip/hip_runtime.h>
#include <hip/hip_bf16.h>
#include <stdint.h>

#define GAS __attribute__((address_space(1)))
#define LAS __attribute__((address_space(3)))
typedef unsigned v4u __attribute__((ext_vector_type(4)));
typedef unsigned v2u __attribute__((ext_vector_type(2)));
typedef float v4f __attribute__((ext_vector_type(4)));
typedef float v2f __attribute__((ext_vector_type(2)));
typedef __bf16 bf16x2 __attribute__((ext_vector_type(2)));
typedef unsigned long long u64;
typedef unsigned short u16;

namespace {
constexpr int NB = 4, NH = 32, SK = 8192, DH = 192, DM = 640, FF = 1536;
constexpr int NWG = 256, NWAVE = 8, NTHR = NWAVE * 64;
constexpr int KEYS_WG = SK / 2;
constexpr int ROWB = DH * 2;
constexpr int TILEB = 8 * ROWB;
constexpr int GRPB = 2 * TILEB;
constexpr int KVB = KEYS_WG * ROWB;
constexpr int NGW = KVB / GRPB / NWAVE;
constexpr int RSRC3 = 0x00020000;
#define POLL_AUX 16
#define STORE_AUX 16
constexpr int AUX_NT = 2, AUX_SC1 = STORE_AUX;
constexpr int OOB = 1 << 28;
constexpr float CS = 0.07216878364870322f * 1.4426950408889634f;
#define TAU16 (174 * 16)
#define XHEAD16 (TAU16 * 10 / 10)
#define XPOLL16 (TAU16 * 3 / 10)
#define XFFN16 (TAU16 * 3 / 10)
#define XDMA16 (TAU16 * 3 / 100)
#define CUSPACE 60
#define WSTOUCH 1
#define VTOUCH 0
#define ENDSYNC 0
#define ADAPT 0
#define TAUMIN16 (171 * 16)
#define TAUMAX16 (188 * 16)
#define TAUUP16 64
#define TAUDN16 32
#define MISS_HI 12
#define MISS_LO 4
#define ALPHA 0
#define PRIO 1
#define PHW 0

constexpr int WS_EPOCH = 0;
constexpr int WS_ML = 4096;
constexpr int WS_INBOX = 65536;
constexpr int WS_QX = WS_INBOX + NB * 64 * 64 * 80;
constexpr int WS_FX = WS_QX + NH * NB * 8 * 128 * 16;
constexpr int WS_FL = WS_FX + 8 * NB * 8 * 384 * 16;
constexpr int WS_BYTES = WS_FL + 8 * 1024;

constexpr int L_XS = 0;
constexpr int L_QP = 5120;
constexpr int QPS = 196;
constexpr int L_QB = L_QP + 8 * QPS * 4;
constexpr int L_OB = L_QB + 384;
constexpr int L_MISC = L_OB + 384;
constexpr int L_G = L_MISC + 256;
constexpr int L_YF = L_G + 3072;
constexpr int L_SC = L_YF + 8 * 480 * 4;
constexpr int L_FP2 = L_YF;
constexpr int L_FP = L_YF + 22 * 4 * 192 * 4;
constexpr int L_SCR = L_FP + 22 * 4 * 192 * 4;
constexpr int L_TR = L_SCR + 256;
constexpr int L_BYTES = L_TR;

constexpr unsigned SPIN_CAP = 1u << 17;

__device__ __forceinline__ float dot2(unsigned a, unsigned b, float c) {
  return __builtin_amdgcn_fdot2_f32_bf16(__builtin_bit_cast(bf16x2, a), __builtin_bit_cast(bf16x2, b), c, false);
}
template <int CTRL> __device__ __forceinline__ float dpp(float x) {
  return __builtin_bit_cast(float, __builtin_amdgcn_mov_dpp(__builtin_bit_cast(int, x), CTRL, 0xf, 0xf, true));
}
constexpr int XOR1 = 0xB1, XOR2 = 0x4E, XOR7 = 0x141, XOR8 = 0x128;
__device__ __forceinline__ float sum16_32(float x) {
  auto s = __builtin_amdgcn_permlane16_swap(__float_as_uint(x), __float_as_uint(x), false, false);
  x = __uint_as_float(s[0]) + __uint_as_float(s[1]);
  auto t = __builtin_amdgcn_permlane32_swap(__float_as_uint(x), __float_as_uint(x), false, false);
  return __uint_as_float(t[0]) + __uint_as_float(t[1]);
}
__device__ __forceinline__ float max16_32(float x) {
  auto s = __builtin_amdgcn_permlane16_swap(__float_as_uint(x), __float_as_uint(x), false, false);
  x = fmaxf(__uint_as_float(s[0]), __uint_as_float(s[1]));
  auto t = __builtin_amdgcn_permlane32_swap(__float_as_uint(x), __float_as_uint(x), false, false);
  return fmaxf(__uint_as_float(t[0]), __uint_as_float(t[1]));
}
__device__ __forceinline__ float oct_sum(float x) { x += dpp<XOR1>(x); x += dpp<XOR2>(x); x += dpp<XOR7>(x); return x; }
__device__ __forceinline__ float xoct_sum(float x) { x += dpp<XOR8>(x); return sum16_32(x); }
__device__ __forceinline__ float wave_sum(float x) { return xoct_sum(oct_sum(x)); }
__device__ __forceinline__ unsigned cvtpk(float lo, float hi) { unsigned r; asm("v_cvt_pk_bf16_f32 %0, %1, %2" : "=v"(r) : "v"(lo), "v"(hi)); return r; }
__device__ __forceinline__ float bflo(unsigned u) { return __uint_as_float(u << 16); }
__device__ __forceinline__ float bfhi(unsigned u) { return __uint_as_float(u & 0xffff0000u); }
__device__ __forceinline__ float ex2(float x) { return __builtin_amdgcn_exp2f(x); }
__device__ __forceinline__ __amdgpu_buffer_rsrc_t rsrc(const void* p, int bytes) { return __builtin_amdgcn_make_buffer_rsrc((void*)p, (short)0, bytes, RSRC3); }
template <int AUX> __device__ __forceinline__ v4u ld16(__amdgpu_buffer_rsrc_t r, int voff, int soff) { return __builtin_amdgcn_raw_buffer_load_b128(r, voff, soff, AUX); }
template <int AUX> __device__ __forceinline__ v2u ld8(__amdgpu_buffer_rsrc_t r, int voff, int soff) { return __builtin_amdgcn_raw_buffer_load_b64(r, voff, soff, AUX); }
__device__ __forceinline__ unsigned now32() { return (unsigned)__builtin_amdgcn_s_memrealtime(); }
#define STAMP(i) do { } while (0)
#define TR(id) do { } while (0)
#define TRV(id, val) do { } while (0)
#define SCHED_FENCE() __builtin_amdgcn_sched_barrier(0)
#define LDS_FENCE() asm volatile("s_waitcnt lgkmcnt(0)" ::: "memory")
#define MEM_FENCE() asm volatile("" ::: "memory")
__device__ __forceinline__ v4u poll16(__amdgpu_buffer_rsrc_t r, int voff) { asm volatile("" : "+v"(voff) : : "memory"); return __builtin_amdgcn_raw_buffer_load_b128(r, voff, 0, POLL_AUX); }
__device__ __forceinline__ int fresh_lane() { int l; asm volatile("v_mbcnt_lo_u32_b32 %0, -1, 0\n\tv_mbcnt_hi_u32_b32 %0, -1, %0" : "=v"(l)); return l; }

#define WO_POLICY ""
__device__ __forceinline__ void dma16(__amdgpu_buffer_rsrc_t r, int voff, int soff, unsigned lds) {
  unsigned keep;
  asm volatile("s_mov_b32 %0, m0\n\ts_mov_b32 m0, %3\n\ts_nop 0\n\tbuffer_load_dwordx4 %1, %2, %4 offen " WO_POLICY " lds\n\ts_mov_b32 m0, %0"
               : "=&s"(keep) : "v"(voff), "s"(r), "s"(lds), "s"(soff) : "memory");
}
__device__ __forceinline__ void dma4(__amdgpu_buffer_rsrc_t r, int voff, unsigned lds) {
  unsigned keep;
  asm volatile("s_mov_b32 %0, m0\n\ts_mov_b32 m0, %3\n\ts_nop 0\n\tbuffer_load_dword %1, %2, 0 offen nt lds\n\ts_mov_b32 m0, %0"
               : "=&s"(keep) : "v"(voff), "s"(r), "s"(lds) : "memory");
}
#define KV_AUX AUX_NT
#define WQ_AUX AUX_NT
#define W2_AUX 0
struct Grp { v4u r[2][3]; };

__global__ void __launch_bounds__(NTHR, 2)
fused_decode(const u16* __restrict__ x, const u16* __restrict__ Kc, const u16* __restrict__ Vc, const u16* __restrict__ wq,
             const u16* __restrict__ wo, const u16* __restrict__ w1, const u16* __restrict__ w3, const u16* __restrict__ w2,
             const u16* __restrict__ gw, u16* __restrict__ out, unsigned char* ws) {
  __shared__ __attribute__((aligned(16))) unsigned char lds_raw[L_BYTES];
  LAS unsigned char* L = (LAS unsigned char*)lds_raw;
  const int tid = threadIdx.x, lane = tid & 63, w = __builtin_amdgcn_readfirstlane(tid >> 6);
  const int k = lane >> 3, c = lane & 7;
  const int blk = blockIdx.x, xg = blk & 7, idx = blk >> 3;
  const int h = xg * 4 + (idx & 3), sh = idx >> 2, b = sh >> 1, s = sh & 1;
  const int j = h * 2 + s;
  STAMP(0);
#define PHASE 0
#define T0OFF 50
#define XGADJ 12
  unsigned T0 = now32() + T0OFF + (3 - xg) * XGADJ + idx * PHASE * (TAU16 / 32) / (16 * 32);
  const __amdgpu_buffer_rsrc_t rws = rsrc(ws, WS_BYTES);
  const v4u tiny = __builtin_nontemporal_load((const GAS v4u*)(tid == 320 ? (const unsigned char*)ws + WS_EPOCH : (const unsigned char*)x + (tid < 320 ? tid : 0) * 16));
  const int qr = tid / 24, qc = tid - qr * 24;
  v4u wqv[4], fwa, fwb;
  {
    const __amdgpu_buffer_rsrc_t rq = rsrc(wq + h * DH, (DM * NH * DH - h * DH) * 2);
    const int oob = tid < 480 ? 0 : OOB;
#pragma unroll
    for (int e = 0; e < 4; ++e) wqv[e] = ld16<WQ_AUX>(rq, (sh * 80 + qr * 4) * (NH * DH * 2) + qc * 16 + oob, e * (NH * DH * 2));
    const int row0 = (idx >> 1) * 40 + qr * 2;
    const __amdgpu_buffer_rsrc_t r13 = rsrc(((idx & 1) ? w3 : w1) + xg * 192, (DM * FF - xg * 192) * 2);
    fwa = ld16<WQ_AUX>(r13, row0 * (FF * 2) + qc * 16 + oob, 0); fwb = ld16<WQ_AUX>(r13, row0 * (FF * 2) + qc * 16 + oob, FF * 2);
  }
  const size_t kvoff = ((size_t)(b * NH + h) * SK + (size_t)s * KEYS_WG) * DH;
  const u16* Kb = Kc + kvoff; const u16* Vb = Vc + kvoff;
  if (w == 7) {
    const int o = lane == 0 ? WS_QX + ((h * 4) * 8 + sh) * 2048 : lane == 1 ? WS_QX + (h * 4 + 3) * 8 * 2048 + 16380
                : lane == 2 ? WS_FX + xg * 4 * 8 * 6144 : lane == 3 ? WS_FX + (xg * 4 + 3) * 8 * 6144 + 8 * 6144 - 4
                : lane == 4 ? WS_INBOX + b * 64 * 64 * 80 : lane == 5 ? WS_INBOX + (b + 1) * 64 * 64 * 80 - 4 : lane == 6 ? WS_ML : OOB;
    dma4(rws, o, L_SCR);
  }
#define SIGMA 1
  const int sigma = SIGMA == 0 ? 0 : SIGMA == 1 ? (int)((blockIdx.x * 2246822519u) >> 29) : (int)((blockIdx.x >> 3) & 7);
  const int kvo = ((w + sigma) & 7) * GRPB + k * ROWB + c * 16;
#define WPH 1
#define GPH 0
  bool alpha_on = false;
  unsigned tn = (T0 << 4) + (WPH == 1 ? w * (TAU16 / 8) : WPH == 2 ? (w >> 2) * (TAU16 / 2) : 0) + (idx & 3) * (TAU16 * GPH / 32);
  unsigned tmin = tn;
  const bool lane0w = lane == 0;
  auto pace = [&](unsigned step16) {
    const unsigned tt = (ALPHA && (int)(tmin - tn) > 0) ? tmin : tn;
    unsigned now = now32() << 4;
    const int late = (int)(now - tn);
    if (late > 2 * TAU16) __builtin_amdgcn_s_setprio(3);
    else if (late > TAU16) __builtin_amdgcn_s_setprio(2);
    else if (late > TAU16 / 4) __builtin_amdgcn_s_setprio(1);
    else __builtin_amdgcn_s_setprio(0);
    while ((int)(now - tt) < 0) { __builtin_amdgcn_s_sleep(1); now = now32() << 4; }
    if (ALPHA) tmin = now + (alpha_on ? TAU16 * ALPHA / 100 : 0);
    if (alpha_on) {
      const unsigned base = T0 << 4;
      unsigned v = 0;
      if (lane0w) v = __hip_atomic_fetch_add((LAS unsigned*)(L + L_MISC + 24 + 4 * 2), (unsigned)(TAU16 / 8 * CUSPACE / 100), __ATOMIC_RELAXED, __HIP_MEMORY_SCOPE_WORKGROUP);
      v = __builtin_amdgcn_readfirstlane(v);
      now = (now32() << 4) - base;
      if ((int)(v - now) > 0) { while ((int)(((now32() << 4) - base) - v) < 0) __builtin_amdgcn_s_sleep(1); }
      else if (lane0w) __hip_atomic_fetch_max((LAS unsigned*)(L + L_MISC + 24 + 4 * 2), now + (unsigned)(TAU16 / 8 * CUSPACE / 100), __ATOMIC_RELAXED, __HIP_MEMORY_SCOPE_WORKGROUP);
    }
    tn += step16;
  };
#define ROT 2
  const int rho = ROT == 0 ? 0 : ROT == 1 ? ((blk * 11) & 31) : ROT == 2 ? (((blk >> 3) + (blk & 7) * 4) & 31) : ROT == 3 ? ((int)((blk * 2654435761u) >> 27)) : (((blk >> 3) * 5 + (blk & 7) * 3) & 31);
#define AMUL 0
  const int amul = AMUL == 0 ? 1 : AMUL == 1 ? (int)(((blk * 0x9E3779B1u) >> 28) * 2 + 1) : (int)((((blk >> 3) + 3 * (blk & 7)) & 15) * 2 + 1);
  unsigned tau = TAU16;
  int wo_t = -1;
  __amdgpu_buffer_rsrc_t ro = rsrc(wo + (size_t)h * DH * DM, DH * DM * 2);
  int wo_vo = 0;
  auto issue = [&](v4u& r00, v4u& r01, v4u& r02, v4u& r10, v4u& r11, v4u& r12, int m) {
    TR(1);
    pace(tau + (wo_t >= 0 ? XDMA16 : 0));
    TR(2);
    if (wo_t >= 0) dma16(ro, wo_vo, wo_t * 6 * (DM * 2), (unsigned)(L_FP + w * 8192 + (wo_t & 7) * 1024));
    const __amdgpu_buffer_rsrc_t r = rsrc(m < NGW ? Kb : Vb, KVB);
    const int so = ((m * amul + rho) & (NGW - 1)) * (NWAVE * GRPB);
    r00 = ld16<KV_AUX>(r, kvo, so); r01 = ld16<KV_AUX>(r, kvo + 128, so); r02 = ld16<KV_AUX>(r, kvo + 256, so);
    r10 = ld16<KV_AUX>(r, kvo, so + TILEB); r11 = ld16<KV_AUX>(r, kvo + 128, so + TILEB); r12 = ld16<KV_AUX>(r, kvo + 256, so + TILEB);
    SCHED_FENCE();
    TR(3);
  };
#define ISSUE(G, m) issue(G.r[0][0], G.r[0][1], G.r[0][2], G.r[1][0], G.r[1][1], G.r[1][2], m)
#define ISSUE_W(o, m) issue(W[o], W[o + 1], W[o + 2], W[o + 3], W[o + 4], W[o + 5], m)
  Grp A, B;
  v4u W[32];
  ISSUE(A, 0);
  ISSUE(B, 1);
  tn += XHEAD16;
  STAMP(1);
  if (tid <= 320) *(LAS v4u*)(L + (tid < 320 ? L_XS + tid * 16 : L_MISC + 16)) = tiny;
  if (tid == 511) { *(LAS unsigned*)(L + L_MISC + 12) = TAU16; *(LAS unsigned*)(L + L_MISC + 32) = 0u; }
  __syncthreads();
  const unsigned tagA = __builtin_amdgcn_readfirstlane(*(LAS unsigned*)(L + L_MISC + 16)) + 1u, tagB = __builtin_amdgcn_readfirstlane(*(LAS unsigned*)(L + L_MISC + 20)) + 1u;
  v2u xq[4]; unsigned fx4[4];
  { const int qrc = qr < 20 ? qr : 0;
#pragma unroll
    for (int e = 0; e < 4; ++e) { xq[e] = *(LAS v2u*)(L + L_XS + e * (DM * 2) + (sh * 80 + qrc * 4) * 2); fx4[e] = *(LAS unsigned*)(L + L_XS + e * (DM * 2) + ((idx >> 1) * 40 + qrc * 2) * 2); } }
  {
    unsigned lo01[4], hi01[4], lo23[4], hi23[4];
#pragma unroll
    for (int d = 0; d < 4; ++d) {
      lo01[d] = __builtin_amdgcn_perm(wqv[1][d], wqv[0][d], 0x05040100u); hi01[d] = __builtin_amdgcn_perm(wqv[1][d], wqv[0][d], 0x07060302u);
      lo23[d] = __builtin_amdgcn_perm(wqv[3][d], wqv[2][d], 0x05040100u); hi23[d] = __builtin_amdgcn_perm(wqv[3][d], wqv[2][d], 0x07060302u);
    }
#pragma unroll
    for (int bb = 0; bb < 4; ++bb) {
      float a[8];
#pragma unroll
      for (int d = 0; d < 4; ++d) { a[2 * d] = dot2(lo23[d], xq[bb][1], dot2(lo01[d], xq[bb][0], 0.f)); a[2 * d + 1] = dot2(hi23[d], xq[bb][1], dot2(hi01[d], xq[bb][0], 0.f)); }
      LAS v4f* p = (LAS v4f*)(L + L_FP + ((qr * 4 + bb) * 192 + qc * 8) * 4);
      p[0] = v4f{a[0], a[1], a[2], a[3]}; p[1] = v4f{a[4], a[5], a[6], a[7]};
    }
  }
  {
    unsigned lo[4], hi[4];
#pragma unroll
    for (int d = 0; d < 4; ++d) { lo[d] = __builtin_amdgcn_perm(fwb[d], fwa[d], 0x05040100u); hi[d] = __builtin_amdgcn_perm(fwb[d], fwa[d], 0x07060302u); }
#pragma unroll
    for (int bb = 0; bb < 4; ++bb) {
      LAS v4f* p = (LAS v4f*)(L + L_FP2 + ((qr * 4 + bb) * 192 + qc * 8) * 4);
      p[0] = v4f{dot2(lo[0], fx4[bb], 0.f), dot2(hi[0], fx4[bb], 0.f), dot2(lo[1], fx4[bb], 0.f), dot2(hi[1], fx4[bb], 0.f)};
      p[1] = v4f{dot2(lo[2], fx4[bb], 0.f), dot2(hi[2], fx4[bb], 0.f), dot2(lo[3], fx4[bb], 0.f), dot2(hi[3], fx4[bb], 0.f)};
    }
  }
  TR(9);
  __syncthreads();
  TR(10);
  STAMP(2);
  if (tid < 384) {
    const int bb = tid / 96, p = tid - bb * 96;
    float a0 = 0.f, a1 = 0.f;
#pragma unroll
    for (int r = 0; r < 20; ++r) { const v2f v = *(LAS v2f*)(L + L_FP + ((r * 4 + bb) * 192 + p * 2) * 4); a0 += v[0]; a1 += v[1]; }
    __builtin_amdgcn_raw_buffer_store_b128(v4u{__float_as_uint(a0), tagA, __float_as_uint(a1), tagB}, rws, WS_QX + (((h * 4 + bb) * 8 + sh) * 128 + p) * 16, 0, AUX_SC1);
    a0 = 0.f; a1 = 0.f;
#pragma unroll
    for (int r = 0; r < 20; ++r) { const v2f v = *(LAS v2f*)(L + L_FP2 + ((r * 4 + bb) * 192 + p * 2) * 4); a0 += v[0]; a1 += v[1]; }
    __builtin_amdgcn_raw_buffer_store_b128(v4u{__float_as_uint(a0), tagA, __float_as_uint(a1), tagB}, rws,
                                           WS_FX + (((((xg * 4 + bb) * 8 + p / 12) * 2 + (idx & 1)) * 16 + (idx >> 1)) * 12 + p % 12) * 16, 0, AUX_SC1);
  }
  if (tid >= 384 && tid < 388)
    __builtin_amdgcn_raw_buffer_store_b128(v4u{T0, tagA, T0, tagB}, rws, WS_QX + (((h * 4 + (tid - 384)) * 8 + sh) * 128 + 96) * 16, 0, AUX_SC1);
  SCHED_FENCE();
  TR(11);
  ISSUE_W(0, 2);
  ISSUE_W(6, 3);
  ISSUE_W(12, 4);
  TR(12);
  if (w == 0) {
    __builtin_amdgcn_s_setprio(3);
    const int qo = WS_QX + (h * 4 + b) * 8 * 2048 + lane * 16;
    v4u ga[8], gb[8];
    for (unsigned spins = 0;;) {
#pragma unroll
      for (int p = 0; p < 8; ++p) { ga[p] = poll16(rws, qo + p * 2048); gb[p] = poll16(rws, qo + p * 2048 + 1024 + (lane <= 32 ? 0 : OOB)); }
      bool ok = true;
#pragma unroll
      for (int p = 0; p < 8; ++p) ok = ok & (ga[p][1] == tagA) & (ga[p][3] == tagB) & ((lane > 32) | ((gb[p][1] == tagA) & (gb[p][3] == tagB)));
      TR(17);
      if (__all(ok) || ++spins > SPIN_CAP) break;
      __builtin_amdgcn_s_sleep(1);
    }
    TR(16);
    float a0 = 0.f, a1 = 0.f, b0 = 0.f, b1 = 0.f;
#pragma unroll
    for (int p = 0; p < 8; ++p) { a0 += __uint_as_float(ga[p][0]); a1 += __uint_as_float(ga[p][2]); b0 += __uint_as_float(gb[p][0]); b1 += __uint_as_float(gb[p][2]); }
    *(LAS unsigned*)(L + L_QB + lane * 4) = cvtpk(a0, a1);
    if (lane < 32) *(LAS unsigned*)(L + L_QB + 256 + lane * 4) = cvtpk(b0, b1);
    unsigned tl = gb[0][0];
#pragma unroll
    for (int p = 1; p < 8; ++p) tl = (int)(gb[p][0] - tl) > 0 ? gb[p][0] : tl;
    if (lane == 32) *(LAS unsigned*)(L + L_MISC + 8) = tl;
  }
  TR(13);
  tn += XPOLL16;
  ISSUE_W(18, 5);
  ISSUE_W(24, 6);
  __syncthreads();
  TR(14);
  STAMP(3);
  { const unsigned tl = __builtin_amdgcn_readfirstlane(*(LAS unsigned*)(L + L_MISC + 8)); if (tl - T0 < 1000u) tn += (tl - T0) << 4; }
#define SLACK 3
  { const unsigned t = (now32() << 4) - SLACK * TAU16; if ((int)(t - tn) > 0) tn = t; }
  alpha_on = true;
  v4u Q[3];
#pragma unroll
  for (int i = 0; i < 3; ++i) Q[i] = *(LAS v4u*)(L + L_QB + (c + 8 * i) * 16);

  float mx = -INFINITY;
  const bool oct0 = c == 0;
  const int scoff = L_SC + w * 2048 + k * 8;
  auto k_tile = [&](const v4u& r0, const v4u& r1, const v4u& r2) -> float {
    float a0 = dot2(r0[0], Q[0][0], 0.f), a1 = dot2(r1[0], Q[1][0], 0.f), a2 = dot2(r2[0], Q[2][0], 0.f);
#pragma unroll
    for (int d = 1; d < 4; ++d) { a0 = dot2(r0[d], Q[0][d], a0); a1 = dot2(r1[d], Q[1][d], a1); a2 = dot2(r2[d], Q[2][d], a2); }
    return (a0 + a1) + a2;
  };
  auto k_put = [&](float s0, float s1, int n) {
    s0 = oct_sum(s0); s1 = oct_sum(s1);
    mx = fmaxf(mx, fmaxf(s0, s1));
    if (oct0) *(LAS v2f*)(L + scoff + n * 64) = v2f{s0, s1};
  };
#define K_STEP(G, n, m) do { float s0_ = k_tile(G.r[0][0], G.r[0][1], G.r[0][2]), s1_ = k_tile(G.r[1][0], G.r[1][1], G.r[1][2]); \
    asm volatile("" : "+v"(s0_), "+v"(s1_) : : "memory"); ISSUE(G, m); k_put(s0_, s1_, n); } while (0)
  K_STEP(A, 0, 7);
#pragma unroll
  for (int g = 0; g < 5; ++g) k_put(k_tile(W[g * 6], W[g * 6 + 1], W[g * 6 + 2]), k_tile(W[g * 6 + 3], W[g * 6 + 4], W[g * 6 + 5]), 2 + g);
  STAMP(4);
  K_STEP(B, 1, 8);
  STAMP(5);
  {
    const int lane = fresh_lane(), tid2 = w * 64 + lane;
    const int yr = lane / 10, yc = lane - yr * 10;
    const int fxo = WS_FX + ((xg * 4 + b) * 8 + (j & 7)) * 6144 + tid2 * 16 + (tid2 < 384 ? 0 : OOB);
    v4u fx = poll16(rws, fxo);
    const __amdgpu_buffer_rsrc_t r2 = rsrc(w2, FF * DM * 2), rg = rsrc(gw, w == 0 ? DM * 2 : 0);
    const int vo2 = (j * 24 + yr) * (DM * 2) + w * 160 + yc * 16 + (lane < 60 ? 0 : OOB);
#pragma unroll
    for (int t = 0; t < 4; ++t) W[8 + t] = ld16<W2_AUX>(r2, vo2, t * 6 * (DM * 2));
    const v4u gg0 = ld16<AUX_NT>(rg, lane * 16, 0), gg1 = ld16<AUX_NT>(rg, 1024 + lane * 16, 0);
    SCHED_FENCE();
    TR(30);
    tn += XFFN16;
    K_STEP(A, 7, 9); K_STEP(B, 8, 10);
    TR(31);
    for (unsigned spins = 0; !__all((tid2 >= 384) | ((fx[1] == tagA) & (fx[3] == tagB))); ) {
      if (++spins > SPIN_CAP) break;
      __builtin_amdgcn_s_sleep(2);
      fx = poll16(rws, fxo);
    }
    TR(32);
    volatile LAS unsigned* flag = (volatile LAS unsigned*)(L + L_MISC + 192);
    if (tid2 < 384) *(LAS v2f*)(L + L_G + ((tid2 / 12) * 24 + (tid2 % 12) * 2) * 4) = v2f{__uint_as_float(fx[0]), __uint_as_float(fx[2])};
    float gate = 0.f;
    if (w == 0) {
      const v4u gx0 = *(LAS v4u*)(L + L_XS + b * (DM * 2) + lane * 16), gx1 = *(LAS v4u*)(L + L_XS + b * (DM * 2) + 1024 + (lane & 15) * 16);
      float d = 0.f;
#pragma unroll
      for (int e = 0; e < 4; ++e) { d = dot2(gx0[e], gg0[e], d); d = dot2(gx1[e], gg1[e], d); }
      gate = 1.f / (1.f + __expf(-wave_sum(d)));
      if (lane == 0) *(LAS float*)(L + L_MISC) = gate;
    }
    LDS_FENCE();
    if (lane == 0) flag[w] = tagA;
    TR(33);
    K_STEP(A, 9, 11);
    if (w == 0) {
      for (unsigned spins = 0; !__all(lane >= 6 || flag[lane < 6 ? lane : 0] == tagA) && ++spins < (SPIN_CAP << 4); ) __builtin_amdgcn_s_sleep(1);
      MEM_FENCE();
      const LAS float* g = (const LAS float*)(L + L_G) + (lane < 24 ? lane : 0);
      float ga = 0.f, up = 0.f;
#pragma unroll
      for (int r = 0; r < 16; ++r) { ga += g[r * 24]; up += g[(16 + r) * 24]; }
      if (lane < 24) *(LAS float*)(L + L_MISC + 64 + lane * 4) = ga / (1.f + __expf(-ga)) * up * (1.f - gate);
      LDS_FENCE();
      if (lane == 0) flag[8] = tagA;
    }
    TR(34);
    K_STEP(B, 10, 12);
    for (unsigned spins = 0; flag[8] != tagA && ++spins < (SPIN_CAP << 4); ) __builtin_amdgcn_s_sleep(1);
    MEM_FENCE();
    v2f ya[4];
#pragma unroll
    for (int e = 0; e < 4; ++e) ya[e] = v2f{0.f, 0.f};
#pragma unroll
    for (int t = 0; t < 4; ++t) {
      const float hv = *(LAS float*)(L + L_MISC + 64 + (t * 6 + (yr < 6 ? yr : 0)) * 4);
#pragma unroll
      for (int d = 0; d < 4; ++d) ya[d] = __builtin_elementwise_fma(v2f{hv, hv}, v2f{bflo(W[8 + t][d]), bfhi(W[8 + t][d])}, ya[d]);
    }
    if (lane < 60) {
      LAS v4f* p = (LAS v4f*)(L + L_YF + (w * 480 + yr * 80 + yc * 8) * 4);
      p[0] = v4f{ya[0][0], ya[0][1], ya[1][0], ya[1][1]}; p[1] = v4f{ya[2][0], ya[2][1], ya[3][0], ya[3][1]};
    }
    wo_vo = yr * (DM * 2) + w * 160 + yc * 16 + (lane < 60 ? 0 : OOB);
  }
  MEM_FENCE();
  TR(35);
  STAMP(13);
#define WO_COPY(t0) do { asm volatile("s_waitcnt vmcnt(12)" ::: "memory"); const int ln_ = fresh_lane(); \
    _Pragma("unroll") for (int t = 0; t < 8; t += 2) {     \
      const v4u a_ = *(LAS v4u*)(L + L_FP + w * 8192 + t * 1024 + ln_ * 16), b_ = *(LAS v4u*)(L + L_FP + w * 8192 + (t + 1) * 1024 + ln_ * 16); \
      _Pragma("unroll") for (int d = 0; d < 4; ++d) { W[t0 + t][d] = __builtin_amdgcn_perm(b_[d], a_[d], 0x05040100u); W[t0 + t + 1][d] = __builtin_amdgcn_perm(b_[d], a_[d], 0x07060302u); } } \
    asm volatile("" : "+v"(W[t0]), "+v"(W[t0 + 1]), "+v"(W[t0 + 2]), "+v"(W[t0 + 3]), "+v"(W[t0 + 4]), "+v"(W[t0 + 5]), "+v"(W[t0 + 6]), "+v"(W[t0 + 7]) : : "memory"); } while (0)
#define CHK_BEGIN(pubc, rdc)
#define CHK_END()
#define V_TOUCH(n)
  int n;
#pragma unroll 1
  for (n = 11; n < 21; n += 2) {
    CHK_BEGIN(n == 15 ? 0 : -1, n == 17 ? 0 : -1);
    V_TOUCH(n);
    wo_t = n < 19 ? n - 11 : -1; K_STEP(A, n, n + 2);
    wo_t = n < 19 ? n - 10 : -1; K_STEP(B, n + 1, n + 3);
    CHK_END();
  }
  WO_COPY(0);
#pragma unroll 1
  for (; n < 31; n += 2) {
    CHK_BEGIN(n == 23 ? 1 : -1, n == 25 ? 1 : -1);
    V_TOUCH(n);
    wo_t = n < 29 ? n - 13 : -1; K_STEP(A, n, n + 2);
    wo_t = n < 29 ? n - 12 : -1; K_STEP(B, n + 1, n + 3);
    CHK_END();
  }
  WO_COPY(8);
  wo_t = -1;
  K_STEP(A, 31, 33);

  STAMP(6);
  mx = fmaxf(mx, dpp<XOR8>(mx)); mx = max16_32(mx);
  const float mw = mx * CS;
  LDS_FENCE();
  {
    const int lane = fresh_lane();
    LAS v4f* p = (LAS v4f*)(L + L_SC + w * 2048 + lane * 32);
    v4f a = p[0], bb = p[1];
    float l = 0.f;
#pragma unroll
    for (int e = 0; e < 4; ++e) { a[e] = ex2(fmaf(a[e], CS, -mw)); bb[e] = ex2(fmaf(bb[e], CS, -mw)); l += a[e] + bb[e]; }
    p[0] = a; p[1] = bb;
    l = wave_sum(l);
    if (lane == 0) __builtin_amdgcn_raw_buffer_store_b128(v4u{__float_as_uint(mw), tagA, __float_as_uint(l), tagB}, rws, WS_ML + (((b * NH + h) * 2 + s) * 8 + w) * 16, 0, AUX_SC1);
  }
  LDS_FENCE();
  STAMP(11);

  v2f acc2[12];
#pragma unroll
  for (int e = 0; e < 12; ++e) acc2[e] = v2f{0.f, 0.f};
#define PIN_ACC() asm volatile("" : "+v"(acc2[0]), "+v"(acc2[1]), "+v"(acc2[2]), "+v"(acc2[3]), "+v"(acc2[4]), "+v"(acc2[5]), "+v"(acc2[6]), "+v"(acc2[7]), \
                                    "+v"(acc2[8]), "+v"(acc2[9]), "+v"(acc2[10]), "+v"(acc2[11]) : : "memory")
  auto v_tile = [&](const v4u& r0, const v4u& r1, const v4u& r2, float p) {
    const v2f pp = v2f{p, p};
#pragma unroll
    for (int d = 0; d < 4; ++d) {
      acc2[d] = __builtin_elementwise_fma(pp, v2f{bflo(r0[d]), bfhi(r0[d])}, acc2[d]);
      acc2[4 + d] = __builtin_elementwise_fma(pp, v2f{bflo(r1[d]), bfhi(r1[d])}, acc2[4 + d]);
      acc2[8 + d] = __builtin_elementwise_fma(pp, v2f{bflo(r2[d]), bfhi(r2[d])}, acc2[8 + d]);
    }
    PIN_ACC();
  };
#define V_USE(G, n) do { const v2f p2_ = *(LAS v2f*)(L + scoff + (n) * 64); v_tile(G.r[0][0], G.r[0][1], G.r[0][2], p2_[0]); v_tile(G.r[1][0], G.r[1][1], G.r[1][2], p2_[1]); } while (0)
#pragma unroll 1
  for (n = 0; n < 10; n += 2) {
    CHK_BEGIN((n & 7) == 0 ? 2 + (n >> 3) : -1, n == 2 ? 2 : -1);
    V_USE(B, n); wo_t = n < 8 ? n + 16 : -1; ISSUE(B, NGW + n + 2);
    V_USE(A, n + 1); wo_t = n < 8 ? n + 17 : -1; ISSUE(A, NGW + n + 3);
    CHK_END();
  }
  WO_COPY(16);
#pragma unroll 1
  for (; n < 20; n += 2) {
    CHK_BEGIN(n == 16 ? 4 : -1, n == 10 ? 3 : n == 18 ? 4 : -1);
    V_USE(B, n); wo_t = n < 18 ? n + 14 : -1; ISSUE(B, NGW + n + 2);
    V_USE(A, n + 1); wo_t = n < 18 ? n + 15 : -1; ISSUE(A, NGW + n + 3);
    CHK_END();
  }
  WO_COPY(24);
  wo_t = -1;
  const int mloff = WS_ML + ((b * NH + h) * 16 + (fresh_lane() & 15)) * 16;
  v4u ml = poll16(rws, mloff);
  V_USE(B, n); ISSUE(B, NGW + n + 2); V_USE(A, n + 1); ISSUE(A, NGW + n + 3);
  for (unsigned spins = 0; !__all((ml[1] == tagA) & (ml[3] == tagB)); ) {
    if (++spins > SPIN_CAP) break;
    __builtin_amdgcn_s_sleep(2);
    ml = poll16(rws, mloff);
  }
  float fw;
  {
    const float m_i = __uint_as_float(ml[0]), l_i = __uint_as_float(ml[2]);
    float M = m_i;
    M = fmaxf(M, dpp<XOR1>(M)); M = fmaxf(M, dpp<XOR2>(M)); M = fmaxf(M, dpp<XOR7>(M)); M = fmaxf(M, dpp<XOR8>(M));
    float Ls = l_i * ex2(m_i - M);
    Ls = oct_sum(Ls); Ls += dpp<XOR8>(Ls);
    fw = __uint_as_float(__builtin_amdgcn_readfirstlane(__float_as_uint(ex2(mw - M) / Ls * *(LAS float*)(L + L_MISC))));
  }
#pragma unroll 1
  for (n += 2; n < NGW - 2; n += 2) {
    V_USE(B, n); ISSUE(B, NGW + n + 2); V_USE(A, n + 1); ISSUE(A, NGW + n + 3);
  }
  V_USE(B, NGW - 2);
  V_USE(A, NGW - 1);

  STAMP(7);
  TR(40);
  {
    float b12[12], c6[6];
#pragma unroll
    for (int i = 0; i < 12; ++i) {
      auto r = __builtin_amdgcn_permlane32_swap(__float_as_uint(acc2[i >> 1][i & 1]), __float_as_uint(acc2[6 + (i >> 1)][i & 1]), false, false);
      b12[i] = __uint_as_float(r[0]) + __uint_as_float(r[1]);
    }
#pragma unroll
    for (int i = 0; i < 6; ++i) {
      auto r = __builtin_amdgcn_permlane16_swap(__float_as_uint(b12[i]), __float_as_uint(b12[i + 6]), false, false);
      c6[i] = __uint_as_float(r[0]) + __uint_as_float(r[1]);
    }
#pragma unroll
    for (int i = 0; i < 6; ++i) c6[i] = (c6[i] + dpp<XOR8>(c6[i])) * fw;
    const int ln = fresh_lane();
    if ((ln & 8) == 0) {
      const int v0 = (ln >> 4) * 6;
#pragma unroll
      for (int p = 0; p < 3; ++p) { const int v = v0 + 2 * p; *(LAS v2f*)(L + L_QP + (w * QPS + (v >> 3) * 64 + (ln & 7) * 8 + (v & 7)) * 4) = v2f{c6[2 * p], c6[2 * p + 1]}; }
    }
  }
  const int ln = fresh_lane();
  const int yr = ln < 60 ? ln / 10 : 0, yc = ln - (ln / 10) * 10;
  float ya[8];
  { const LAS v4f* p = (const LAS v4f*)(L + L_YF + (w * 480 + yr * 80 + yc * 8) * 4); const v4f a = p[0], bb = p[1];
#pragma unroll
    for (int e = 0; e < 4; ++e) { ya[e] = a[e]; ya[4 + e] = bb[e]; } }
  STAMP(8);
  TR(41);
  __syncthreads();
  TR(42);
  if (w < 6) {
    const int pi = w * 16 + (ln >> 2), r = pi >> 4, u = pi & 15, qq = ln & 3;
    const LAS float* p = (const LAS float*)(L + L_QP) + 2 * qq * QPS + u * 12 + r;
    float a0 = p[0] + p[QPS], a1 = p[6] + p[QPS + 6];
    a0 += dpp<XOR1>(a0); a1 += dpp<XOR1>(a1); a0 += dpp<XOR2>(a0); a1 += dpp<XOR2>(a1);
    if (qq == 0) *(LAS unsigned*)(L + L_OB + pi * 4) = cvtpk(a0, a1);
  }
  TR(43);
  __syncthreads();
  TR(44);
  {
    LAS float* yrp = (LAS float*)(L + L_FP) + w * 480;
    const LAS v4u* op = (const LAS v4u*)(L + L_OB + yr * 64);
#pragma unroll
    for (int u4 = 0; u4 < 4; ++u4) {
      const v4u o4 = op[u4];
#pragma unroll
      for (int e = 0; e < 4; ++e) {
        const int u = u4 * 4 + e;
#pragma unroll
        for (int d = 0; d < 4; ++d) { ya[2 * d] = dot2(W[2 * u][d], o4[e], ya[2 * d]); ya[2 * d + 1] = dot2(W[2 * u + 1][d], o4[e], ya[2 * d + 1]); }
      }
    }
    asm volatile("" : "+v"(ya[0]), "+v"(ya[1]), "+v"(ya[2]), "+v"(ya[3]), "+v"(ya[4]), "+v"(ya[5]), "+v"(ya[6]), "+v"(ya[7]));
    TR(45);
    if (ln < 60) { LAS v4f* p = (LAS v4f*)(yrp + yr * 80 + yc * 8); p[0] = v4f{ya[0], ya[1], ya[2], ya[3]}; p[1] = v4f{ya[4], ya[5], ya[6], ya[7]}; }
    LDS_FENCE();
    TR(46);
    if (ln < 40) {
      v2f y = v2f{0.f, 0.f};
#pragma unroll
      for (int r = 0; r < 6; ++r) y += *(LAS v2f*)(yrp + r * 80 + ln * 2);
      const int cons = w * 8 + ln / 5, g5 = ln % 5;
      __builtin_amdgcn_raw_buffer_store_b128(v4u{__float_as_uint(y[0]), tagA, __float_as_uint(y[1]), tagB}, rws, WS_INBOX + (((b * 64 + cons) * 64 + j) * 5 + g5) * 16, 0, AUX_SC1);
    }
  }
  STAMP(9);
  TR(47);
  if (w == 0) {
    const int io = WS_INBOX + ((b * 64 + j) * 64 + ln) * 80;
    v4u g5[5];
    for (unsigned spins = 0;;) {
      bool ok = true;
#pragma unroll
      for (int e = 0; e < 5; ++e) g5[e] = poll16(rws, io + e * 16);
#pragma unroll
      for (int e = 0; e < 5; ++e) ok = ok & (g5[e][1] == tagA) & (g5[e][3] == tagB);
      if (__all(ok) || ++spins > SPIN_CAP) break;
      __builtin_amdgcn_s_sleep(1);
    }
    TR(48);
    const unsigned yv[10] = {g5[0][0], g5[0][2], g5[1][0], g5[1][2], g5[4][0], g5[2][0], g5[2][2], g5[3][0], g5[3][2], g5[4][2]};
    float b5[5];
#pragma unroll
    for (int i = 0; i < 5; ++i) { auto r = __builtin_amdgcn_permlane32_swap(yv[i], yv[5 + i], false, false); b5[i] = __uint_as_float(r[0]) + __uint_as_float(r[1]); }
    float z[3];
    { auto r = __builtin_amdgcn_permlane16_swap(__float_as_uint(b5[0]), __float_as_uint(b5[2]), false, false); z[0] = __uint_as_float(r[0]) + __uint_as_float(r[1]); }
    { auto r = __builtin_amdgcn_permlane16_swap(__float_as_uint(b5[1]), __float_as_uint(b5[3]), false, false); z[1] = __uint_as_float(r[0]) + __uint_as_float(r[1]); }
    { auto r = __builtin_amdgcn_permlane16_swap(__float_as_uint(b5[4]), __float_as_uint(b5[4]), false, false); z[2] = __uint_as_float(r[0]) + __uint_as_float(r[1]); }
#pragma unroll
    for (int i = 0; i < 3; ++i) { z[i] += dpp<XOR1>(z[i]); z[i] += dpp<XOR2>(z[i]); z[i] += dpp<XOR7>(z[i]); z[i] += dpp<XOR8>(z[i]); }
    auto r89 = __builtin_amdgcn_permlane32_swap(__float_as_uint(z[2]), __float_as_uint(z[2]), false, false);
    const unsigned o03 = cvtpk(z[0], z[1]), o4 = cvtpk(__uint_as_float(r89[0]), __uint_as_float(r89[1]));
    if ((ln & 15) == 0 || ln == 1) *(GAS unsigned*)(out + b * DM + j * 10 + (ln == 1 ? 8 : (ln >> 4) * 2)) = ln == 1 ? o4 : o03;
    if (b == 0 && j == 0 && ln == 0) __hip_atomic_store((GAS u64*)(ws + WS_EPOCH), ((u64)tagB << 32) | tagA, __ATOMIC_RELAXED, __HIP_MEMORY_SCOPE_AGENT);
  }
  TR(49);
}
}

extern "C" void kernel_launch(void* const* d_in, const int* in_sizes, int n_in, void* d_out, int out_size, void* d_ws, size_t ws_size,
                              hipStream_t stream) {
  (void)in_sizes; (void)n_in; (void)out_size; (void)ws_size;
  hipLaunchKernelGGL(fused_decode, dim3(NWG), dim3(NTHR), 0, stream, (const u16*)d_in[0], (const u16*)d_in[1], (const u16*)d_in[2], (const u16*)d_in[3],
                     (const u16*)d_in[4], (const u16*)d_in[6], (const u16*)d_in[7], (const u16*)d_in[8], (const u16*)d_in[9], (u16*)d_out, (unsigned char*)d_ws);
}
```

```cpp
#include <hip/hip_runtime.h>
#include <hip/hip_bf16.h>
#include <stdint.h>

#define GAS __attribute__((address_space(1)))
#define LAS __attribute__((address_space(3)))
typedef unsigned v4u __attribute__((ext_vector_type(4)));
typedef unsigned v2u __attribute__((ext_vector_type(2)));
typedef float v4f __attribute__((ext_vector_type(4)));
typedef float v2f __attribute__((ext_vector_type(2)));
typedef __bf16 bf16x2 __attribute__((ext_vector_type(2)));
typedef unsigned long long u64;
typedef unsigned short u16;

namespace {
constexpr int NB = 4, NH = 32, SK = 8192, DH = 192, DM = 640, FF = 1536;
constexpr int NWG = 256, NWAVE = 8, NTHR = NWAVE * 64;
constexpr int KEYS_WG = SK / 2;
constexpr int ROWB = DH * 2;
constexpr int TILEB = 8 * ROWB;
constexpr int GRPB = 2 * TILEB;
constexpr int KVB = KEYS_WG * ROWB;
constexpr int NGW = KVB / GRPB / NWAVE;
constexpr int RSRC3 = 0x00020000;
constexpr int AUX_NT = 2, AUX_SC1 = 16;
constexpr int OOB = 1 << 28;
constexpr float CS = 0.07216878364870322f * 1.4426950408889634f;
constexpr unsigned TAU16 = 174 * 16;
constexpr unsigned XHEAD16 = TAU16;
constexpr unsigned XPOLL16 = TAU16 * 3 / 10, XFFN16 = TAU16 * 3 / 10;
constexpr unsigned XDMA16 = TAU16 * 3 / 100;
constexpr unsigned CUSP16 = TAU16 / 8 * 60 / 100;
constexpr int T0OFF = 50, XGADJ = 12;

constexpr int WS_EPOCH = 0;
constexpr int WS_ML = 4096;
constexpr int WS_INBOX = 65536;
constexpr int WS_QX = WS_INBOX + NB * 64 * 64 * 80;
constexpr int WS_FX = WS_QX + NH * NB * 8 * 128 * 16;
constexpr int WS_BYTES = WS_FX + 8 * NB * 8 * 384 * 16;

constexpr int L_XS = 0;
constexpr int L_QP = 5120;
constexpr int QPS = 196;
constexpr int L_QB = L_QP + 8 * QPS * 4;
constexpr int L_OB = L_QB + 384;
constexpr int L_MISC = L_OB + 384;
constexpr int L_G = L_MISC + 256;
constexpr int L_YF = L_G + 3072;
constexpr int L_SC = L_YF + 8 * 480 * 4;
constexpr int L_FP2 = L_YF;
constexpr int L_FP = L_YF + 22 * 4 * 192 * 4;
constexpr int L_SCR = L_FP + 22 * 4 * 192 * 4;
constexpr int L_BYTES = L_SCR + 256;

constexpr unsigned SPIN_CAP = 1u << 17;

__device__ __forceinline__ float dot2(unsigned a, unsigned b, float c) {
  return __builtin_amdgcn_fdot2_f32_bf16(__builtin_bit_cast(bf16x2, a), __builtin_bit_cast(bf16x2, b), c, false);
}
template <int CTRL> __device__ __forceinline__ float dpp(float x) {
  return __builtin_bit_cast(float, __builtin_amdgcn_mov_dpp(__builtin_bit_cast(int, x), CTRL, 0xf, 0xf, true));
}
constexpr int XOR1 = 0xB1, XOR2 = 0x4E, XOR7 = 0x141, XOR8 = 0x128;
__device__ __forceinline__ float sum16_32(float x) {
  auto s = __builtin_amdgcn_permlane16_swap(__float_as_uint(x), __float_as_uint(x), false, false);
  x = __uint_as_float(s[0]) + __uint_as_float(s[1]);
  auto t = __builtin_amdgcn_permlane32_swap(__float_as_uint(x), __float_as_uint(x), false, false);
  return __uint_as_float(t[0]) + __uint_as_float(t[1]);
}
__device__ __forceinline__ float max16_32(float x) {
  auto s = __builtin_amdgcn_permlane16_swap(__float_as_uint(x), __float_as_uint(x), false, false);
  x = fmaxf(__uint_as_float(s[0]), __uint_as_float(s[1]));
  auto t = __builtin_amdgcn_permlane32_swap(__float_as_uint(x), __float_as_uint(x), false, false);
  return fmaxf(__uint_as_float(t[0]), __uint_as_float(t[1]));
}
__device__ __forceinline__ float oct_sum(float x) { x += dpp<XOR1>(x); x += dpp<XOR2>(x); x += dpp<XOR7>(x); return x; }
__device__ __forceinline__ float xoct_sum(float x) { x += dpp<XOR8>(x); return sum16_32(x); }
__device__ __forceinline__ float wave_sum(float x) { return xoct_sum(oct_sum(x)); }
__device__ __forceinline__ unsigned cvtpk(float lo, float hi) { unsigned r; asm("v_cvt_pk_bf16_f32 %0, %1, %2" : "=v"(r) : "v"(lo), "v"(hi)); return r; }
__device__ __forceinline__ float bflo(unsigned u) { return __uint_as_float(u << 16); }
__device__ __forceinline__ float bfhi(unsigned u) { return __uint_as_float(u & 0xffff0000u); }
__device__ __forceinline__ float ex2(float x) { return __builtin_amdgcn_exp2f(x); }
__device__ __forceinline__ __amdgpu_buffer_rsrc_t rsrc(const void* p, int bytes) { return __builtin_amdgcn_make_buffer_rsrc((void*)p, (short)0, bytes, RSRC3); }
template <int AUX> __device__ __forceinline__ v4u ld16(__amdgpu_buffer_rsrc_t r, int voff, int soff) { return __builtin_amdgcn_raw_buffer_load_b128(r, voff, soff, AUX); }
__device__ __forceinline__ unsigned now32() { return (unsigned)__builtin_amdgcn_s_memrealtime(); }
#define SCHED_FENCE() __builtin_amdgcn_sched_barrier(0)
#define LDS_FENCE() asm volatile("s_waitcnt lgkmcnt(0)" ::: "memory")
#define MEM_FENCE() asm volatile("" ::: "memory")
__device__ __forceinline__ v4u poll16(__amdgpu_buffer_rsrc_t r, int voff) { asm volatile("" : "+v"(voff) : : "memory"); return __builtin_amdgcn_raw_buffer_load_b128(r, voff, 0, AUX_SC1); }
__device__ __forceinline__ int fresh_lane() { int l; asm volatile("v_mbcnt_lo_u32_b32 %0, -1, 0\n\tv_mbcnt_hi_u32_b32 %0, -1, %0" : "=v"(l)); return l; }

__device__ __forceinline__ void dma16(__amdgpu_buffer_rsrc_t r, int voff, int soff, unsigned lds) {
  unsigned keep;
  asm volatile("s_mov_b32 %0, m0\n\ts_mov_b32 m0, %3\n\ts_nop 0\n\tbuffer_load_dwordx4 %1, %2, %4 offen lds\n\ts_mov_b32 m0, %0"
               : "=&s"(keep) : "v"(voff), "s"(r), "s"(lds), "s"(soff) : "memory");
}
__device__ __forceinline__ void dma4(__amdgpu_buffer_rsrc_t r, int voff, unsigned lds) {
  unsigned keep;
  asm volatile("s_mov_b32 %0, m0\n\ts_mov_b32 m0, %3\n\ts_nop 0\n\tbuffer_load_dword %1, %2, 0 offen sc1 lds\n\ts_mov_b32 m0, %0"
               : "=&s"(keep) : "v"(voff), "s"(r), "s"(lds) : "memory");
}
struct Grp { v4u r[2][3]; };

__global__ void __launch_bounds__(NTHR, 2)
fused_decode(const u16* __restrict__ x, const u16* __restrict__ Kc, const u16* __restrict__ Vc, const u16* __restrict__ wq,
             const u16* __restrict__ wo, const u16* __restrict__ w1, const u16* __restrict__ w3, const u16* __restrict__ w2,
             const u16* __restrict__ gw, u16* __restrict__ out, unsigned char* ws) {
  __shared__ __attribute__((aligned(16))) unsigned char lds_raw[L_BYTES];
  LAS unsigned char* L = (LAS unsigned char*)lds_raw;
  const int tid = threadIdx.x, lane = tid & 63, w = __builtin_amdgcn_readfirstlane(tid >> 6);
  const int k = lane >> 3, c = lane & 7;
  const int blk = blockIdx.x, xg = blk & 7, idx = blk >> 3;
  const int h = xg * 4 + (idx & 3), sh = idx >> 2, b = sh >> 1, s = sh & 1;
  const int j = h * 2 + s;
  const unsigned T0 = now32() + T0OFF + (3 - xg) * XGADJ;
  const __amdgpu_buffer_rsrc_t rws = rsrc(ws, WS_BYTES);
  const v4u tiny = __builtin_nontemporal_load((const GAS v4u*)(tid == 320 ? (const unsigned char*)ws + WS_EPOCH : (const unsigned char*)x + (tid < 320 ? tid : 0) * 16));
  const int qr = tid / 24, qc = tid - qr * 24;
  v4u wqv[4], fwa, fwb;
  {
    const __amdgpu_buffer_rsrc_t rq = rsrc(wq + h * DH, (DM * NH * DH - h * DH) * 2);
    const int oob = tid < 480 ? 0 : OOB;
#pragma unroll
    for (int e = 0; e < 4; ++e) wqv[e] = ld16<AUX_NT>(rq, (sh * 80 + qr * 4) * (NH * DH * 2) + qc * 16 + oob, e * (NH * DH * 2));
    const int row0 = (idx >> 1) * 40 + qr * 2;
    const __amdgpu_buffer_rsrc_t r13 = rsrc(((idx & 1) ? w3 : w1) + xg * 192, (DM * FF - xg * 192) * 2);
    fwa = ld16<AUX_NT>(r13, row0 * (FF * 2) + qc * 16 + oob, 0); fwb = ld16<AUX_NT>(r13, row0 * (FF * 2) + qc * 16 + oob, FF * 2);
  }
  const size_t kvoff = ((size_t)(b * NH + h) * SK + (size_t)s * KEYS_WG) * DH;
  const u16* Kb = Kc + kvoff; const u16* Vb = Vc + kvoff;
  if (w == 7) {
    const int o = lane == 0 ? WS_QX + ((h * 4) * 8 + sh) * 2048 : lane == 1 ? WS_QX + (h * 4 + 3) * 8 * 2048 + 16380
                : lane == 2 ? WS_FX + xg * 4 * 8 * 6144 : lane == 3 ? WS_FX + (xg * 4 + 3) * 8 * 6144 + 8 * 6144 - 4
                : lane == 4 ? WS_INBOX + b * 64 * 64 * 80 : lane == 5 ? WS_INBOX + (b + 1) * 64 * 64 * 80 - 4 : lane == 6 ? WS_ML : OOB;
    dma4(rws, o, L_SCR);
  }
  const int rho = (idx + xg * 4) & 31, sigma = (int)((blockIdx.x * 2246822519u) >> 29);
  const int kvo = ((w + sigma) & 7) * GRPB + k * ROWB + c * 16;
  unsigned tn = (T0 << 4) + w * (TAU16 / 8);
  bool streaming = false;
  const bool lane0 = lane == 0;
  auto pace = [&](unsigned step16) {
    unsigned now = now32() << 4;
    const int late = (int)(now - tn);
    if (late > (int)(2 * TAU16)) __builtin_amdgcn_s_setprio(3);
    else if (late > (int)TAU16) __builtin_amdgcn_s_setprio(2);
    else if (late > (int)(TAU16 / 4)) __builtin_amdgcn_s_setprio(1);
    else __builtin_amdgcn_s_setprio(0);
    while ((int)(now - tn) < 0) { __builtin_amdgcn_s_sleep(1); now = now32() << 4; }
    if (streaming) {
      LAS unsigned* cu = (LAS unsigned*)(L + L_MISC + 32);
      const unsigned base = T0 << 4;
      unsigned v = 0;
      if (lane0) v = __hip_atomic_fetch_add(cu, CUSP16, __ATOMIC_RELAXED, __HIP_MEMORY_SCOPE_WORKGROUP);
      v = __builtin_amdgcn_readfirstlane(v);
      now = (now32() << 4) - base;
      if ((int)(v - now) > 0) { while ((int)(((now32() << 4) - base) - v) < 0) __builtin_amdgcn_s_sleep(1); }
      else if (lane0) __hip_atomic_fetch_max(cu, now + CUSP16, __ATOMIC_RELAXED, __HIP_MEMORY_SCOPE_WORKGROUP);
    }
    tn += step16;
  };
  int wo_t = -1;
  __amdgpu_buffer_rsrc_t ro = rsrc(wo + (size_t)h * DH * DM, DH * DM * 2);
  int wo_vo = 0;
  auto issue = [&](v4u& r00, v4u& r01, v4u& r02, v4u& r10, v4u& r11, v4u& r12, int m) {
    pace(TAU16 + (wo_t >= 0 ? XDMA16 : 0));
    if (wo_t >= 0) dma16(ro, wo_vo, wo_t * 6 * (DM * 2), (unsigned)(L_FP + w * 8192 + (wo_t & 7) * 1024));
    const __amdgpu_buffer_rsrc_t r = rsrc(m < NGW ? Kb : Vb, KVB);
    const int so = ((m + rho) & (NGW - 1)) * (NWAVE * GRPB);
    r00 = ld16<AUX_NT>(r, kvo, so); r01 = ld16<AUX_NT>(r, kvo + 128, so); r02 = ld16<AUX_NT>(r, kvo + 256, so);
    r10 = ld16<AUX_NT>(r, kvo, so + TILEB); r11 = ld16<AUX_NT>(r, kvo + 128, so + TILEB); r12 = ld16<AUX_NT>(r, kvo + 256, so + TILEB);
    SCHED_FENCE();
  };
#define ISSUE(G, m) issue(G.r[0][0], G.r[0][1], G.r[0][2], G.r[1][0], G.r[1][1], G.r[1][2], m)
#define ISSUE_W(o, m) issue(W[o], W[o + 1], W[o + 2], W[o + 3], W[o + 4], W[o + 5], m)
  Grp A, B;
  v4u W[32];
  ISSUE(A, 0);
  ISSUE(B, 1);
  tn += XHEAD16;
  if (tid <= 320) *(LAS v4u*)(L + (tid < 320 ? L_XS + tid * 16 : L_MISC + 16)) = tiny;
  if (tid == 511) *(LAS unsigned*)(L + L_MISC + 32) = 0u;
  __syncthreads();
  const unsigned tagA = __builtin_amdgcn_readfirstlane(*(LAS unsigned*)(L + L_MISC + 16)) + 1u, tagB = __builtin_amdgcn_readfirstlane(*(LAS unsigned*)(L + L_MISC + 20)) + 1u;
  v2u xq[4]; unsigned fx4[4];
  { const int qrc = qr < 20 ? qr : 0;
#pragma unroll
    for (int e = 0; e < 4; ++e) { xq[e] = *(LAS v2u*)(L + L_XS + e * (DM * 2) + (sh * 80 + qrc * 4) * 2); fx4[e] = *(LAS unsigned*)(L + L_XS + e * (DM * 2) + ((idx >> 1) * 40 + qrc * 2) * 2); } }
  {
    unsigned lo01[4], hi01[4], lo23[4], hi23[4];
#pragma unroll
    for (int d = 0; d < 4; ++d) {
      lo01[d] = __builtin_amdgcn_perm(wqv[1][d], wqv[0][d], 0x05040100u); hi01[d] = __builtin_amdgcn_perm(wqv[1][d], wqv[0][d], 0x07060302u);
      lo23[d] = __builtin_amdgcn_perm(wqv[3][d], wqv[2][d], 0x05040100u); hi23[d] = __builtin_amdgcn_perm(wqv[3][d], wqv[2][d], 0x07060302u);
    }
#pragma unroll
    for (int bb = 0; bb < 4; ++bb) {
      float a[8];
#pragma unroll
      for (int d = 0; d < 4; ++d) { a[2 * d] = dot2(lo23[d], xq[bb][1], dot2(lo01[d], xq[bb][0], 0.f)); a[2 * d + 1] = dot2(hi23[d], xq[bb][1], dot2(hi01[d], xq[bb][0], 0.f)); }
      LAS v4f* p = (LAS v4f*)(L + L_FP + ((qr * 4 + bb) * 192 + qc * 8) * 4);
      p[0] = v4f{a[0], a[1], a[2], a[3]}; p[1] = v4f{a[4], a[5], a[6], a[7]};
    }
  }
  {
    unsigned lo[4], hi[4];
#pragma unroll
    for (int d = 0; d < 4; ++d) { lo[d] = __builtin_amdgcn_perm(fwb[d], fwa[d], 0x05040100u); hi[d] = __builtin_amdgcn_perm(fwb[d], fwa[d], 0x07060302u); }
#pragma unroll
    for (int bb = 0; bb < 4; ++bb) {
      LAS v4f* p = (LAS v4f*)(L + L_FP2 + ((qr * 4 + bb) * 192 + qc * 8) * 4);
      p[0] = v4f{dot2(lo[0], fx4[bb], 0.f), dot2(hi[0], fx4[bb], 0.f), dot2(lo[1], fx4[bb], 0.f), dot2(hi[1], fx4[bb], 0.f)};
      p[1] = v4f{dot2(lo[2], fx4[bb], 0.f), dot2(hi[2], fx4[bb], 0.f), dot2(lo[3], fx4[bb], 0.f), dot2(hi[3], fx4[bb], 0.f)};
    }
  }
  __syncthreads();
  if (tid < 384) {
    const int bb = tid / 96, p = tid - bb * 96;
    float a0 = 0.f, a1 = 0.f;
#pragma unroll
    for (int r = 0; r < 20; ++r) { const v2f v = *(LAS v2f*)(L + L_FP + ((r * 4 + bb) * 192 + p * 2) * 4); a0 += v[0]; a1 += v[1]; }
    __builtin_amdgcn_raw_buffer_store_b128(v4u{__float_as_uint(a0), tagA, __float_as_uint(a1), tagB}, rws, WS_QX + (((h * 4 + bb) * 8 + sh) * 128 + p) * 16, 0, AUX_SC1);
    a0 = 0.f; a1 = 0.f;
#pragma unroll
    for (int r = 0; r < 20; ++r) { const v2f v = *(LAS v2f*)(L + L_FP2 + ((r * 4 + bb) * 192 + p * 2) * 4); a0 += v[0]; a1 += v[1]; }
    __builtin_amdgcn_raw_buffer_store_b128(v4u{__float_as_uint(a0), tagA, __float_as_uint(a1), tagB}, rws,
                                           WS_FX + (((((xg * 4 + bb) * 8 + p / 12) * 2 + (idx & 1)) * 16 + (idx >> 1)) * 12 + p % 12) * 16, 0, AUX_SC1);
  }
  if (tid >= 384 && tid < 388)
    __builtin_amdgcn_raw_buffer_store_b128(v4u{T0, tagA, T0, tagB}, rws, WS_QX + (((h * 4 + (tid - 384)) * 8 + sh) * 128 + 96) * 16, 0, AUX_SC1);
  SCHED_FENCE();
  ISSUE_W(0, 2);
  ISSUE_W(6, 3);
  ISSUE_W(12, 4);
  if (w == 0) {
    __builtin_amdgcn_s_setprio(3);
    const int qo = WS_QX + (h * 4 + b) * 8 * 2048 + lane * 16;
    v4u ga[8], gb[8];
    for (unsigned spins = 0;;) {
#pragma unroll
      for (int p = 0; p < 8; ++p) { ga[p] = poll16(rws, qo + p * 2048); gb[p] = poll16(rws, qo + p * 2048 + 1024 + (lane <= 32 ? 0 : OOB)); }
      bool ok = true;
#pragma unroll
      for (int p = 0; p < 8; ++p) ok = ok & (ga[p][1] == tagA) & (ga[p][3] == tagB) & ((lane > 32) | ((gb[p][1] == tagA) & (gb[p][3] == tagB)));
      if (__all(ok) || ++spins > SPIN_CAP) break;
      __builtin_amdgcn_s_sleep(1);
    }
    float a0 = 0.f, a1 = 0.f, b0 = 0.f, b1 = 0.f;
#pragma unroll
    for (int p = 0; p < 8; ++p) { a0 += __uint_as_float(ga[p][0]); a1 += __uint_as_float(ga[p][2]); b0 += __uint_as_float(gb[p][0]); b1 += __uint_as_float(gb[p][2]); }
    *(LAS unsigned*)(L + L_QB + lane * 4) = cvtpk(a0, a1);
    if (lane < 32) *(LAS unsigned*)(L + L_QB + 256 + lane * 4) = cvtpk(b0, b1);
    unsigned tl = gb[0][0];
#pragma unroll
    for (int p = 1; p < 8; ++p) tl = (int)(gb[p][0] - tl) > 0 ? gb[p][0] : tl;
    if (lane == 32) *(LAS unsigned*)(L + L_MISC + 8) = tl;
  }
  tn += XPOLL16;
  ISSUE_W(18, 5);
  ISSUE_W(24, 6);
  __syncthreads();
  { const unsigned tl = __builtin_amdgcn_readfirstlane(*(LAS unsigned*)(L + L_MISC + 8)); if (tl - T0 < 1000u) tn += (tl - T0) << 4; }
  { const unsigned t = (now32() << 4) - 3 * TAU16; if ((int)(t - tn) > 0) tn = t; }
  streaming = true;
  v4u Q[3];
#pragma unroll
  for (int i = 0; i < 3; ++i) Q[i] = *(LAS v4u*)(L + L_QB + (c + 8 * i) * 16);

  float mx = -INFINITY;
  const bool oct0 = c == 0;
  const int scoff = L_SC + w * 2048 + k * 8;
  auto k_tile = [&](const v4u& r0, const v4u& r1, const v4u& r2) -> float {
    float a0 = dot2(r0[0], Q[0][0], 0.f), a1 = dot2(r1[0], Q[1][0], 0.f), a2 = dot2(r2[0], Q[2][0], 0.f);
#pragma unroll
    for (int d = 1; d < 4; ++d) { a0 = dot2(r0[d], Q[0][d], a0); a1 = dot2(r1[d], Q[1][d], a1); a2 = dot2(r2[d], Q[2][d], a2); }
    return (a0 + a1) + a2;
  };
  auto k_put = [&](float s0, float s1, int n) {
    s0 = oct_sum(s0); s1 = oct_sum(s1);
    mx = fmaxf(mx, fmaxf(s0, s1));
    if (oct0) *(LAS v2f*)(L + scoff + n * 64) = v2f{s0, s1};
  };
#define K_STEP(G, n, m) do { float s0_ = k_tile(G.r[0][0], G.r[0][1], G.r[0][2]), s1_ = k_tile(G.r[1][0], G.r[1][1], G.r[1][2]); \
    asm volatile("" : "+v"(s0_), "+v"(s1_) : : "memory"); ISSUE(G, m); k_put(s0_, s1_, n); } while (0)
  K_STEP(A, 0, 7);
#pragma unroll
  for (int g = 0; g < 5; ++g) k_put(k_tile(W[g * 6], W[g * 6 + 1], W[g * 6 + 2]), k_tile(W[g * 6 + 3], W[g * 6 + 4], W[g * 6 + 5]), 2 + g);
  K_STEP(B, 1, 8);
  {
    const int lane = fresh_lane(), tid2 = w * 64 + lane;
    const int yr = lane / 10, yc = lane - yr * 10;
    const int fxo = WS_FX + ((xg * 4 + b) * 8 + (j & 7)) * 6144 + tid2 * 16 + (tid2 < 384 ? 0 : OOB);
    v4u fx = poll16(rws, fxo);
    const __amdgpu_buffer_rsrc_t r2 = rsrc(w2, FF * DM * 2), rg = rsrc(gw, w == 0 ? DM * 2 : 0);
    const int vo2 = (j * 24 + yr) * (DM * 2) + w * 160 + yc * 16 + (lane < 60 ? 0 : OOB);
#pragma unroll
    for (int t = 0; t < 4; ++t) W[8 + t] = ld16<0>(r2, vo2, t * 6 * (DM * 2));
    const v4u gg0 = ld16<AUX_NT>(rg, lane * 16, 0), gg1 = ld16<AUX_NT>(rg, 1024 + lane * 16, 0);
    SCHED_FENCE();
    tn += XFFN16;
    K_STEP(A, 7, 9); K_STEP(B, 8, 10);
    for (unsigned spins = 0; !__all((tid2 >= 384) | ((fx[1] == tagA) & (fx[3] == tagB))); ) {
      if (++spins > SPIN_CAP) break;
      __builtin_amdgcn_s_sleep(2);
      fx = poll16(rws, fxo);
    }
    volatile LAS unsigned* flag = (volatile LAS unsigned*)(L + L_MISC + 192);
    if (tid2 < 384) *(LAS v2f*)(L + L_G + ((tid2 / 12) * 24 + (tid2 % 12) * 2) * 4) = v2f{__uint_as_float(fx[0]), __uint_as_float(fx[2])};
    float gate = 0.f;
    if (w == 0) {
      const v4u gx0 = *(LAS v4u*)(L + L_XS + b * (DM * 2) + lane * 16), gx1 = *(LAS v4u*)(L + L_XS + b * (DM * 2) + 1024 + (lane & 15) * 16);
      float d = 0.f;
#pragma unroll
      for (int e = 0; e < 4; ++e) { d = dot2(gx0[e], gg0[e], d); d = dot2(gx1[e], gg1[e], d); }
      gate = 1.f / (1.f + __expf(-wave_sum(d)));
      if (lane == 0) *(LAS float*)(L + L_MISC) = gate;
    }
    LDS_FENCE();
    if (lane == 0) flag[w] = tagA;
    K_STEP(A, 9, 11);
    if (w == 0) {
      for (unsigned spins = 0; !__all(lane >= 6 || flag[lane < 6 ? lane : 0] == tagA) && ++spins < (SPIN_CAP << 4); ) __builtin_amdgcn_s_sleep(1);
      MEM_FENCE();
      const LAS float* g = (const LAS float*)(L + L_G) + (lane < 24 ? lane : 0);
      float ga = 0.f, up = 0.f;
#pragma unroll
      for (int r = 0; r < 16; ++r) { ga += g[r * 24]; up += g[(16 + r) * 24]; }
      if (lane < 24) *(LAS float*)(L + L_MISC + 64 + lane * 4) = ga / (1.f + __expf(-ga)) * up * (1.f - gate);
      LDS_FENCE();
      if (lane == 0) flag[8] = tagA;
    }
    K_STEP(B, 10, 12);
    for (unsigned spins = 0; flag[8] != tagA && ++spins < (SPIN_CAP << 4); ) __builtin_amdgcn_s_sleep(1);
    MEM_FENCE();
    v2f ya[4];
#pragma unroll
    for (int e = 0; e < 4; ++e) ya[e] = v2f{0.f, 0.f};
#pragma unroll
    for (int t = 0; t < 4; ++t) {
      const float hv = *(LAS float*)(L + L_MISC + 64 + (t * 6 + (yr < 6 ? yr : 0)) * 4);
#pragma unroll
      for (int d = 0; d < 4; ++d) ya[d] = __builtin_elementwise_fma(v2f{hv, hv}, v2f{bflo(W[8 + t][d]), bfhi(W[8 + t][d])}, ya[d]);
    }
    if (lane < 60) {
      LAS v4f* p = (LAS v4f*)(L + L_YF + (w * 480 + yr * 80 + yc * 8) * 4);
      p[0] = v4f{ya[0][0], ya[0][1], ya[1][0], ya[1][1]}; p[1] = v4f{ya[2][0], ya[2][1], ya[3][0], ya[3][1]};
    }
    wo_vo = yr * (DM * 2) + w * 160 + yc * 16 + (lane < 60 ? 0 : OOB);
  }
  MEM_FENCE();
#define WO_COPY(t0) do { asm volatile("s_waitcnt vmcnt(12)" ::: "memory"); const int ln_ = fresh_lane(); \
    _Pragma("unroll") for (int t = 0; t < 8; t += 2) {     \
      const v4u a_ = *(LAS v4u*)(L + L_FP + w * 8192 + t * 1024 + ln_ * 16), b_ = *(LAS v4u*)(L + L_FP + w * 8192 + (t + 1) * 1024 + ln_ * 16); \
      _Pragma("unroll") for (int d = 0; d < 4; ++d) { W[t0 + t][d] = __builtin_amdgcn_perm(b_[d], a_[d], 0x05040100u); W[t0 + t + 1][d] = __builtin_amdgcn_perm(b_[d], a_[d], 0x07060302u); } } \
    asm volatile("" : "+v"(W[t0]), "+v"(W[t0 + 1]), "+v"(W[t0 + 2]), "+v"(W[t0 + 3]), "+v"(W[t0 + 4]), "+v"(W[t0 + 5]), "+v"(W[t0 + 6]), "+v"(W[t0 + 7]) : : "memory"); } while (0)
  int n;
#pragma unroll 1
  for (n = 11; n < 21; n += 2) {
    wo_t = n < 19 ? n - 11 : -1; K_STEP(A, n, n + 2);
    wo_t = n < 19 ? n - 10 : -1; K_STEP(B, n + 1, n + 3);
  }
  WO_COPY(0);
#pragma unroll 1
  for (; n < 31; n += 2) {
    wo_t = n < 29 ? n - 13 : -1; K_STEP(A, n, n + 2);
    wo_t = n < 29 ? n - 12 : -1; K_STEP(B, n + 1, n + 3);
  }
  WO_COPY(8);
  wo_t = -1;
  K_STEP(A, 31, 33);
  mx = fmaxf(mx, dpp<XOR8>(mx)); mx = max16_32(mx);
  const float mw = mx * CS;
  LDS_FENCE();
  {
    const int lane = fresh_lane();
    LAS v4f* p = (LAS v4f*)(L + L_SC + w * 2048 + lane * 32);
    v4f a = p[0], bb = p[1];
    float l = 0.f;
#pragma unroll
    for (int e = 0; e < 4; ++e) { a[e] = ex2(fmaf(a[e], CS, -mw)); bb[e] = ex2(fmaf(bb[e], CS, -mw)); l += a[e] + bb[e]; }
    p[0] = a; p[1] = bb;
    l = wave_sum(l);
    if (lane == 0) __builtin_amdgcn_raw_buffer_store_b128(v4u{__float_as_uint(mw), tagA, __float_as_uint(l), tagB}, rws, WS_ML + (((b * NH + h) * 2 + s) * 8 + w) * 16, 0, AUX_SC1);
  }
  LDS_FENCE();

  v2f acc2[12];
#pragma unroll
  for (int e = 0; e < 12; ++e) acc2[e] = v2f{0.f, 0.f};
#define PIN_ACC() asm volatile("" : "+v"(acc2[0]), "+v"(acc2[1]), "+v"(acc2[2]), "+v"(acc2[3]), "+v"(acc2[4]), "+v"(acc2[5]), "+v"(acc2[6]), "+v"(acc2[7]), \
                                    "+v"(acc2[8]), "+v"(acc2[9]), "+v"(acc2[10]), "+v"(acc2[11]) : : "memory")
  auto v_tile = [&](const v4u& r0, const v4u& r1, const v4u& r2, float p) {
    const v2f pp = v2f{p, p};
#pragma unroll
    for (int d = 0; d < 4; ++d) {
      acc2[d] = __builtin_elementwise_fma(pp, v2f{bflo(r0[d]), bfhi(r0[d])}, acc2[d]);
      acc2[4 + d] = __builtin_elementwise_fma(pp, v2f{bflo(r1[d]), bfhi(r1[d])}, acc2[4 + d]);
      acc2[8 + d] = __builtin_elementwise_fma(pp, v2f{bflo(r2[d]), bfhi(r2[d])}, acc2[8 + d]);
    }
    PIN_ACC();
  };
#define V_USE(G, n) do { const v2f p2_ = *(LAS v2f*)(L + scoff + (n) * 64); v_tile(G.r[0][0], G.r[0][1], G.r[0][2], p2_[0]); v_tile(G.r[1][0], G.r[1][1], G.r[1][2], p2_[1]); } while (0)
#pragma unroll 1
  for (n = 0; n < 10; n += 2) {
    V_USE(B, n); wo_t = n < 8 ? n + 16 : -1; ISSUE(B, NGW + n + 2);
    V_USE(A, n + 1); wo_t = n < 8 ? n + 17 : -1; ISSUE(A, NGW + n + 3);
  }
  WO_COPY(16);
#pragma unroll 1
  for (; n < 20; n += 2) {
    V_USE(B, n); wo_t = n < 18 ? n + 14 : -1; ISSUE(B, NGW + n + 2);
    V_USE(A, n + 1); wo_t = n < 18 ? n + 15 : -1; ISSUE(A, NGW + n + 3);
  }
  WO_COPY(24);
  wo_t = -1;
  const int mloff = WS_ML + ((b * NH + h) * 16 + (fresh_lane() & 15)) * 16;
  v4u ml = poll16(rws, mloff);
  V_USE(B, n); ISSUE(B, NGW + n + 2); V_USE(A, n + 1); ISSUE(A, NGW + n + 3);
  for (unsigned spins = 0; !__all((ml[1] == tagA) & (ml[3] == tagB)); ) {
    if (++spins > SPIN_CAP) break;
    __builtin_amdgcn_s_sleep(2);
    ml = poll16(rws, mloff);
  }
  float fw;
  {
    const float m_i = __uint_as_float(ml[0]), l_i = __uint_as_float(ml[2]);
    float M = m_i;
    M = fmaxf(M, dpp<XOR1>(M)); M = fmaxf(M, dpp<XOR2>(M)); M = fmaxf(M, dpp<XOR7>(M)); M = fmaxf(M, dpp<XOR8>(M));
    float Ls = l_i * ex2(m_i - M);
    Ls = oct_sum(Ls); Ls += dpp<XOR8>(Ls);
    fw = __uint_as_float(__builtin_amdgcn_readfirstlane(__float_as_uint(ex2(mw - M) / Ls * *(LAS float*)(L + L_MISC))));
  }
#pragma unroll 1
  for (n += 2; n < NGW - 2; n += 2) { V_USE(B, n); ISSUE(B, NGW + n + 2); V_USE(A, n + 1); ISSUE(A, NGW + n + 3); }
  V_USE(B, NGW - 2);
  V_USE(A, NGW - 1);
  {
    float b12[12], c6[6];
#pragma unroll
    for (int i = 0; i < 12; ++i) {
      auto r = __builtin_amdgcn_permlane32_swap(__float_as_uint(acc2[i >> 1][i & 1]), __float_as_uint(acc2[6 + (i >> 1)][i & 1]), false, false);
      b12[i] = __uint_as_float(r[0]) + __uint_as_float(r[1]);
    }
#pragma unroll
    for (int i = 0; i < 6; ++i) {
      auto r = __builtin_amdgcn_permlane16_swap(__float_as_uint(b12[i]), __float_as_uint(b12[i + 6]), false, false);
      c6[i] = __uint_as_float(r[0]) + __uint_as_float(r[1]);
    }
#pragma unroll
    for (int i = 0; i < 6; ++i) c6[i] = (c6[i] + dpp<XOR8>(c6[i])) * fw;
    const int ln = fresh_lane();
    if ((ln & 8) == 0) {
      const int v0 = (ln >> 4) * 6;
#pragma unroll
      for (int p = 0; p < 3; ++p) { const int v = v0 + 2 * p; *(LAS v2f*)(L + L_QP + (w * QPS + (v >> 3) * 64 + (ln & 7) * 8 + (v & 7)) * 4) = v2f{c6[2 * p], c6[2 * p + 1]}; }
    }
  }
  const int ln = fresh_lane();
  const int yr = ln < 60 ? ln / 10 : 0, yc = ln - (ln / 10) * 10;
  float ya[8];
  { const LAS v4f* p = (const LAS v4f*)(L + L_YF + (w * 480 + yr * 80 + yc * 8) * 4); const v4f a = p[0], bb = p[1];
#pragma unroll
    for (int e = 0; e < 4; ++e) { ya[e] = a[e]; ya[4 + e] = bb[e]; } }
  __syncthreads();
  if (w < 6) {
    const int pi = w * 16 + (ln >> 2), r = pi >> 4, u = pi & 15, qq = ln & 3;
    const LAS float* p = (const LAS float*)(L + L_QP) + 2 * qq * QPS + u * 12 + r;
    float a0 = p[0] + p[QPS], a1 = p[6] + p[QPS + 6];
    a0 += dpp<XOR1>(a0); a1 += dpp<XOR1>(a1); a0 += dpp<XOR2>(a0); a1 += dpp<XOR2>(a1);
    if (qq == 0) *(LAS unsigned*)(L + L_OB + pi * 4) = cvtpk(a0, a1);
  }
  __syncthreads();
  {
    LAS float* yrp = (LAS float*)(L + L_FP) + w * 480;
    const LAS v4u* op = (const LAS v4u*)(L + L_OB + yr * 64);
#pragma unroll
    for (int u4 = 0; u4 < 4; ++u4) {
      const v4u o4 = op[u4];
#pragma unroll
      for (int e = 0; e < 4; ++e) {
        const int u = u4 * 4 + e;
#pragma unroll
        for (int d = 0; d < 4; ++d) { ya[2 * d] = dot2(W[2 * u][d], o4[e], ya[2 * d]); ya[2 * d + 1] = dot2(W[2 * u + 1][d], o4[e], ya[2 * d + 1]); }
      }
    }
    asm volatile("" : "+v"(ya[0]), "+v"(ya[1]), "+v"(ya[2]), "+v"(ya[3]), "+v"(ya[4]), "+v"(ya[5]), "+v"(ya[6]), "+v"(ya[7]));
    if (ln < 60) { LAS v4f* p = (LAS v4f*)(yrp + yr * 80 + yc * 8); p[0] = v4f{ya[0], ya[1], ya[2], ya[3]}; p[1] = v4f{ya[4], ya[5], ya[6], ya[7]}; }
    LDS_FENCE();
    if (ln < 40) {
      v2f y = v2f{0.f, 0.f};
#pragma unroll
      for (int r = 0; r < 6; ++r) y += *(LAS v2f*)(yrp + r * 80 + ln * 2);
      const int cons = w * 8 + ln / 5, g5 = ln % 5;
      __builtin_amdgcn_raw_buffer_store_b128(v4u{__float_as_uint(y[0]), tagA, __float_as_uint(y[1]), tagB}, rws, WS_INBOX + (((b * 64 + cons) * 64 + j) * 5 + g5) * 16, 0, AUX_SC1);
    }
  }
  if (w == 0) {
    const int io = WS_INBOX + ((b * 64 + j) * 64 + ln) * 80;
    v4u g5[5];
    for (unsigned spins = 0;;) {
      bool ok = true;
#pragma unroll
      for (int e = 0; e < 5; ++e) g5[e] = poll16(rws, io + e * 16);
#pragma unroll
      for (int e = 0; e < 5; ++e) ok = ok & (g5[e][1] == tagA) & (g5[e][3] == tagB);
      if (__all(ok) || ++spins > SPIN_CAP) break;
      __builtin_amdgcn_s_sleep(1);
    }
    const unsigned yv[10] = {g5[0][0], g5[0][2], g5[1][0], g5[1][2], g5[4][0], g5[2][0], g5[2][2], g5[3][0], g5[3][2], g5[4][2]};
    float b5[5];
#pragma unroll
    for (int i = 0; i < 5; ++i) { auto r = __builtin_amdgcn_permlane32_swap(yv[i], yv[5 + i], false, false); b5[i] = __uint_as_float(r[0]) + __uint_as_float(r[1]); }
    float z[3];
    { auto r = __builtin_amdgcn_permlane16_swap(__float_as_uint(b5[0]), __float_as_uint(b5[2]), false, false); z[0] = __uint_as_float(r[0]) + __uint_as_float(r[1]); }
    { auto r = __builtin_amdgcn_permlane16_swap(__float_as_uint(b5[1]), __float_as_uint(b5[3]), false, false); z[1] = __uint_as_float(r[0]) + __uint_as_float(r[1]); }
    { auto r = __builtin_amdgcn_permlane16_swap(__float_as_uint(b5[4]), __float_as_uint(b5[4]), false, false); z[2] = __uint_as_float(r[0]) + __uint_as_float(r[1]); }
#pragma unroll
    for (int i = 0; i < 3; ++i) { z[i] += dpp<XOR1>(z[i]); z[i] += dpp<XOR2>(z[i]); z[i] += dpp<XOR7>(z[i]); z[i] += dpp<XOR8>(z[i]); }
    auto r89 = __builtin_amdgcn_permlane32_swap(__float_as_uint(z[2]), __float_as_uint(z[2]), false, false);
    const unsigned o03 = cvtpk(z[0], z[1]), o4 = cvtpk(__uint_as_float(r89[0]), __uint_as_float(r89[1]));
    if ((ln & 15) == 0 || ln == 1) *(GAS unsigned*)(out + b * DM + j * 10 + (ln == 1 ? 8 : (ln >> 4) * 2)) = ln == 1 ? o4 : o03;
    if (b == 0 && j == 0 && ln == 0) __hip_atomic_store((GAS u64*)(ws + WS_EPOCH), ((u64)tagB << 32) | tagA, __ATOMIC_RELAXED, __HIP_MEMORY_SCOPE_AGENT);
  }
}
}

extern "C" void kernel_launch(void* const* d_in, const int* in_sizes, int n_in, void* d_out, int out_size, void* d_ws, size_t ws_size,
                              hipStream_t stream) {
  (void)in_sizes; (void)n_in; (void)out_size; (void)ws_size;
  hipLaunchKernelGGL(fused_decode, dim3(NWG), dim3(NTHR), 0, stream, (const u16*)d_in[0], (const u16*)d_in[1], (const u16*)d_in[2], (const u16*)d_in[3],
                     (const u16*)d_in[4], (const u16*)d_in[6], (const u16*)d_in[7], (const u16*)d_in[8], (const u16*)d_in[9], (u16*)d_out, (unsigned char*)d_ws);
}
```

```cpp
#include <hip/hip_runtime.h>
#include <hip/hip_bf16.h>
#include <stdint.h>

#define GAS __attribute__((address_space(1)))
#define LAS __attribute__((address_space(3)))
typedef unsigned v4u __attribute__((ext_vector_type(4)));
typedef unsigned v2u __attribute__((ext_vector_type(2)));
typedef float v4f __attribute__((ext_vector_type(4)));
typedef float v2f __attribute__((ext_vector_type(2)));
typedef __bf16 bf16x2 __attribute__((ext_vector_type(2)));
typedef unsigned long long u64;
typedef unsigned short u16;

namespace {
constexpr int NB = 4, NH = 32, SK = 8192, DH = 192, DM = 640, FF = 1536;
constexpr int NWG = 256, NWAVE = 8, NTHR = NWAVE * 64;
constexpr int KEYS_WG = SK / 2;
constexpr int ROWB = DH * 2;
constexpr int TILEB = 8 * ROWB;
constexpr int GRPB = 2 * TILEB;
constexpr int KVB = KEYS_WG * ROWB;
constexpr int NGW = KVB / GRPB / NWAVE;
constexpr int RSRC3 = 0x00020000;
constexpr int AUX_NT = 2, AUX_SC1 = 16;
constexpr int OOB = 1 << 28;
constexpr float CS = 0.07216878364870322f * 1.4426950408889634f;
constexpr unsigned TAU16 = 174 * 16;
constexpr unsigned XHEAD16 = TAU16;
constexpr unsigned XPOLL16 = TAU16 * 3 / 10, XFFN16 = TAU16 * 1 / 10;
constexpr unsigned XDMA16 = TAU16 * 3 / 100;
constexpr unsigned CUSP16 = TAU16 / 8 * 60 / 100;
constexpr int T0OFF = 30, XGADJ = 12;

constexpr int WS_EPOCH = 0;
constexpr int WS_ML = 4096;
constexpr int WS_INBOX = 65536;
constexpr int WS_QX = WS_INBOX + NB * 64 * 64 * 80;
constexpr int WS_FX = WS_QX + NH * NB * 8 * 128 * 16;
constexpr int WS_BYTES = WS_FX + 8 * NB * 8 * 384 * 16;

constexpr int L_XS = 0;
constexpr int L_QP = 5120;
constexpr int QPS = 196;
constexpr int L_QB = L_QP + 8 * QPS * 4;
constexpr int L_OB = L_QB + 384;
constexpr int L_MISC = L_OB + 384;
constexpr int L_G = L_MISC + 256;
constexpr int L_YF = L_G + 3072;
constexpr int L_SC = L_YF + 8 * 480 * 4;
constexpr int L_FP2 = L_YF;
constexpr int L_FP = L_YF + 22 * 4 * 192 * 4;
constexpr int L_SCR = L_FP + 22 * 4 * 192 * 4;
constexpr int L_BYTES = L_SCR + 256;

constexpr unsigned SPIN_CAP = 1u << 17;

__device__ __forceinline__ float dot2(unsigned a, unsigned b, float c) {
  return __builtin_amdgcn_fdot2_f32_bf16(__builtin_bit_cast(bf16x2, a), __builtin_bit_cast(bf16x2, b), c, false);
}
template <int CTRL> __device__ __forceinline__ float dpp(float x) {
  return __builtin_bit_cast(float, __builtin_amdgcn_mov_dpp(__builtin_bit_cast(int, x), CTRL, 0xf, 0xf, true));
}
constexpr int XOR1 = 0xB1, XOR2 = 0x4E, XOR7 = 0x141, XOR8 = 0x128;
__device__ __forceinline__ float sum16_32(float x) {
  auto s = __builtin_amdgcn_permlane16_swap(__float_as_uint(x), __float_as_uint(x), false, false);
  x = __uint_as_float(s[0]) + __uint_as_float(s[1]);
  auto t = __builtin_amdgcn_permlane32_swap(__float_as_uint(x), __float_as_uint(x), false, false);
  return __uint_as_float(t[0]) + __uint_as_float(t[1]);
}
__device__ __forceinline__ float max16_32(float x) {
  auto s = __builtin_amdgcn_permlane16_swap(__float_as_uint(x), __float_as_uint(x), false, false);
  x = fmaxf(__uint_as_float(s[0]), __uint_as_float(s[1]));
  auto t = __builtin_amdgcn_permlane32_swap(__float_as_uint(x), __float_as_uint(x), false, false);
  return fmaxf(__uint_as_float(t[0]), __uint_as_float(t[1]));
}
__device__ __forceinline__ float oct_sum(float x) { x += dpp<XOR1>(x); x += dpp<XOR2>(x); x += dpp<XOR7>(x); return x; }
__device__ __forceinline__ float xoct_sum(float x) { x += dpp<XOR8>(x); return sum16_32(x); }
__device__ __forceinline__ float wave_sum(float x) { return xoct_sum(oct_sum(x)); }
__device__ __forceinline__ unsigned cvtpk(float lo, float hi) { unsigned r; asm("v_cvt_pk_bf16_f32 %0, %1, %2" : "=v"(r) : "v"(lo), "v"(hi)); return r; }
__device__ __forceinline__ float bflo(unsigned u) { return __uint_as_float(u << 16); }
__device__ __forceinline__ float bfhi(unsigned u) { return __uint_as_float(u & 0xffff0000u); }
__device__ __forceinline__ float ex2(float x) { return __builtin_amdgcn_exp2f(x); }
__device__ __forceinline__ __amdgpu_buffer_rsrc_t rsrc(const void* p, int bytes) { return __builtin_amdgcn_make_buffer_rsrc((void*)p, (short)0, bytes, RSRC3); }
template <int AUX> __device__ __forceinline__ v4u ld16(__amdgpu_buffer_rsrc_t r, int voff, int soff) { return __builtin_amdgcn_raw_buffer_load_b128(r, voff, soff, AUX); }
__device__ __forceinline__ unsigned now32() { return (unsigned)__builtin_amdgcn_s_memrealtime(); }
#define SCHED_FENCE() __builtin_amdgcn_sched_barrier(0)
#define LDS_FENCE() asm volatile("s_waitcnt lgkmcnt(0)" ::: "memory")
#define MEM_FENCE() asm volatile("" ::: "memory")
__device__ __forceinline__ v4u poll16(__amdgpu_buffer_rsrc_t r, int voff) { asm volatile("" : "+v"(voff) : : "memory"); return __builtin_amdgcn_raw_buffer_load_b128(r, voff, 0, AUX_SC1); }
__device__ __forceinline__ int fresh_lane() { int l; asm volatile("v_mbcnt_lo_u32_b32 %0, -1, 0\n\tv_mbcnt_hi_u32_b32 %0, -1, %0" : "=v"(l)); return l; }

__device__ __forceinline__ void dma16(__amdgpu_buffer_rsrc_t r, int voff, int soff, unsigned lds) {
  unsigned keep;
  asm volatile("s_mov_b32 %0, m0\n\ts_mov_b32 m0, %3\n\ts_nop 0\n\tbuffer_load_dwordx4 %1, %2, %4 offen lds\n\ts_mov_b32 m0, %0"
               : "=&s"(keep) : "v"(voff), "s"(r), "s"(lds), "s"(soff) : "memory");
}
__device__ __forceinline__ void dma4(__amdgpu_buffer_rsrc_t r, int voff, unsigned lds) {
  unsigned keep;
  asm volatile("s_mov_b32 %0, m0\n\ts_mov_b32 m0, %3\n\ts_nop 0\n\tbuffer_load_dword %1, %2, 0 offen sc1 lds\n\ts_mov_b32 m0, %0"
               : "=&s"(keep) : "v"(voff), "s"(r), "s"(lds) : "memory");
}
struct Grp { v4u r[2][3]; };

__global__ void __launch_bounds__(NTHR, 2)
fused_decode(const u16* __restrict__ x, const u16* __restrict__ Kc, const u16* __restrict__ Vc, const u16* __restrict__ wq,
             const u16* __restrict__ wo, const u16* __restrict__ w1, const u16* __restrict__ w3, const u16* __restrict__ w2,
             const u16* __restrict__ gw, u16* __restrict__ out, unsigned char* ws) {
  __shared__ __attribute__((aligned(16))) unsigned char lds_raw[L_BYTES];
  LAS unsigned char* L = (LAS unsigned char*)lds_raw;
  const int tid = threadIdx.x, lane = tid & 63, w = __builtin_amdgcn_readfirstlane(tid >> 6);
  const int k = lane >> 3, c = lane & 7;
  const int blk = blockIdx.x, xg = blk & 7, idx = blk >> 3;
  const int h = xg * 4 + (idx & 3), sh = idx >> 2, b = sh >> 1, s = sh & 1;
  const int j = h * 2 + s;
  const unsigned T0 = now32() + T0OFF + (3 - xg) * XGADJ;
  const __amdgpu_buffer_rsrc_t rws = rsrc(ws, WS_BYTES);
  const v4u tiny = __builtin_nontemporal_load((const GAS v4u*)(tid == 320 ? (const unsigned char*)ws + WS_EPOCH : (const unsigned char*)x + (tid < 320 ? tid : 0) * 16));
  const int qr = tid / 24, qc = tid - qr * 24;
  v4u wqv[4], fwa, fwb;
  {
    const __amdgpu_buffer_rsrc_t rq = rsrc(wq + h * DH, (DM * NH * DH - h * DH) * 2);
    const int oob = tid < 480 ? 0 : OOB;
#pragma unroll
    for (int e = 0; e < 4; ++e) wqv[e] = ld16<AUX_NT>(rq, (sh * 80 + qr * 4) * (NH * DH * 2) + qc * 16 + oob, e * (NH * DH * 2));
    const int row0 = (idx >> 1) * 40 + qr * 2;
    const __amdgpu_buffer_rsrc_t r13 = rsrc(((idx & 1) ? w3 : w1) + xg * 192, (DM * FF - xg * 192) * 2);
    fwa = ld16<AUX_NT>(r13, row0 * (FF * 2) + qc * 16 + oob, 0); fwb = ld16<AUX_NT>(r13, row0 * (FF * 2) + qc * 16 + oob, FF * 2);
  }
  const size_t kvoff = ((size_t)(b * NH + h) * SK + (size_t)s * KEYS_WG) * DH;
  const u16* Kb = Kc + kvoff; const u16* Vb = Vc + kvoff;
  if (w == 7) {
    const int o = lane == 0 ? WS_QX + ((h * 4) * 8 + sh) * 2048 : lane == 1 ? WS_QX + (h * 4 + 3) * 8 * 2048 + 16380
                : lane == 2 ? WS_FX + xg * 4 * 8 * 6144 : lane == 3 ? WS_FX + (xg * 4 + 3) * 8 * 6144 + 8 * 6144 - 4
                : lane == 4 ? WS_INBOX + b * 64 * 64 * 80 : lane == 5 ? WS_INBOX + (b + 1) * 64 * 64 * 80 - 4 : lane == 6 ? WS_ML : OOB;
    dma4(rws, o, L_SCR);
  }
  const int rho = (idx + xg * 4) & 31, sigma = (int)((blockIdx.x * 2246822519u) >> 29);
  const int kvo = ((w + sigma) & 7) * GRPB + k * ROWB + c * 16;
  unsigned tn = (T0 << 4) + w * (TAU16 / 8);
  bool streaming = false;
  const bool lane0 = lane == 0;
  auto pace = [&](unsigned step16) {
    unsigned now = now32() << 4;
    const int late = (int)(now - tn);
    if (late > (int)(2 * TAU16)) __builtin_amdgcn_s_setprio(3);
    else if (late > (int)TAU16) __builtin_amdgcn_s_setprio(2);
    else if (late > (int)(TAU16 / 4)) __builtin_amdgcn_s_setprio(1);
    else __builtin_amdgcn_s_setprio(0);
    while ((int)(now - tn) < 0) { __builtin_amdgcn_s_sleep(1); now = now32() << 4; }
    if (streaming) {
      LAS unsigned* cu = (LAS unsigned*)(L + L_MISC + 32);
      const unsigned base = T0 << 4;
      unsigned v = 0;
      if (lane0) v = __hip_atomic_fetch_add(cu, CUSP16, __ATOMIC_RELAXED, __HIP_MEMORY_SCOPE_WORKGROUP);
      v = __builtin_amdgcn_readfirstlane(v);
      now = (now32() << 4) - base;
      if ((int)(v - now) > 0) { while ((int)(((now32() << 4) - base) - v) < 0) __builtin_amdgcn_s_sleep(1); }
      else if (lane0) __hip_atomic_fetch_max(cu, now + CUSP16, __ATOMIC_RELAXED, __HIP_MEMORY_SCOPE_WORKGROUP);
    }
    tn += step16;
  };
  int wo_t = -1;
  __amdgpu_buffer_rsrc_t ro = rsrc(wo + (size_t)h * DH * DM, DH * DM * 2);
  int wo_vo = 0;
  auto issue = [&](v4u& r00, v4u& r01, v4u& r02, v4u& r10, v4u& r11, v4u& r12, int m) {
    pace(TAU16 + (wo_t >= 0 ? XDMA16 : 0));
    if (wo_t >= 0) dma16(ro, wo_vo, wo_t * 6 * (DM * 2), (unsigned)(L_FP + w * 8192 + (wo_t & 7) * 1024));
    const __amdgpu_buffer_rsrc_t r = rsrc(m < NGW ? Kb : Vb, KVB);
    const int so = ((m + rho) & (NGW - 1)) * (NWAVE * GRPB);
    r00 = ld16<AUX_NT>(r, kvo, so); r01 = ld16<AUX_NT>(r, kvo + 128, so); r02 = ld16<AUX_NT>(r, kvo + 256, so);
    r10 = ld16<AUX_NT>(r, kvo, so + TILEB); r11 = ld16<AUX_NT>(r, kvo + 128, so + TILEB); r12 = ld16<AUX_NT>(r, kvo + 256, so + TILEB);
    SCHED_FENCE();
  };
#define ISSUE(G, m) issue(G.r[0][0], G.r[0][1], G.r[0][2], G.r[1][0], G.r[1][1], G.r[1][2], m)
#define ISSUE_W(o, m) issue(W[o], W[o + 1], W[o + 2], W[o + 3], W[o + 4], W[o + 5], m)
  Grp A, B;
  v4u W[32];
  ISSUE(A, 0);
  ISSUE(B, 1);
  tn += XHEAD16;
  if (tid <= 320) *(LAS v4u*)(L + (tid < 320 ? L_XS + tid * 16 : L_MISC + 16)) = tiny;
  if (tid == 511) *(LAS unsigned*)(L + L_MISC + 32) = 0u;
  __syncthreads();
  const unsigned tagA = __builtin_amdgcn_readfirstlane(*(LAS unsigned*)(L + L_MISC + 16)) + 1u, tagB = __builtin_amdgcn_readfirstlane(*(LAS unsigned*)(L + L_MISC + 20)) + 1u;
  v2u xq[4]; unsigned fx4[4];
  { const int qrc = qr < 20 ? qr : 0;
#pragma unroll
    for (int e = 0; e < 4; ++e) { xq[e] = *(LAS v2u*)(L + L_XS + e * (DM * 2) + (sh * 80 + qrc * 4) * 2); fx4[e] = *(LAS unsigned*)(L + L_XS + e * (DM * 2) + ((idx >> 1) * 40 + qrc * 2) * 2); } }
  {
    unsigned lo01[4], hi01[4], lo23[4], hi23[4];
#pragma unroll
    for (int d = 0; d < 4; ++d) {
      lo01[d] = __builtin_amdgcn_perm(wqv[1][d], wqv[0][d], 0x05040100u); hi01[d] = __builtin_amdgcn_perm(wqv[1][d], wqv[0][d], 0x07060302u);
      lo23[d] = __builtin_amdgcn_perm(wqv[3][d], wqv[2][d], 0x05040100u); hi23[d] = __builtin_amdgcn_perm(wqv[3][d], wqv[2][d], 0x07060302u);
    }
#pragma unroll
    for (int bb = 0; bb < 4; ++bb) {
      float a[8];
#pragma unroll
      for (int d = 0; d < 4; ++d) { a[2 * d] = dot2(lo23[d], xq[bb][1], dot2(lo01[d], xq[bb][0], 0.f)); a[2 * d + 1] = dot2(hi23[d], xq[bb][1], dot2(hi01[d], xq[bb][0], 0.f)); }
      LAS v4f* p = (LAS v4f*)(L + L_FP + ((qr * 4 + bb) * 192 + qc * 8) * 4);
      p[0] = v4f{a[0], a[1], a[2], a[3]}; p[1] = v4f{a[4], a[5], a[6], a[7]};
    }
  }
  {
    unsigned lo[4], hi[4];
#pragma unroll
    for (int d = 0; d < 4; ++d) { lo[d] = __builtin_amdgcn_perm(fwb[d], fwa[d], 0x05040100u); hi[d] = __builtin_amdgcn_perm(fwb[d], fwa[d], 0x07060302u); }
#pragma unroll
    for (int bb = 0; bb < 4; ++bb) {
      LAS v4f* p = (LAS v4f*)(L + L_FP2 + ((qr * 4 + bb) * 192 + qc * 8) * 4);
      p[0] = v4f{dot2(lo[0], fx4[bb], 0.f), dot2(hi[0], fx4[bb], 0.f), dot2(lo[1], fx4[bb], 0.f), dot2(hi[1], fx4[bb], 0.f)};
      p[1] = v4f{dot2(lo[2], fx4[bb], 0.f), dot2(hi[2], fx4[bb], 0.f), dot2(lo[3], fx4[bb], 0.f), dot2(hi[3], fx4[bb], 0.f)};
    }
  }
  __syncthreads();
  if (tid < 384) {
    const int bb = tid / 96, p = tid - bb * 96;
    float a0 = 0.f, a1 = 0.f;
#pragma unroll
    for (int r = 0; r < 20; ++r) { const v2f v = *(LAS v2f*)(L + L_FP + ((r * 4 + bb) * 192 + p * 2) * 4); a0 += v[0]; a1 += v[1]; }
    __builtin_amdgcn_raw_buffer_store_b128(v4u{__float_as_uint(a0), tagA, __float_as_uint(a1), tagB}, rws, WS_QX + (((h * 4 + bb) * 8 + sh) * 128 + p) * 16, 0, AUX_SC1);
    a0 = 0.f; a1 = 0.f;
#pragma unroll
    for (int r = 0; r < 20; ++r) { const v2f v = *(LAS v2f*)(L + L_FP2 + ((r * 4 + bb) * 192 + p * 2) * 4); a0 += v[0]; a1 += v[1]; }
    __builtin_amdgcn_raw_buffer_store_b128(v4u{__float_as_uint(a0), tagA, __float_as_uint(a1), tagB}, rws,
                                           WS_FX + (((((xg * 4 + bb) * 8 + p / 12) * 2 + (idx & 1)) * 16 + (idx >> 1)) * 12 + p % 12) * 16, 0, AUX_SC1);
  }
  if (tid >= 384 && tid < 388)
    __builtin_amdgcn_raw_buffer_store_b128(v4u{T0, tagA, T0, tagB}, rws, WS_QX + (((h * 4 + (tid - 384)) * 8 + sh) * 128 + 96) * 16, 0, AUX_SC1);
  SCHED_FENCE();
  ISSUE_W(0, 2);
  ISSUE_W(6, 3);
  ISSUE_W(12, 4);
  if (w == 0) {
    __builtin_amdgcn_s_setprio(3);
    const int qo = WS_QX + (h * 4 + b) * 8 * 2048 + lane * 16;
    v4u ga[8], gb[8];
    for (unsigned spins = 0;;) {
#pragma unroll
      for (int p = 0; p < 8; ++p) { ga[p] = poll16(rws, qo + p * 2048); gb[p] = poll16(rws, qo + p * 2048 + 1024 + (lane <= 32 ? 0 : OOB)); }
      bool ok = true;
#pragma unroll
      for (int p = 0; p < 8; ++p) ok = ok & (ga[p][1] == tagA) & (ga[p][3] == tagB) & ((lane > 32) | ((gb[p][1] == tagA) & (gb[p][3] == tagB)));
      if (__all(ok) || ++spins > SPIN_CAP) break;
      __builtin_amdgcn_s_sleep(1);
    }
    float a0 = 0.f, a1 = 0.f, b0 = 0.f, b1 = 0.f;
#pragma unroll
    for (int p = 0; p < 8; ++p) { a0 += __uint_as_float(ga[p][0]); a1 += __uint_as_float(ga[p][2]); b0 += __uint_as_float(gb[p][0]); b1 += __uint_as_float(gb[p][2]); }
    *(LAS unsigned*)(L + L_QB + lane * 4) = cvtpk(a0, a1);
    if (lane < 32) *(LAS unsigned*)(L + L_QB + 256 + lane * 4) = cvtpk(b0, b1);
    unsigned tl = gb[0][0];
#pragma unroll
    for (int p = 1; p < 8; ++p) tl = (int)(gb[p][0] - tl) > 0 ? gb[p][0] : tl;
    if (lane == 32) *(LAS unsigned*)(L + L_MISC + 8) = tl;
  }
  tn += XPOLL16;
  ISSUE_W(18, 5);
  ISSUE_W(24, 6);
  __syncthreads();
  { const unsigned tl = __builtin_amdgcn_readfirstlane(*(LAS unsigned*)(L + L_MISC + 8)); if (tl - T0 < 1000u) tn += (tl - T0) << 4; }
  { const unsigned t = (now32() << 4) - 3 * TAU16; if ((int)(t - tn) > 0) tn = t; }
  streaming = true;
  v4u Q[3];
#pragma unroll
  for (int i = 0; i < 3; ++i) Q[i] = *(LAS v4u*)(L + L_QB + (c + 8 * i) * 16);

  float mx = -INFINITY;
  const bool oct0 = c == 0;
  const int scoff = L_SC + w * 2048 + k * 8;
  auto k_tile = [&](const v4u& r0, const v4u& r1, const v4u& r2) -> float {
    float a0 = dot2(r0[0], Q[0][0], 0.f), a1 = dot2(r1[0], Q[1][0], 0.f), a2 = dot2(r2[0], Q[2][0], 0.f);
#pragma unroll
    for (int d = 1; d < 4; ++d) { a0 = dot2(r0[d], Q[0][d], a0); a1 = dot2(r1[d], Q[1][d], a1); a2 = dot2(r2[d], Q[2][d], a2); }
    return (a0 + a1) + a2;
  };
  auto k_put = [&](float s0, float s1, int n) {
    s0 = oct_sum(s0); s1 = oct_sum(s1);
    mx = fmaxf(mx, fmaxf(s0, s1));
    if (oct0) *(LAS v2f*)(L + scoff + n * 64) = v2f{s0, s1};
  };
#define K_STEP(G, n, m) do { float s0_ = k_tile(G.r[0][0], G.r[0][1], G.r[0][2]), s1_ = k_tile(G.r[1][0], G.r[1][1], G.r[1][2]); \
    asm volatile("" : "+v"(s0_), "+v"(s1_) : : "memory"); ISSUE(G, m); k_put(s0_, s1_, n); } while (0)
  K_STEP(A, 0, 7);
#pragma unroll
  for (int g = 0; g < 5; ++g) k_put(k_tile(W[g * 6], W[g * 6 + 1], W[g * 6 + 2]), k_tile(W[g * 6 + 3], W[g * 6 + 4], W[g * 6 + 5]), 2 + g);
  K_STEP(B, 1, 8);
  {
    const int lane = fresh_lane(), tid2 = w * 64 + lane;
    const int yr = lane / 10, yc = lane - yr * 10;
    const int fxo = WS_FX + ((xg * 4 + b) * 8 + (j & 7)) * 6144 + tid2 * 16 + (tid2 < 384 ? 0 : OOB);
    v4u fx = poll16(rws, fxo);
    const __amdgpu_buffer_rsrc_t r2 = rsrc(w2, FF * DM * 2), rg = rsrc(gw, w == 0 ? DM * 2 : 0);
    const int vo2 = (j * 24 + yr) * (DM * 2) + w * 160 + yc * 16 + (lane < 60 ? 0 : OOB);
#pragma unroll
    for (int t = 0; t < 4; ++t) W[8 + t] = ld16<0>(r2, vo2, t * 6 * (DM * 2));
    const v4u gg0 = ld16<AUX_NT>(rg, lane * 16, 0), gg1 = ld16<AUX_NT>(rg, 1024 + lane * 16, 0);
    SCHED_FENCE();
    tn += XFFN16;
    K_STEP(A, 7, 9); K_STEP(B, 8, 10);
    for (unsigned spins = 0; !__all((tid2 >= 384) | ((fx[1] == tagA) & (fx[3] == tagB))); ) {
      if (++spins > SPIN_CAP) break;
      __builtin_amdgcn_s_sleep(2);
      fx = poll16(rws, fxo);
    }
    volatile LAS unsigned* flag = (volatile LAS unsigned*)(L + L_MISC + 192);
    if (tid2 < 384) *(LAS v2f*)(L + L_G + ((tid2 / 12) * 24 + (tid2 % 12) * 2) * 4) = v2f{__uint_as_float(fx[0]), __uint_as_float(fx[2])};
    float gate = 0.f;
    if (w == 0) {
      const v4u gx0 = *(LAS v4u*)(L + L_XS + b * (DM * 2) + lane * 16), gx1 = *(LAS v4u*)(L + L_XS + b * (DM * 2) + 1024 + (lane & 15) * 16);
      float d = 0.f;
#pragma unroll
      for (int e = 0; e < 4; ++e) { d = dot2(gx0[e], gg0[e], d); d = dot2(gx1[e], gg1[e], d); }
      gate = 1.f / (1.f + __expf(-wave_sum(d)));
      if (lane == 0) *(LAS float*)(L + L_MISC) = gate;
    }
    LDS_FENCE();
    if (lane == 0) flag[w] = tagA;
    K_STEP(A, 9, 11);
    if (w == 0) {
      for (unsigned spins = 0; !__all(lane >= 6 || flag[lane < 6 ? lane : 0] == tagA) && ++spins < (SPIN_CAP << 4); ) __builtin_amdgcn_s_sleep(1);
      MEM_FENCE();
      const LAS float* g = (const LAS float*)(L + L_G) + (lane < 24 ? lane : 0);
      float ga = 0.f, up = 0.f;
#pragma unroll
      for (int r = 0; r < 16; ++r) { ga += g[r * 24]; up += g[(16 + r) * 24]; }
      if (lane < 24) *(LAS float*)(L + L_MISC + 64 + lane * 4) = ga / (1.f + __expf(-ga)) * up * (1.f - gate);
      LDS_FENCE();
      if (lane == 0) flag[8] = tagA;
    }
    K_STEP(B, 10, 12);
    for (unsigned spins = 0; flag[8] != tagA && ++spins < (SPIN_CAP << 4); ) __builtin_amdgcn_s_sleep(1);
    MEM_FENCE();
    v2f ya[4];
#pragma unroll
    for (int e = 0; e < 4; ++e) ya[e] = v2f{0.f, 0.f};
#pragma unroll
    for (int t = 0; t < 4; ++t) {
      const float hv = *(LAS float*)(L + L_MISC + 64 + (t * 6 + (yr < 6 ? yr : 0)) * 4);
#pragma unroll
      for (int d = 0; d < 4; ++d) ya[d] = __builtin_elementwise_fma(v2f{hv, hv}, v2f{bflo(W[8 + t][d]), bfhi(W[8 + t][d])}, ya[d]);
    }
    if (lane < 60) {
      LAS v4f* p = (LAS v4f*)(L + L_YF + (w * 480 + yr * 80 + yc * 8) * 4);
      p[0] = v4f{ya[0][0], ya[0][1], ya[1][0], ya[1][1]}; p[1] = v4f{ya[2][0], ya[2][1], ya[3][0], ya[3][1]};
    }
    wo_vo = yr * (DM * 2) + w * 160 + yc * 16 + (lane < 60 ? 0 : OOB);
  }
  MEM_FENCE();
#define WO_COPY(t0) do { asm volatile("s_waitcnt vmcnt(12)" ::: "memory"); const int ln_ = fresh_lane(); \
    _Pragma("unroll") for (int t = 0; t < 8; t += 2) {     \
      const v4u a_ = *(LAS v4u*)(L + L_FP + w * 8192 + t * 1024 + ln_ * 16), b_ = *(LAS v4u*)(L + L_FP + w * 8192 + (t + 1) * 1024 + ln_ * 16); \
      _Pragma("unroll") for (int d = 0; d < 4; ++d) { W[t0 + t][d] = __builtin_amdgcn_perm(b_[d], a_[d], 0x05040100u); W[t0 + t + 1][d] = __builtin_amdgcn_perm(b_[d], a_[d], 0x07060302u); } } \
    asm volatile("" : "+v"(W[t0]), "+v"(W[t0 + 1]), "+v"(W[t0 + 2]), "+v"(W[t0 + 3]), "+v"(W[t0 + 4]), "+v"(W[t0 + 5]), "+v"(W[t0 + 6]), "+v"(W[t0 + 7]) : : "memory"); } while (0)
  int n;
#pragma unroll 1
  for (n = 11; n < 21; n += 2) {
    wo_t = n < 19 ? n - 11 : -1; K_STEP(A, n, n + 2);
    wo_t = n < 19 ? n - 10 : -1; K_STEP(B, n + 1, n + 3);
  }
  WO_COPY(0);
#pragma unroll 1
  for (; n < 31; n += 2) {
    wo_t = n < 29 ? n - 13 : -1; K_STEP(A, n, n + 2);
    wo_t = n < 29 ? n - 12 : -1; K_STEP(B, n + 1, n + 3);
  }
  WO_COPY(8);
  wo_t = -1;
  K_STEP(A, 31, 33);
  mx = fmaxf(mx, dpp<XOR8>(mx)); mx = max16_32(mx);
  const float mw = mx * CS;
  LDS_FENCE();
  {
    const int lane = fresh_lane();
    LAS v4f* p = (LAS v4f*)(L + L_SC + w * 2048 + lane * 32);
    v4f a = p[0], bb = p[1];
    float l = 0.f;
#pragma unroll
    for (int e = 0; e < 4; ++e) { a[e] = ex2(fmaf(a[e], CS, -mw)); bb[e] = ex2(fmaf(bb[e], CS, -mw)); l += a[e] + bb[e]; }
    p[0] = a; p[1] = bb;
    l = wave_sum(l);
    if (lane == 0) __builtin_amdgcn_raw_buffer_store_b128(v4u{__float_as_uint(mw), tagA, __float_as_uint(l), tagB}, rws, WS_ML + (((b * NH + h) * 2 + s) * 8 + w) * 16, 0, AUX_SC1);
  }
  LDS_FENCE();

  v2f acc2[12];
#pragma unroll
  for (int e = 0; e < 12; ++e) acc2[e] = v2f{0.f, 0.f};
#define PIN_ACC() asm volatile("" : "+v"(acc2[0]), "+v"(acc2[1]), "+v"(acc2[2]), "+v"(acc2[3]), "+v"(acc2[4]), "+v"(acc2[5]), "+v"(acc2[6]), "+v"(acc2[7]), \
                                    "+v"(acc2[8]), "+v"(acc2[9]), "+v"(acc2[10]), "+v"(acc2[11]) : : "memory")
  auto v_tile = [&](const v4u& r0, const v4u& r1, const v4u& r2, float p) {
    const v2f pp = v2f{p, p};
#pragma unroll
    for (int d = 0; d < 4; ++d) {
      acc2[d] = __builtin_elementwise_fma(pp, v2f{bflo(r0[d]), bfhi(r0[d])}, acc2[d]);
      acc2[4 + d] = __builtin_elementwise_fma(pp, v2f{bflo(r1[d]), bfhi(r1[d])}, acc2[4 + d]);
      acc2[8 + d] = __builtin_elementwise_fma(pp, v2f{bflo(r2[d]), bfhi(r2[d])}, acc2[8 + d]);
    }
    PIN_ACC();
  };
#define V_USE(G, n) do { const v2f p2_ = *(LAS v2f*)(L + scoff + (n) * 64); v_tile(G.r[0][0], G.r[0][1], G.r[0][2], p2_[0]); v_tile(G.r[1][0], G.r[1][1], G.r[1][2], p2_[1]); } while (0)
#pragma unroll 1
  for (n = 0; n < 10; n += 2) {
    V_USE(B, n); wo_t = n < 8 ? n + 16 : -1; ISSUE(B, NGW + n + 2);
    V_USE(A, n + 1); wo_t = n < 8 ? n + 17 : -1; ISSUE(A, NGW + n + 3);
  }
  WO_COPY(16);
#pragma unroll 1
  for (; n < 20; n += 2) {
    V_USE(B, n); wo_t = n < 18 ? n + 14 : -1; ISSUE(B, NGW + n + 2);
    V_USE(A, n + 1); wo_t = n < 18 ? n + 15 : -1; ISSUE(A, NGW + n + 3);
  }
  WO_COPY(24);
  wo_t = -1;
  const int mloff = WS_ML + ((b * NH + h) * 16 + (fresh_lane() & 15)) * 16;
  v4u ml = poll16(rws, mloff);
  V_USE(B, n); ISSUE(B, NGW + n + 2); V_USE(A, n + 1); ISSUE(A, NGW + n + 3);
  for (unsigned spins = 0; !__all((ml[1] == tagA) & (ml[3] == tagB)); ) {
    if (++spins > SPIN_CAP) break;
    __builtin_amdgcn_s_sleep(2);
    ml = poll16(rws, mloff);
  }
  float fw;
  {
    const float m_i = __uint_as_float(ml[0]), l_i = __uint_as_float(ml[2]);
    float M = m_i;
    M = fmaxf(M, dpp<XOR1>(M)); M = fmaxf(M, dpp<XOR2>(M)); M = fmaxf(M, dpp<XOR7>(M)); M = fmaxf(M, dpp<XOR8>(M));
    float Ls = l_i * ex2(m_i - M);
    Ls = oct_sum(Ls); Ls += dpp<XOR8>(Ls);
    fw = __uint_as_float(__builtin_amdgcn_readfirstlane(__float_as_uint(ex2(mw - M) / Ls * *(LAS float*)(L + L_MISC))));
  }
#pragma unroll 1
  for (n += 2; n < NGW - 2; n += 2) { V_USE(B, n); ISSUE(B, NGW + n + 2); V_USE(A, n + 1); ISSUE(A, NGW + n + 3); }
  V_USE(B, NGW - 2);
  V_USE(A, NGW - 1);
  {
    float b12[12], c6[6];
#pragma unroll
    for (int i = 0; i < 12; ++i) {
      auto r = __builtin_amdgcn_permlane32_swap(__float_as_uint(acc2[i >> 1][i & 1]), __float_as_uint(acc2[6 + (i >> 1)][i & 1]), false, false);
      b12[i] = __uint_as_float(r[0]) + __uint_as_float(r[1]);
    }
#pragma unroll
    for (int i = 0; i < 6; ++i) {
      auto r = __builtin_amdgcn_permlane16_swap(__float_as_uint(b12[i]), __float_as_uint(b12[i + 6]), false, false);
      c6[i] = __uint_as_float(r[0]) + __uint_as_float(r[1]);
    }
#pragma unroll
    for (int i = 0; i < 6; ++i) c6[i] = (c6[i] + dpp<XOR8>(c6[i])) * fw;
    const int ln = fresh_lane();
    if ((ln & 8) == 0) {
      const int v0 = (ln >> 4) * 6;
#pragma unroll
      for (int p = 0; p < 3; ++p) { const int v = v0 + 2 * p; *(LAS v2f*)(L + L_QP + (w * QPS + (v >> 3) * 64 + (ln & 7) * 8 + (v & 7)) * 4) = v2f{c6[2 * p], c6[2 * p + 1]}; }
    }
  }
  const int ln = fresh_lane();
  const int yr = ln < 60 ? ln / 10 : 0, yc = ln - (ln / 10) * 10;
  float ya[8];
  { const LAS v4f* p = (const LAS v4f*)(L + L_YF + (w * 480 + yr * 80 + yc * 8) * 4); const v4f a = p[0], bb = p[1];
#pragma unroll
    for (int e = 0; e < 4; ++e) { ya[e] = a[e]; ya[4 + e] = bb[e]; } }
  __syncthreads();
  if (w < 6) {
    const int pi = w * 16 + (ln >> 2), r = pi >> 4, u = pi & 15, qq = ln & 3;
    const LAS float* p = (const LAS float*)(L + L_QP) + 2 * qq * QPS + u * 12 + r;
    float a0 = p[0] + p[QPS], a1 = p[6] + p[QPS + 6];
    a0 += dpp<XOR1>(a0); a1 += dpp<XOR1>(a1); a0 += dpp<XOR2>(a0); a1 += dpp<XOR2>(a1);
    if (qq == 0) *(LAS unsigned*)(L + L_OB + pi * 4) = cvtpk(a0, a1);
  }
  __syncthreads();
  {
    LAS float* yrp = (LAS float*)(L + L_FP) + w * 480;
    const LAS v4u* op = (const LAS v4u*)(L + L_OB + yr * 64);
#pragma unroll
    for (int u4 = 0; u4 < 4; ++u4) {
      const v4u o4 = op[u4];
#pragma unroll
      for (int e = 0; e < 4; ++e) {
        const int u = u4 * 4 + e;
#pragma unroll
        for (int d = 0; d < 4; ++d) { ya[2 * d] = dot2(W[2 * u][d], o4[e], ya[2 * d]); ya[2 * d + 1] = dot2(W[2 * u + 1][d], o4[e], ya[2 * d + 1]); }
      }
    }
    asm volatile("" : "+v"(ya[0]), "+v"(ya[1]), "+v"(ya[2]), "+v"(ya[3]), "+v"(ya[4]), "+v"(ya[5]), "+v"(ya[6]), "+v"(ya[7]));
    if (ln < 60) { LAS v4f* p = (LAS v4f*)(yrp + yr * 80 + yc * 8); p[0] = v4f{ya[0], ya[1], ya[2], ya[3]}; p[1] = v4f{ya[4], ya[5], ya[6], ya[7]}; }
    LDS_FENCE();
    if (ln < 40) {
      v2f y = v2f{0.f, 0.f};
#pragma unroll
      for (int r = 0; r < 6; ++r) y += *(LAS v2f*)(yrp + r * 80 + ln * 2);
      const int cons = w * 8 + ln / 5, g5 = ln % 5;
      __builtin_amdgcn_raw_buffer_store_b128(v4u{__float_as_uint(y[0]), tagA, __float_as_uint(y[1]), tagB}, rws, WS_INBOX + (((b * 64 + cons) * 64 + j) * 5 + g5) * 16, 0, AUX_SC1);
    }
  }
  if (w == 0) {
    const int io = WS_INBOX + (b * 64 + j) * 5120 + ln * 16;
    __builtin_amdgcn_s_sleep(24);
    v4u g5[5];
    for (unsigned spins = 0;;) {
      bool ok = true;
#pragma unroll
      for (int e = 0; e < 5; ++e) g5[e] = poll16(rws, io + e * 1024);
#pragma unroll
      for (int e = 0; e < 5; ++e) ok = ok & (g5[e][1] == tagA) & (g5[e][3] == tagB);
      if (__all(ok) || ++spins > SPIN_CAP) break;
      __builtin_amdgcn_s_sleep(2);
    }
    unsigned pv[5][2];
    { const int r5 = ln % 5;
#pragma unroll
      for (int c5 = 0; c5 < 5; ++c5) {
        unsigned v0 = 0, v1 = 0;
#pragma unroll
        for (int r = 0; r < 5; ++r) { const int e = (4 * (c5 - r + 5)) % 5; v0 = r5 == r ? g5[e][0] : v0; v1 = r5 == r ? g5[e][2] : v1; }
        pv[c5][0] = v0; pv[c5][1] = v1;
      } }
    const unsigned yv[10] = {pv[0][0], pv[0][1], pv[1][0], pv[1][1], pv[4][0], pv[2][0], pv[2][1], pv[3][0], pv[3][1], pv[4][1]};
    float b5[5];
#pragma unroll
    for (int i = 0; i < 5; ++i) { auto r = __builtin_amdgcn_permlane32_swap(yv[i], yv[5 + i], false, false); b5[i] = __uint_as_float(r[0]) + __uint_as_float(r[1]); }
    float z[3];
    { auto r = __builtin_amdgcn_permlane16_swap(__float_as_uint(b5[0]), __float_as_uint(b5[2]), false, false); z[0] = __uint_as_float(r[0]) + __uint_as_float(r[1]); }
    { auto r = __builtin_amdgcn_permlane16_swap(__float_as_uint(b5[1]), __float_as_uint(b5[3]), false, false); z[1] = __uint_as_float(r[0]) + __uint_as_float(r[1]); }
    { auto r = __builtin_amdgcn_permlane16_swap(__float_as_uint(b5[4]), __float_as_uint(b5[4]), false, false); z[2] = __uint_as_float(r[0]) + __uint_as_float(r[1]); }
#pragma unroll
    for (int i = 0; i < 3; ++i) { z[i] += dpp<XOR1>(z[i]); z[i] += dpp<XOR2>(z[i]); z[i] += dpp<XOR7>(z[i]); z[i] += dpp<XOR8>(z[i]); }
    auto r89 = __builtin_amdgcn_permlane32_swap(__float_as_uint(z[2]), __float_as_uint(z[2]), false, false);
    const unsigned o03 = cvtpk(z[0], z[1]), o4 = cvtpk(__uint_as_float(r89[0]), __uint_as_float(r89[1]));
    if ((ln & 15) == 0 || ln == 1) *(GAS unsigned*)(out + b * DM + j * 10 + (ln == 1 ? 8 : (ln >> 4) * 2)) = ln == 1 ? o4 : o03;
    if (b == 0 && j == 0 && ln == 0) __hip_atomic_store((GAS u64*)(ws + WS_EPOCH), ((u64)tagB << 32) | tagA, __ATOMIC_RELAXED, __HIP_MEMORY_SCOPE_AGENT);
  }
}
}

extern "C" void kernel_launch(void* const* d_in, const int* in_sizes, int n_in, void* d_out, int out_size, void* d_ws, size_t ws_size,
                              hipStream_t stream) {
  (void)in_sizes; (void)n_in; (void)out_size; (void)ws_size;
  hipLaunchKernelGGL(fused_decode, dim3(NWG), dim3(NTHR), 0, stream, (const u16*)d_in[0], (const u16*)d_in[1], (const u16*)d_in[2], (const u16*)d_in[3],
                     (const u16*)d_in[4], (const u16*)d_in[6], (const u16*)d_in[7], (const u16*)d_in[8], (const u16*)d_in[9], (u16*)d_out, (unsigned char*)d_ws);
}
```

```cpp
#include <hip/hip_runtime.h>
#include <hip/hip_bf16.h>
#include <stdint.h>

#define GAS __attribute__((address_space(1)))
#define LAS __attribute__((address_space(3)))
typedef unsigned v4u __attribute__((ext_vector_type(4)));
typedef unsigned v2u __attribute__((ext_vector_type(2)));
typedef float v4f __attribute__((ext_vector_type(4)));
typedef float v2f __attribute__((ext_vector_type(2)));
typedef __bf16 bf16x2 __attribute__((ext_vector_type(2)));
typedef unsigned long long u64;
typedef unsigned short u16;

namespace {
constexpr int NB = 4, NH = 32, SK = 8192, DH = 192, DM = 640, FF = 1536;
constexpr int NWG = 256, NWAVE = 8, NTHR = NWAVE * 64;
constexpr int KEYS_WG = SK / 2;
constexpr int ROWB = DH * 2;
constexpr int TILEB = 8 * ROWB;
constexpr int GRPB = 2 * TILEB;
constexpr int KVB = KEYS_WG * ROWB;
constexpr int NGW = KVB / GRPB / NWAVE;
constexpr int RSRC3 = 0x00020000;
constexpr int AUX_NT = 2, AUX_SC1 = 16;
constexpr int OOB = 1 << 28;
constexpr float CS = 0.07216878364870322f * 1.4426950408889634f;
constexpr unsigned TAU16 = 175 * 16;
constexpr unsigned XHEAD16 = TAU16;
constexpr unsigned XPOLL16 = TAU16 * 3 / 10, XFFN16 = TAU16 * 3 / 10;
constexpr unsigned XDMA16 = TAU16 * 3 / 100;
constexpr unsigned CUSP16 = TAU16 / 8 * 60 / 100;
constexpr int T0OFF = 40, XGADJ = 12;

constexpr int WS_EPOCH = 0;
constexpr int WS_ML = 4096;
constexpr int WS_INBOX = 65536;
constexpr int WS_QX = WS_INBOX + NB * 64 * 64 * 128;
constexpr int WS_FX = WS_QX + NH * NB * 8 * 128 * 16;
constexpr int WS_BYTES = WS_FX + 8 * NB * 8 * 384 * 16;

constexpr int L_XS = 0;
constexpr int L_QP = 5120;
constexpr int QPS = 196;
constexpr int L_QB = L_QP + 8 * QPS * 4;
constexpr int L_OB = L_QB + 384;
constexpr int L_MISC = L_OB + 384;
constexpr int L_G = L_MISC + 256;
constexpr int L_YF = L_G + 3072;
constexpr int L_SC = L_YF + 8 * 480 * 4;
constexpr int L_FP2 = L_YF;
constexpr int L_FP = L_YF + 22 * 4 * 192 * 4;
constexpr int L_SCR = L_FP + 22 * 4 * 192 * 4;
constexpr int L_BYTES = L_SCR + 256;

constexpr unsigned SPIN_CAP = 1u << 17;

__device__ __forceinline__ float dot2(unsigned a, unsigned b, float c) {
  return __builtin_amdgcn_fdot2_f32_bf16(__builtin_bit_cast(bf16x2, a), __builtin_bit_cast(bf16x2, b), c, false);
}
template <int CTRL> __device__ __forceinline__ float dpp(float x) {
  return __builtin_bit_cast(float, __builtin_amdgcn_mov_dpp(__builtin_bit_cast(int, x), CTRL, 0xf, 0xf, true));
}
constexpr int XOR1 = 0xB1, XOR2 = 0x4E, XOR7 = 0x141, XOR8 = 0x128;
__device__ __forceinline__ float sum16_32(float x) {
  auto s = __builtin_amdgcn_permlane16_swap(__float_as_uint(x), __float_as_uint(x), false, false);
  x = __uint_as_float(s[0]) + __uint_as_float(s[1]);
  auto t = __builtin_amdgcn_permlane32_swap(__float_as_uint(x), __float_as_uint(x), false, false);
  return __uint_as_float(t[0]) + __uint_as_float(t[1]);
}
__device__ __forceinline__ float max16_32(float x) {
  auto s = __builtin_amdgcn_permlane16_swap(__float_as_uint(x), __float_as_uint(x), false, false);
  x = fmaxf(__uint_as_float(s[0]), __uint_as_float(s[1]));
  auto t = __builtin_amdgcn_permlane32_swap(__float_as_uint(x), __float_as_uint(x), false, false);
  return fmaxf(__uint_as_float(t[0]), __uint_as_float(t[1]));
}
__device__ __forceinline__ float oct_sum(float x) { x += dpp<XOR1>(x); x += dpp<XOR2>(x); x += dpp<XOR7>(x); return x; }
__device__ __forceinline__ float xoct_sum(float x) { x += dpp<XOR8>(x); return sum16_32(x); }
__device__ __forceinline__ float wave_sum(float x) { return xoct_sum(oct_sum(x)); }
__device__ __forceinline__ unsigned cvtpk(float lo, float hi) { unsigned r; asm("v_cvt_pk_bf16_f32 %0, %1, %2" : "=v"(r) : "v"(lo), "v"(hi)); return r; }
__device__ __forceinline__ float bflo(unsigned u) { return __uint_as_float(u << 16); }
__device__ __forceinline__ float bfhi(unsigned u) { return __uint_as_float(u & 0xffff0000u); }
__device__ __forceinline__ float ex2(float x) { return __builtin_amdgcn_exp2f(x); }
__device__ __forceinline__ __amdgpu_buffer_rsrc_t rsrc(const void* p, int bytes) { return __builtin_amdgcn_make_buffer_rsrc((void*)p, (short)0, bytes, RSRC3); }
template <int AUX> __device__ __forceinline__ v4u ld16(__amdgpu_buffer_rsrc_t r, int voff, int soff) { return __builtin_amdgcn_raw_buffer_load_b128(r, voff, soff, AUX); }
__device__ __forceinline__ unsigned now32() { return (unsigned)__builtin_amdgcn_s_memrealtime(); }
#define SCHED_FENCE() __builtin_amdgcn_sched_barrier(0)
#define LDS_FENCE() asm volatile("s_waitcnt lgkmcnt(0)" ::: "memory")
#define MEM_FENCE() asm volatile("" ::: "memory")
__device__ __forceinline__ v4u poll16(__amdgpu_buffer_rsrc_t r, int voff) { asm volatile("" : "+v"(voff) : : "memory"); return __builtin_amdgcn_raw_buffer_load_b128(r, voff, 0, AUX_SC1); }
__device__ __forceinline__ int fresh_lane() { int l; asm volatile("v_mbcnt_lo_u32_b32 %0, -1, 0\n\tv_mbcnt_hi_u32_b32 %0, -1, %0" : "=v"(l)); return l; }

__device__ __forceinline__ void dma16(__amdgpu_buffer_rsrc_t r, int voff, int soff, unsigned lds) {
  unsigned keep;
  asm volatile("s_mov_b32 %0, m0\n\ts_mov_b32 m0, %3\n\ts_nop 0\n\tbuffer_load_dwordx4 %1, %2, %4 offen lds\n\ts_mov_b32 m0, %0"
               : "=&s"(keep) : "v"(voff), "s"(r), "s"(lds), "s"(soff) : "memory");
}
__device__ __forceinline__ void dma4(__amdgpu_buffer_rsrc_t r, int voff, unsigned lds) {
  unsigned keep;
  asm volatile("s_mov_b32 %0, m0\n\ts_mov_b32 m0, %3\n\ts_nop 0\n\tbuffer_load_dword %1, %2, 0 offen sc1 lds\n\ts_mov_b32 m0, %0"
               : "=&s"(keep) : "v"(voff), "s"(r), "s"(lds) : "memory");
}
struct Grp { v4u r[2][3]; };

__global__ void __launch_bounds__(NTHR, 2)
fused_decode(const u16* __restrict__ x, const u16* __restrict__ Kc, const u16* __restrict__ Vc, const u16* __restrict__ wq,
             const u16* __restrict__ wo, const u16* __restrict__ w1, const u16* __restrict__ w3, const u16* __restrict__ w2,
             const u16* __restrict__ gw, u16* __restrict__ out, unsigned char* ws) {
  __shared__ __attribute__((aligned(16))) unsigned char lds_raw[L_BYTES];
  LAS unsigned char* L = (LAS unsigned char*)lds_raw;
  const int tid = threadIdx.x, lane = tid & 63, w = __builtin_amdgcn_readfirstlane(tid >> 6);
  const int k = lane >> 3, c = lane & 7;
  const int blk = blockIdx.x, xg = blk & 7, idx = blk >> 3;
  const int h = xg * 4 + (idx & 3), sh = idx >> 2, b = sh >> 1, s = sh & 1;
  const int j = h * 2 + s;
  const unsigned T0 = now32() + T0OFF + (3 - xg) * XGADJ;
  const __amdgpu_buffer_rsrc_t rws = rsrc(ws, WS_BYTES);
  const v4u tiny = __builtin_nontemporal_load((const GAS v4u*)(tid == 320 ? (const unsigned char*)ws + WS_EPOCH : (const unsigned char*)x + (tid < 320 ? tid : 0) * 16));
  const int qr = tid / 24, qc = tid - qr * 24;
  v4u wqv[4], fwa, fwb;
  {
    const __amdgpu_buffer_rsrc_t rq = rsrc(wq + h * DH, (DM * NH * DH - h * DH) * 2);
    const int oob = tid < 480 ? 0 : OOB;
#pragma unroll
    for (int e = 0; e < 4; ++e) wqv[e] = ld16<AUX_NT>(rq, (sh * 80 + qr * 4) * (NH * DH * 2) + qc * 16 + oob, e * (NH * DH * 2));
    const int row0 = (idx >> 1) * 40 + qr * 2;
    const __amdgpu_buffer_rsrc_t r13 = rsrc(((idx & 1) ? w3 : w1) + xg * 192, (DM * FF - xg * 192) * 2);
    fwa = ld16<AUX_NT>(r13, row0 * (FF * 2) + qc * 16 + oob, 0); fwb = ld16<AUX_NT>(r13, row0 * (FF * 2) + qc * 16 + oob, FF * 2);
  }
  const size_t kvoff = ((size_t)(b * NH + h) * SK + (size_t)s * KEYS_WG) * DH;
  const u16* Kb = Kc + kvoff; const u16* Vb = Vc + kvoff;
  if (w == 7) {
    const int o = lane == 0 ? WS_QX + ((h * 4) * 8 + sh) * 2048 : lane == 1 ? WS_QX + (h * 4 + 3) * 8 * 2048 + 16380
                : lane == 2 ? WS_FX + xg * 4 * 8 * 6144 : lane == 3 ? WS_FX + (xg * 4 + 3) * 8 * 6144 + 8 * 6144 - 4
                : lane == 4 ? WS_INBOX + b * 64 * 64 * 128 : lane == 5 ? WS_INBOX + (b + 1) * 64 * 64 * 128 - 4 : lane == 6 ? WS_ML : OOB;
    dma4(rws, o, L_SCR);
  }
  const int rho = (idx + xg * 4) & 31, sigma = (int)((blockIdx.x * 2246822519u) >> 29);
  const int kvo = ((w + sigma) & 7) * GRPB + k * ROWB + c * 16;
  unsigned tn = (T0 << 4) + w * (TAU16 / 8);
  bool streaming = false;
  const bool lane0 = lane == 0;
  auto pace = [&](unsigned step16) {
    unsigned now = now32() << 4;
    const int late = (int)(now - tn);
    if (late > (int)(2 * TAU16)) __builtin_amdgcn_s_setprio(3);
    else if (late > (int)TAU16) __builtin_amdgcn_s_setprio(2);
    else if (late > (int)(TAU16 / 4)) __builtin_amdgcn_s_setprio(1);
    else __builtin_amdgcn_s_setprio(0);
    while ((int)(now - tn) < 0) { __builtin_amdgcn_s_sleep(1); now = now32() << 4; }
    if (streaming) {
      LAS unsigned* cu = (LAS unsigned*)(L + L_MISC + 32);
      const unsigned base = T0 << 4;
      unsigned v = 0;
      if (lane0) v = __hip_atomic_fetch_add(cu, CUSP16, __ATOMIC_RELAXED, __HIP_MEMORY_SCOPE_WORKGROUP);
      v = __builtin_amdgcn_readfirstlane(v);
      now = (now32() << 4) - base;
      if ((int)(v - now) > 0) { while ((int)(((now32() << 4) - base) - v) < 0) __builtin_amdgcn_s_sleep(1); }
      else if (lane0) __hip_atomic_fetch_max(cu, now + CUSP16, __ATOMIC_RELAXED, __HIP_MEMORY_SCOPE_WORKGROUP);
    }
    tn += step16;
  };
  int wo_t = -1;
  __amdgpu_buffer_rsrc_t ro = rsrc(wo + (size_t)h * DH * DM, DH * DM * 2);
  int wo_vo = 0;
  auto issue = [&](v4u& r00, v4u& r01, v4u& r02, v4u& r10, v4u& r11, v4u& r12, int m) {
    pace(TAU16 + (wo_t >= 0 ? XDMA16 : 0));
    if (wo_t >= 0) dma16(ro, wo_vo, wo_t * 6 * (DM * 2), (unsigned)(L_FP + w * 8192 + (wo_t & 7) * 1024));
    const __amdgpu_buffer_rsrc_t r = rsrc(m < NGW ? Kb : Vb, KVB);
    const int so = ((m + rho) & (NGW - 1)) * (NWAVE * GRPB);
    r00 = ld16<AUX_NT>(r, kvo, so); r01 = ld16<AUX_NT>(r, kvo + 128, so); r02 = ld16<AUX_NT>(r, kvo + 256, so);
    r10 = ld16<AUX_NT>(r, kvo, so + TILEB); r11 = ld16<AUX_NT>(r, kvo + 128, so + TILEB); r12 = ld16<AUX_NT>(r, kvo + 256, so + TILEB);
    SCHED_FENCE();
  };
#define ISSUE(G, m) issue(G.r[0][0], G.r[0][1], G.r[0][2], G.r[1][0], G.r[1][1], G.r[1][2], m)
#define ISSUE_W(o, m) issue(W[o], W[o + 1], W[o + 2], W[o + 3], W[o + 4], W[o + 5], m)
  Grp A, B;
  v4u W[32];
  ISSUE(A, 0);
  ISSUE(B, 1);
  tn += XHEAD16;
  if (tid <= 320) *(LAS v4u*)(L + (tid < 320 ? L_XS + tid * 16 : L_MISC + 16)) = tiny;
  if (tid == 511) *(LAS unsigned*)(L + L_MISC + 32) = 0u;
  __syncthreads();
  const unsigned tagA = __builtin_amdgcn_readfirstlane(*(LAS unsigned*)(L + L_MISC + 16)) + 1u, tagB = __builtin_amdgcn_readfirstlane(*(LAS unsigned*)(L + L_MISC + 20)) + 1u;
  v2u xq[4]; unsigned fx4[4];
  { const int qrc = qr < 20 ? qr : 0;
#pragma unroll
    for (int e = 0; e < 4; ++e) { xq[e] = *(LAS v2u*)(L + L_XS + e * (DM * 2) + (sh * 80 + qrc * 4) * 2); fx4[e] = *(LAS unsigned*)(L + L_XS + e * (DM * 2) + ((idx >> 1) * 40 + qrc * 2) * 2); } }
  {
    unsigned lo01[4], hi01[4], lo23[4], hi23[4];
#pragma unroll
    for (int d = 0; d < 4; ++d) {
      lo01[d] = __builtin_amdgcn_perm(wqv[1][d], wqv[0][d], 0x05040100u); hi01[d] = __builtin_amdgcn_perm(wqv[1][d], wqv[0][d], 0x07060302u);
      lo23[d] = __builtin_amdgcn_perm(wqv[3][d], wqv[2][d], 0x05040100u); hi23[d] = __builtin_amdgcn_perm(wqv[3][d], wqv[2][d], 0x07060302u);
    }
#pragma unroll
    for (int bb = 0; bb < 4; ++bb) {
      float a[8];
#pragma unroll
      for (int d = 0; d < 4; ++d) { a[2 * d] = dot2(lo23[d], xq[bb][1], dot2(lo01[d], xq[bb][0], 0.f)); a[2 * d + 1] = dot2(hi23[d], xq[bb][1], dot2(hi01[d], xq[bb][0], 0.f)); }
      LAS v4f* p = (LAS v4f*)(L + L_FP + ((qr * 4 + bb) * 192 + qc * 8) * 4);
      p[0] = v4f{a[0], a[1], a[2], a[3]}; p[1] = v4f{a[4], a[5], a[6], a[7]};
    }
  }
  {
    unsigned lo[4], hi[4];
#pragma unroll
    for (int d = 0; d < 4; ++d) { lo[d] = __builtin_amdgcn_perm(fwb[d], fwa[d], 0x05040100u); hi[d] = __builtin_amdgcn_perm(fwb[d], fwa[d], 0x07060302u); }
#pragma unroll
    for (int bb = 0; bb < 4; ++bb) {
      LAS v4f* p = (LAS v4f*)(L + L_FP2 + ((qr * 4 + bb) * 192 + qc * 8) * 4);
      p[0] = v4f{dot2(lo[0], fx4[bb], 0.f), dot2(hi[0], fx4[bb], 0.f), dot2(lo[1], fx4[bb], 0.f), dot2(hi[1], fx4[bb], 0.f)};
      p[1] = v4f{dot2(lo[2], fx4[bb], 0.f), dot2(hi[2], fx4[bb], 0.f), dot2(lo[3], fx4[bb], 0.f), dot2(hi[3], fx4[bb], 0.f)};
    }
  }
  __syncthreads();
  if (tid < 384) {
    const int bb = tid / 96, p = tid - bb * 96;
    float a0 = 0.f, a1 = 0.f;
#pragma unroll
    for (int r = 0; r < 20; ++r) { const v2f v = *(LAS v2f*)(L + L_FP + ((r * 4 + bb) * 192 + p * 2) * 4); a0 += v[0]; a1 += v[1]; }
    __builtin_amdgcn_raw_buffer_store_b128(v4u{__float_as_uint(a0), tagA, __float_as_uint(a1), tagB}, rws, WS_QX + (((h * 4 + bb) * 8 + sh) * 128 + p) * 16, 0, AUX_SC1);
    a0 = 0.f; a1 = 0.f;
#pragma unroll
    for (int r = 0; r < 20; ++r) { const v2f v = *(LAS v2f*)(L + L_FP2 + ((r * 4 + bb) * 192 + p * 2) * 4); a0 += v[0]; a1 += v[1]; }
    __builtin_amdgcn_raw_buffer_store_b128(v4u{__float_as_uint(a0), tagA, __float_as_uint(a1), tagB}, rws,
                                           WS_FX + (((((xg * 4 + bb) * 8 + p / 12) * 2 + (idx & 1)) * 16 + (idx >> 1)) * 12 + p % 12) * 16, 0, AUX_SC1);
  }
  if (tid >= 384 && tid < 388)
    __builtin_amdgcn_raw_buffer_store_b128(v4u{T0, tagA, T0, tagB}, rws, WS_QX + (((h * 4 + (tid - 384)) * 8 + sh) * 128 + 96) * 16, 0, AUX_SC1);
  SCHED_FENCE();
  ISSUE_W(0, 2);
  ISSUE_W(6, 3);
  ISSUE_W(12, 4);
  if (w == 0) {
    __builtin_amdgcn_s_setprio(3);
    const int qo = WS_QX + (h * 4 + b) * 8 * 2048 + lane * 16;
    v4u ga[8], gb[8];
    for (unsigned spins = 0;;) {
#pragma unroll
      for (int p = 0; p < 8; ++p) { ga[p] = poll16(rws, qo + p * 2048); gb[p] = poll16(rws, qo + p * 2048 + 1024 + (lane <= 32 ? 0 : OOB)); }
      bool ok = true;
#pragma unroll
      for (int p = 0; p < 8; ++p) ok = ok & (ga[p][1] == tagA) & (ga[p][3] == tagB) & ((lane > 32) | ((gb[p][1] == tagA) & (gb[p][3] == tagB)));
      if (__all(ok) || ++spins > SPIN_CAP) break;
      __builtin_amdgcn_s_sleep(1);
    }
    float a0 = 0.f, a1 = 0.f, b0 = 0.f, b1 = 0.f;
#pragma unroll
    for (int p = 0; p < 8; ++p) { a0 += __uint_as_float(ga[p][0]); a1 += __uint_as_float(ga[p][2]); b0 += __uint_as_float(gb[p][0]); b1 += __uint_as_float(gb[p][2]); }
    *(LAS unsigned*)(L + L_QB + lane * 4) = cvtpk(a0, a1);
    if (lane < 32) *(LAS unsigned*)(L + L_QB + 256 + lane * 4) = cvtpk(b0, b1);
    unsigned tl = gb[0][0];
#pragma unroll
    for (int p = 1; p < 8; ++p) tl = (int)(gb[p][0] - tl) > 0 ? gb[p][0] : tl;
    if (lane == 32) *(LAS unsigned*)(L + L_MISC + 8) = tl;
  }
  tn += XPOLL16;
  ISSUE_W(18, 5);
  ISSUE_W(24, 6);
  __syncthreads();
  { const unsigned tl = __builtin_amdgcn_readfirstlane(*(LAS unsigned*)(L + L_MISC + 8)); if (tl - T0 < 1000u) tn += (tl - T0) << 4; }
  { const unsigned t = (now32() << 4) - 3 * TAU16; if ((int)(t - tn) > 0) tn = t; }
  streaming = true;
  v4u Q[3];
#pragma unroll
  for (int i = 0; i < 3; ++i) Q[i] = *(LAS v4u*)(L + L_QB + (c + 8 * i) * 16);

  float mx = -INFINITY;
  const bool oct0 = c == 0;
  const int scoff = L_SC + w * 2048 + k * 8;
  auto k_tile = [&](const v4u& r0, const v4u& r1, const v4u& r2) -> float {
    float a0 = dot2(r0[0], Q[0][0], 0.f), a1 = dot2(r1[0], Q[1][0], 0.f), a2 = dot2(r2[0], Q[2][0], 0.f);
#pragma unroll
    for (int d = 1; d < 4; ++d) { a0 = dot2(r0[d], Q[0][d], a0); a1 = dot2(r1[d], Q[1][d], a1); a2 = dot2(r2[d], Q[2][d], a2); }
    return (a0 + a1) + a2;
  };
  auto k_put = [&](float s0, float s1, int n) {
    s0 = oct_sum(s0); s1 = oct_sum(s1);
    mx = fmaxf(mx, fmaxf(s0, s1));
    if (oct0) *(LAS v2f*)(L + scoff + n * 64) = v2f{s0, s1};
  };
#define K_STEP(G, n, m) do { float s0_ = k_tile(G.r[0][0], G.r[0][1], G.r[0][2]), s1_ = k_tile(G.r[1][0], G.r[1][1], G.r[1][2]); \
    asm volatile("" : "+v"(s0_), "+v"(s1_) : : "memory"); ISSUE(G, m); k_put(s0_, s1_, n); } while (0)
  K_STEP(A, 0, 7);
#pragma unroll
  for (int g = 0; g < 5; ++g) k_put(k_tile(W[g * 6], W[g * 6 + 1], W[g * 6 + 2]), k_tile(W[g * 6 + 3], W[g * 6 + 4], W[g * 6 + 5]), 2 + g);
  K_STEP(B, 1, 8);
  {
    const int lane = fresh_lane(), tid2 = w * 64 + lane;
    const int yr = lane / 10, yc = lane - yr * 10;
    const int fxo = WS_FX + ((xg * 4 + b) * 8 + (j & 7)) * 6144 + tid2 * 16 + (tid2 < 384 ? 0 : OOB);
    v4u fx = poll16(rws, fxo);
    const __amdgpu_buffer_rsrc_t r2 = rsrc(w2, FF * DM * 2), rg = rsrc(gw, w == 0 ? DM * 2 : 0);
    const int vo2 = (j * 24 + yr) * (DM * 2) + w * 160 + yc * 16 + (lane < 60 ? 0 : OOB);
#pragma unroll
    for (int t = 0; t < 4; ++t) W[8 + t] = ld16<0>(r2, vo2, t * 6 * (DM * 2));
    const v4u gg0 = ld16<AUX_NT>(rg, lane * 16, 0), gg1 = ld16<AUX_NT>(rg, 1024 + lane * 16, 0);
    SCHED_FENCE();
    tn += XFFN16;
    K_STEP(A, 7, 9); K_STEP(B, 8, 10);
    for (unsigned spins = 0; !__all((tid2 >= 384) | ((fx[1] == tagA) & (fx[3] == tagB))); ) {
      if (++spins > SPIN_CAP) break;
      __builtin_amdgcn_s_sleep(2);
      fx = poll16(rws, fxo);
    }
    volatile LAS unsigned* flag = (volatile LAS unsigned*)(L + L_MISC + 192);
    if (tid2 < 384) *(LAS v2f*)(L + L_G + ((tid2 / 12) * 24 + (tid2 % 12) * 2) * 4) = v2f{__uint_as_float(fx[0]), __uint_as_float(fx[2])};
    float gate = 0.f;
    if (w == 0) {
      const v4u gx0 = *(LAS v4u*)(L + L_XS + b * (DM * 2) + lane * 16), gx1 = *(LAS v4u*)(L + L_XS + b * (DM * 2) + 1024 + (lane & 15) * 16);
      float d = 0.f;
#pragma unroll
      for (int e = 0; e < 4; ++e) { d = dot2(gx0[e], gg0[e], d); d = dot2(gx1[e], gg1[e], d); }
      gate = 1.f / (1.f + __expf(-wave_sum(d)));
      if (lane == 0) *(LAS float*)(L + L_MISC) = gate;
    }
    LDS_FENCE();
    if (lane == 0) flag[w] = tagA;
    K_STEP(A, 9, 11);
    if (w == 0) {
      for (unsigned spins = 0; !__all(lane >= 6 || flag[lane < 6 ? lane : 0] == tagA) && ++spins < (SPIN_CAP << 4); ) __builtin_amdgcn_s_sleep(1);
      MEM_FENCE();
      const LAS float* g = (const LAS float*)(L + L_G) + (lane < 24 ? lane : 0);
      float ga = 0.f, up = 0.f;
#pragma unroll
      for (int r = 0; r < 16; ++r) { ga += g[r * 24]; up += g[(16 + r) * 24]; }
      if (lane < 24) *(LAS float*)(L + L_MISC + 64 + lane * 4) = ga / (1.f + __expf(-ga)) * up * (1.f - gate);
      LDS_FENCE();
      if (lane == 0) flag[8] = tagA;
    }
    K_STEP(B, 10, 12);
    for (unsigned spins = 0; flag[8] != tagA && ++spins < (SPIN_CAP << 4); ) __builtin_amdgcn_s_sleep(1);
    MEM_FENCE();
    v2f ya[4];
#pragma unroll
    for (int e = 0; e < 4; ++e) ya[e] = v2f{0.f, 0.f};
#pragma unroll
    for (int t = 0; t < 4; ++t) {
      const float hv = *(LAS float*)(L + L_MISC + 64 + (t * 6 + (yr < 6 ? yr : 0)) * 4);
#pragma unroll
      for (int d = 0; d < 4; ++d) ya[d] = __builtin_elementwise_fma(v2f{hv, hv}, v2f{bflo(W[8 + t][d]), bfhi(W[8 + t][d])}, ya[d]);
    }
    if (lane < 60) {
      LAS v4f* p = (LAS v4f*)(L + L_YF + (w * 480 + yr * 80 + yc * 8) * 4);
      p[0] = v4f{ya[0][0], ya[0][1], ya[1][0], ya[1][1]}; p[1] = v4f{ya[2][0], ya[2][1], ya[3][0], ya[3][1]};
    }
    wo_vo = yr * (DM * 2) + w * 160 + yc * 16 + (lane < 60 ? 0 : OOB);
  }
  MEM_FENCE();
#define WO_COPY(t0) do { asm volatile("s_waitcnt vmcnt(12)" ::: "memory"); const int ln_ = fresh_lane(); \
    _Pragma("unroll") for (int t = 0; t < 8; t += 2) {     \
      const v4u a_ = *(LAS v4u*)(L + L_FP + w * 8192 + t * 1024 + ln_ * 16), b_ = *(LAS v4u*)(L + L_FP + w * 8192 + (t + 1) * 1024 + ln_ * 16); \
      _Pragma("unroll") for (int d = 0; d < 4; ++d) { W[t0 + t][d] = __builtin_amdgcn_perm(b_[d], a_[d], 0x05040100u); W[t0 + t + 1][d] = __builtin_amdgcn_perm(b_[d], a_[d], 0x07060302u); } } \
    asm volatile("" : "+v"(W[t0]), "+v"(W[t0 + 1]), "+v"(W[t0 + 2]), "+v"(W[t0 + 3]), "+v"(W[t0 + 4]), "+v"(W[t0 + 5]), "+v"(W[t0 + 6]), "+v"(W[t0 + 7]) : : "memory"); } while (0)
  int n;
#pragma unroll 1
  for (n = 11; n < 21; n += 2) {
    wo_t = n < 19 ? n - 11 : -1; K_STEP(A, n, n + 2);
    wo_t = n < 19 ? n - 10 : -1; K_STEP(B, n + 1, n + 3);
  }
  WO_COPY(0);
#pragma unroll 1
  for (; n < 31; n += 2) {
    wo_t = n < 29 ? n - 13 : -1; K_STEP(A, n, n + 2);
    wo_t = n < 29 ? n - 12 : -1; K_STEP(B, n + 1, n + 3);
  }
  WO_COPY(8);
  wo_t = -1;
  K_STEP(A, 31, 33);
  mx = fmaxf(mx, dpp<XOR8>(mx)); mx = max16_32(mx);
  const float mw = mx * CS;
  LDS_FENCE();
  {
    const int lane = fresh_lane();
    LAS v4f* p = (LAS v4f*)(L + L_SC + w * 2048 + lane * 32);
    v4f a = p[0], bb = p[1];
    float l = 0.f;
#pragma unroll
    for (int e = 0; e < 4; ++e) { a[e] = ex2(fmaf(a[e], CS, -mw)); bb[e] = ex2(fmaf(bb[e], CS, -mw)); l += a[e] + bb[e]; }
    p[0] = a; p[1] = bb;
    l = wave_sum(l);
    if (lane == 0) __builtin_amdgcn_raw_buffer_store_b128(v4u{__float_as_uint(mw), tagA, __float_as_uint(l), tagB}, rws, WS_ML + (((b * NH + h) * 2 + s) * 8 + w) * 16, 0, AUX_SC1);
  }
  LDS_FENCE();

  v2f acc2[12];
#pragma unroll
  for (int e = 0; e < 12; ++e) acc2[e] = v2f{0.f, 0.f};
#define PIN_ACC() asm volatile("" : "+v"(acc2[0]), "+v"(acc2[1]), "+v"(acc2[2]), "+v"(acc2[3]), "+v"(acc2[4]), "+v"(acc2[5]), "+v"(acc2[6]), "+v"(acc2[7]), \
                                    "+v"(acc2[8]), "+v"(acc2[9]), "+v"(acc2[10]), "+v"(acc2[11]) : : "memory")
  auto v_tile = [&](const v4u& r0, const v4u& r1, const v4u& r2, float p) {
    const v2f pp = v2f{p, p};
#pragma unroll
    for (int d = 0; d < 4; ++d) {
      acc2[d] = __builtin_elementwise_fma(pp, v2f{bflo(r0[d]), bfhi(r0[d])}, acc2[d]);
      acc2[4 + d] = __builtin_elementwise_fma(pp, v2f{bflo(r1[d]), bfhi(r1[d])}, acc2[4 + d]);
      acc2[8 + d] = __builtin_elementwise_fma(pp, v2f{bflo(r2[d]), bfhi(r2[d])}, acc2[8 + d]);
    }
    PIN_ACC();
  };
#define V_USE(G, n) do { const v2f p2_ = *(LAS v2f*)(L + scoff + (n) * 64); v_tile(G.r[0][0], G.r[0][1], G.r[0][2], p2_[0]); v_tile(G.r[1][0], G.r[1][1], G.r[1][2], p2_[1]); } while (0)
#pragma unroll 1
  for (n = 0; n < 10; n += 2) {
    V_USE(B, n); wo_t = n < 8 ? n + 16 : -1; ISSUE(B, NGW + n + 2);
    V_USE(A, n + 1); wo_t = n < 8 ? n + 17 : -1; ISSUE(A, NGW + n + 3);
  }
  WO_COPY(16);
#pragma unroll 1
  for (; n < 20; n += 2) {
    V_USE(B, n); wo_t = n < 18 ? n + 14 : -1; ISSUE(B, NGW + n + 2);
    V_USE(A, n + 1); wo_t = n < 18 ? n + 15 : -1; ISSUE(A, NGW + n + 3);
  }
  WO_COPY(24);
  wo_t = -1;
  const int mloff = WS_ML + ((b * NH + h) * 16 + (fresh_lane() & 15)) * 16;
  v4u ml = poll16(rws, mloff);
  V_USE(B, n); ISSUE(B, NGW + n + 2); V_USE(A, n + 1); ISSUE(A, NGW + n + 3);
  for (unsigned spins = 0; !__all((ml[1] == tagA) & (ml[3] == tagB)); ) {
    if (++spins > SPIN_CAP) break;
    __builtin_amdgcn_s_sleep(2);
    ml = poll16(rws, mloff);
  }
  float fw;
  {
    const float m_i = __uint_as_float(ml[0]), l_i = __uint_as_float(ml[2]);
    float M = m_i;
    M = fmaxf(M, dpp<XOR1>(M)); M = fmaxf(M, dpp<XOR2>(M)); M = fmaxf(M, dpp<XOR7>(M)); M = fmaxf(M, dpp<XOR8>(M));
    float Ls = l_i * ex2(m_i - M);
    Ls = oct_sum(Ls); Ls += dpp<XOR8>(Ls);
    fw = __uint_as_float(__builtin_amdgcn_readfirstlane(__float_as_uint(ex2(mw - M) / Ls * *(LAS float*)(L + L_MISC))));
  }
#pragma unroll 1
  for (n += 2; n < NGW - 2; n += 2) { V_USE(B, n); ISSUE(B, NGW + n + 2); V_USE(A, n + 1); ISSUE(A, NGW + n + 3); }
  V_USE(B, NGW - 2);
  V_USE(A, NGW - 1);
  {
    float b12[12], c6[6];
#pragma unroll
    for (int i = 0; i < 12; ++i) {
      auto r = __builtin_amdgcn_permlane32_swap(__float_as_uint(acc2[i >> 1][i & 1]), __float_as_uint(acc2[6 + (i >> 1)][i & 1]), false, false);
      b12[i] = __uint_as_float(r[0]) + __uint_as_float(r[1]);
    }
#pragma unroll
    for (int i = 0; i < 6; ++i) {
      auto r = __builtin_amdgcn_permlane16_swap(__float_as_uint(b12[i]), __float_as_uint(b12[i + 6]), false, false);
      c6[i] = __uint_as_float(r[0]) + __uint_as_float(r[1]);
    }
#pragma unroll
    for (int i = 0; i < 6; ++i) c6[i] = (c6[i] + dpp<XOR8>(c6[i])) * fw;
    const int ln = fresh_lane();
    if ((ln & 8) == 0) {
      const int v0 = (ln >> 4) * 6;
#pragma unroll
      for (int p = 0; p < 3; ++p) { const int v = v0 + 2 * p; *(LAS v2f*)(L + L_QP + (w * QPS + (v >> 3) * 64 + (ln & 7) * 8 + (v & 7)) * 4) = v2f{c6[2 * p], c6[2 * p + 1]}; }
    }
  }
  const int ln = fresh_lane();
  const int yr = ln < 60 ? ln / 10 : 0, yc = ln - (ln / 10) * 10;
  float ya[8];
  { const LAS v4f* p = (const LAS v4f*)(L + L_YF + (w * 480 + yr * 80 + yc * 8) * 4); const v4f a = p[0], bb = p[1];
#pragma unroll
    for (int e = 0; e < 4; ++e) { ya[e] = a[e]; ya[4 + e] = bb[e]; } }
  __syncthreads();
  if (w < 6) {
    const int pi = w * 16 + (ln >> 2), r = pi >> 4, u = pi & 15, qq = ln & 3;
    const LAS float* p = (const LAS float*)(L + L_QP) + 2 * qq * QPS + u * 12 + r;
    float a0 = p[0] + p[QPS], a1 = p[6] + p[QPS + 6];
    a0 += dpp<XOR1>(a0); a1 += dpp<XOR1>(a1); a0 += dpp<XOR2>(a0); a1 += dpp<XOR2>(a1);
    if (qq == 0) *(LAS unsigned*)(L + L_OB + pi * 4) = cvtpk(a0, a1);
  }
  __syncthreads();
  {
    LAS float* yrp = (LAS float*)(L + L_FP) + w * 480;
    const LAS v4u* op = (const LAS v4u*)(L + L_OB + yr * 64);
#pragma unroll
    for (int u4 = 0; u4 < 4; ++u4) {
      const v4u o4 = op[u4];
#pragma unroll
      for (int e = 0; e < 4; ++e) {
        const int u = u4 * 4 + e;
#pragma unroll
        for (int d = 0; d < 4; ++d) { ya[2 * d] = dot2(W[2 * u][d], o4[e], ya[2 * d]); ya[2 * d + 1] = dot2(W[2 * u + 1][d], o4[e], ya[2 * d + 1]); }
      }
    }
    asm volatile("" : "+v"(ya[0]), "+v"(ya[1]), "+v"(ya[2]), "+v"(ya[3]), "+v"(ya[4]), "+v"(ya[5]), "+v"(ya[6]), "+v"(ya[7]));
    if (ln < 60) { LAS v4f* p = (LAS v4f*)(yrp + yr * 80 + yc * 8); p[0] = v4f{ya[0], ya[1], ya[2], ya[3]}; p[1] = v4f{ya[4], ya[5], ya[6], ya[7]}; }
    LDS_FENCE();
    if (ln < 40) {
      v2f y = v2f{0.f, 0.f};
#pragma unroll
      for (int r = 0; r < 6; ++r) y += *(LAS v2f*)(yrp + r * 80 + ln * 2);
      const int cons = w * 8 + ln / 5, g5 = ln % 5;
      __builtin_amdgcn_raw_buffer_store_b128(v4u{__float_as_uint(y[0]), tagA, __float_as_uint(y[1]), tagB}, rws, WS_INBOX + (((b * 64 + cons) * 64 + j) * 8 + g5) * 16, 0, AUX_SC1);
    }
  }
  if (w == 0) {
    const int io = WS_INBOX + (b * 64 + j) * 8192 + ln * 16 + ((ln & 7) < 5 ? 0 : OOB);
    __builtin_amdgcn_s_sleep(24);
    v4u g8[8];
    for (unsigned spins = 0;;) {
      bool ok = true;
#pragma unroll
      for (int e = 0; e < 8; ++e) g8[e] = poll16(rws, io + e * 1024);
#pragma unroll
      for (int e = 0; e < 8; ++e) ok = ok & (g8[e][1] == tagA) & (g8[e][3] == tagB);
      if (__all(ok | ((ln & 7) >= 5)) || ++spins > SPIN_CAP) break;
      __builtin_amdgcn_s_sleep(2);
    }
    float y0 = 0.f, y1 = 0.f;
#pragma unroll
    for (int e = 0; e < 8; ++e) { y0 += __uint_as_float(g8[e][0]); y1 += __uint_as_float(g8[e][2]); }
    const unsigned o = cvtpk(xoct_sum(y0), xoct_sum(y1));
    if (ln < 5) *(GAS unsigned*)(out + b * DM + j * 10 + ln * 2) = o;
    if (b == 0 && j == 0 && ln == 0) __hip_atomic_store((GAS u64*)(ws + WS_EPOCH), ((u64)tagB << 32) | tagA, __ATOMIC_RELAXED, __HIP_MEMORY_SCOPE_AGENT);
  }
}
}

extern "C" void kernel_launch(void* const* d_in, const int* in_sizes, int n_in, void* d_out, int out_size, void* d_ws, size_t ws_size,
                              hipStream_t stream) {
  (void)in_sizes; (void)n_in; (void)out_size; (void)ws_size;
  hipLaunchKernelGGL(fused_decode, dim3(NWG), dim3(NTHR), 0, stream, (const u16*)d_in[0], (const u16*)d_in[1], (const u16*)d_in[2], (const u16*)d_in[3],
                     (const u16*)d_in[4], (const u16*)d_in[6], (const u16*)d_in[7], (const u16*)d_in[8], (const u16*)d_in[9], (u16*)d_out, (unsigned char*)d_ws);
}
```
